```python
import torch

def _softmax(x, axis=-1):
    return torch.softmax(x, dim=axis)
def _gelu(x):
    return torch.nn.functional.gelu(x, approximate="tanh")
def _silu(x):
    return torch.nn.functional.silu(x)
def _sigmoid(x):
    return torch.sigmoid(x)
def _layernorm(x, axis=-1, eps=1e-5):
    m = x.mean(dim=axis, keepdim=True)
    v = x.var(dim=axis, keepdim=True, unbiased=False)
    return (x - m) / torch.sqrt(v + eps)
def _scatter_add(tgt, idx, src, axis=0):
    return tgt.clone().index_add_(axis, idx, src)
def _segment_sum(src, seg, n):
    out = torch.zeros((n,) + src.shape[1:], dtype=src.dtype)
    return out.index_add_(0, seg, src)
def _pad(x, pw):
    flat = tuple(v for pair in reversed(pw) for v in pair)
    return torch.nn.functional.pad(x, flat)

kernel_name = 'sequential_preLN__softmax_full_mha__dense_activation_relu__prompt'

def setup_inputs():

    _w = (lambda a: (lambda t: t.to(torch.bfloat16) if t.is_floating_point() else t)(torch.as_tensor(a)))
    def _A_setup():
        B, S, D = 1, 2048, 640
        return {
            "x": _w((lambda *s: torch.randn(*s, dtype=torch.bfloat16) * 0.05)(B, S, D)),
            "wqkv": _w((lambda *s: torch.randn(*s, dtype=torch.bfloat16) * 0.05)(D, 3 * D)),
            "wo": _w((lambda *s: torch.randn(*s, dtype=torch.bfloat16) * 0.05)(D, D)),
        }
    _A_in = _A_setup()

    def _B_setup():
        B, S, D, Dff = 1, 2048, 640, 1536
        return {
            "x": _w((lambda *s: torch.randn(*s, dtype=torch.bfloat16) * 0.05)(B, S, D)),
            "w1": _w((lambda *s: torch.randn(*s, dtype=torch.bfloat16) * 0.05)(D, Dff)),
            "w2": _w((lambda *s: torch.randn(*s, dtype=torch.bfloat16) * 0.05)(Dff, D)),
        }
    _B_in = _B_setup()

    return {
        "A_x": _A_in["x"],
        "A_wqkv": _A_in["wqkv"],
        "A_wo": _A_in["wo"],
        "B_x": _B_in["x"],
        "B_w1": _B_in["w1"],
        "B_w2": _B_in["w2"],
    }

def reference(A_x, A_wqkv, A_wo, B_x, B_w1, B_w2):
    def _A_ref(x, wqkv, wo):
        B, S, D = x.shape
        H = 32; Dh = D // H
        qkv = (lambda x, *d: x.permute(*d))((x @ wqkv).reshape(B, S, 3, H, Dh), 2, 0, 3, 1, 4)
        q, k, v = qkv[0], qkv[1], qkv[2]
        scale = Dh ** -0.5
        scores = torch.einsum("bhqd,bhkd->bhqk", q, k) * scale
        p = _softmax(scores, axis=-1)
        o = torch.einsum("bhqk,bhkd->bhqd", p, v)
        return (lambda x, *d: x.permute(*d))(o, 0, 2, 1, 3).reshape(B, S, D) @ wo

    def _B_ref(x, w1, w2):
        h = torch.einsum("bsd,df->bsf", x, w1)
        zero = torch.zeros_like(h)
        h = torch.maximum(h, zero)
        return torch.einsum("bsf,fd->bsd", h, w2)

    x = A_x
    h = x + _A_ref(_layernorm(x, axis=-1), A_wqkv, A_wo)
    h2 = h + _B_ref(_layernorm(h, axis=-1), B_w1, B_w2)
    return h2
```

```cpp
#include <hip/hip_runtime.h>
#include <hip/hip_bf16.h>
typedef unsigned short u16;
__device__ __forceinline__ float bf2f(u16 v) { return __uint_as_float((unsigned)v << 16); }
__device__ __forceinline__ u16 f2bf(float f) { unsigned u = __float_as_uint(f); u += 0x7fffu + ((u >> 16) & 1u); return (u16)(u >> 16); }
constexpr int S = 2048, D = 640, H = 32, DH = 20, FF = 1536;

__global__ void ln_k(const u16* x, u16* y) {
  int row = blockIdx.x; int t = threadIdx.x;
  float v[10]; float s = 0;
  for (int i = 0; i < 10; ++i) { v[i] = bf2f(x[row * D + t + 64 * i]); s += v[i]; }
  for (int o = 32; o; o >>= 1) s += __shfl_xor(s, o);
  float m = s / D; float q = 0;
  for (int i = 0; i < 10; ++i) { v[i] -= m; q += v[i] * v[i]; }
  for (int o = 32; o; o >>= 1) q += __shfl_xor(q, o);
  float r = rsqrtf(q / D + 1e-5f);
  for (int i = 0; i < 10; ++i) y[row * D + t + 64 * i] = f2bf(v[i] * r);
}
template<int MODE>
__global__ void gemm_k(const u16* A, const u16* B, const u16* res, u16* C, int N, int K) {
  __shared__ float As[16][65], Bs[16][65];
  int tx = threadIdx.x & 15, ty = threadIdx.x >> 4;
  int m0 = blockIdx.y * 64, n0 = blockIdx.x * 64;
  float acc[4][4] = {};
  for (int k0 = 0; k0 < K; k0 += 16) {
    for (int i = threadIdx.x; i < 1024; i += 256) {
      int r = i >> 4, c = i & 15; As[c][r] = bf2f(A[(m0 + r) * K + k0 + c]);
      int kk = i >> 6, n = i & 63; Bs[kk][n] = bf2f(B[(k0 + kk) * N + n0 + n]);
    }
    __syncthreads();
    for (int k = 0; k < 16; ++k)
      for (int i = 0; i < 4; ++i) for (int j = 0; j < 4; ++j) acc[i][j] += As[k][ty * 4 + i] * Bs[k][tx * 4 + j];
    __syncthreads();
  }
  for (int i = 0; i < 4; ++i) for (int j = 0; j < 4; ++j) {
    int m = m0 + ty * 4 + i, n = n0 + tx * 4 + j; float v = acc[i][j];
    if (MODE == 1) v = fmaxf(v, 0.f);
    if (MODE == 2) v += bf2f(res[m * N + n]);
    C[m * N + n] = f2bf(v);
  }
}
__global__ void attn_k(const u16* qkv, u16* o) {
  int h = blockIdx.y; int q = blockIdx.x * 64 + threadIdx.x;
  __shared__ float Ks[64][DH], Vs[64][DH];
  float qv[DH], acc[DH]; float m = -1e30f, l = 0;
  for (int d = 0; d < DH; ++d) { qv[d] = bf2f(qkv[q * 3 * D + h * DH + d]) * 0.22360679f; acc[d] = 0; }
  for (int k0 = 0; k0 < S; k0 += 64) {
    for (int d = 0; d < DH; ++d) { Ks[threadIdx.x][d] = bf2f(qkv[(k0 + threadIdx.x) * 3 * D + D + h * DH + d]); Vs[threadIdx.x][d] = bf2f(qkv[(k0 + threadIdx.x) * 3 * D + 2 * D + h * DH + d]); }
    __syncthreads();
    for (int k = 0; k < 64; ++k) {
      float s = 0; for (int d = 0; d < DH; ++d) s += qv[d] * Ks[k][d];
      float mn = fmaxf(m, s); float a = __expf(m - mn), p = __expf(s - mn); m = mn; l = l * a + p;
      for (int d = 0; d < DH; ++d) acc[d] = acc[d] * a + p * Vs[k][d];
    }
    __syncthreads();
  }
  for (int d = 0; d < DH; ++d) o[q * D + h * DH + d] = f2bf(acc[d] / l);
}
extern "C" void kernel_launch(void* const* d_in, const int* in_sizes, int n_in, void* d_out, int out_size, void* d_ws, size_t ws_size, hipStream_t stream) {
  const u16* x = (const u16*)d_in[0]; const u16* wqkv = (const u16*)d_in[1]; const u16* wo = (const u16*)d_in[2];
  const u16* w1 = (const u16*)d_in[4]; const u16* w2 = (const u16*)d_in[5];
  u16* ws = (u16*)d_ws; u16* xn = ws; u16* qkv = xn + S * D; u16* o = qkv + S * 3 * D; u16* h = o + S * D; u16* hn = h + S * D; u16* f = hn + S * D;
  ln_k<<<S, 64, 0, stream>>>(x, xn);
  gemm_k<0><<<dim3(30, 32), 256, 0, stream>>>(xn, wqkv, nullptr, qkv, 3 * D, D);
  attn_k<<<dim3(32, 32), 64, 0, stream>>>(qkv, o);
  gemm_k<2><<<dim3(10, 32), 256, 0, stream>>>(o, wo, x, h, D, D);
  ln_k<<<S, 64, 0, stream>>>(h, hn);
  gemm_k<1><<<dim3(24, 32), 256, 0, stream>>>(hn, w1, nullptr, f, FF, D);
  gemm_k<2><<<dim3(10, 32), 256, 0, stream>>>(f, w2, h, (u16*)d_out, D, FF);
}
```

```cpp
#include <hip/hip_runtime.h>
#include <hip/hip_bf16.h>

#define DI __device__ __forceinline__
#define LAS __attribute__((address_space(3)))
#define GAS __attribute__((address_space(1)))
typedef unsigned short u16;
typedef unsigned long long u64;
typedef __attribute__((ext_vector_type(8))) short s16x8;
typedef __attribute__((ext_vector_type(4))) short s16x4;
typedef __attribute__((ext_vector_type(8))) __bf16 bf16x8;
typedef __attribute__((ext_vector_type(16))) __bf16 bf16x16;
typedef __attribute__((ext_vector_type(2))) __bf16 bf16x2;
typedef __attribute__((ext_vector_type(2))) float f32x2;
typedef __attribute__((ext_vector_type(4))) float f32x4;
typedef __attribute__((ext_vector_type(8))) float f32x8;
typedef __attribute__((ext_vector_type(16))) float f32x16;
typedef __attribute__((ext_vector_type(32))) float f32x32;
typedef __attribute__((ext_vector_type(4))) unsigned u32x4;
typedef __attribute__((ext_vector_type(2))) unsigned u32x2;
typedef LAS char* lptr;
typedef __amdgpu_buffer_rsrc_t rsrc_t;

constexpr int S = 2048, D = 640, NH = 32, DH = 20, FF = 1536;
constexpr float LN_EPS = 1e-5f;
constexpr float QSCALE = 0.22360679774997896f * 1.4426950408889634f;

constexpr size_t WS_EPOCH = 0, WS_F1 = 4096, WS_F2 = 8192, WS_F3 = 12288, WS_F4 = 16384;
constexpr size_t WS_QK = 65536;
constexpr size_t WS_V = WS_QK + (size_t)160 * 2048 * 16;
constexpr size_t WS_O = WS_V + (size_t)256 * 640 * 16;
constexpr size_t WS_H = WS_O + (size_t)S * D * 2;
constexpr size_t WS_F = WS_H + (size_t)S * D * 2;
constexpr size_t WS_END = WS_F + (size_t)S * FF * 2;
constexpr int LDS_BYTES = 163840;
constexpr int LDS_B = 65536;
constexpr unsigned SPIN_CAP = 1u << 18;

#define STAMP(k)
struct Params { const char* x; const char* wqkv; const char* wo; const char* w1; const char* w2; char* out; char* ws; };

DI unsigned rfl(unsigned v) { return __builtin_amdgcn_readfirstlane(v); }
DI unsigned cvtpk(float lo, float hi) { f32x2 v = {lo, hi}; return __builtin_bit_cast(unsigned, __builtin_convertvector(v, bf16x2)); }
DI float bflo(unsigned v) { return __uint_as_float(v << 16); }
DI float bfhi(unsigned v) { return __uint_as_float(v & 0xffff0000u); }
DI void glds(const char* sbase, unsigned voff, unsigned lds) {
  asm volatile("s_mov_b32 m0, %2\n\ts_nop 0\n\tglobal_load_lds_dwordx4 %0, %1" :: "v"(voff), "s"(sbase), "s"(lds) : "memory");
}
DI void glds_half(const char* sbase, unsigned voff, unsigned lds) {
  asm volatile("s_mov_b32 m0, %2\n\ts_mov_b32 exec_hi, 0\n\tglobal_load_lds_dwordx4 %0, %1\n\ts_mov_b32 exec_hi, -1" :: "v"(voff), "s"(sbase), "s"(lds) : "memory");
}
DI void glds_d(const char* sbase, unsigned voff, unsigned lds) {
  asm volatile("s_mov_b32 m0, %2\n\ts_nop 0\n\tglobal_load_lds_dwordx4 %0, %1 sc1" :: "v"(voff), "s"(sbase), "s"(lds) : "memory");
}
#define WAIT_BAR(N) asm volatile("s_waitcnt vmcnt(" #N ") lgkmcnt(0)\n\ts_barrier" ::: "memory")
#define LDS_BAR() asm volatile("s_waitcnt lgkmcnt(0)\n\ts_barrier" ::: "memory")
#define WAIT3_BAR(c, T, A, B) asm volatile("s_cmp_lt_u32 %0, " #T "\n\ts_cbranch_scc1 1f\n\ts_cmp_eq_u32 %0, " #T "\n\ts_cbranch_scc1 2f\n\ts_waitcnt vmcnt(0)\n" \
    "2:\n\ts_waitcnt vmcnt(" #B ")\n1:\n\ts_waitcnt vmcnt(" #A ") lgkmcnt(0)\n\ts_barrier" :: "s"(c) : "memory", "scc")
#define SB() __builtin_amdgcn_sched_barrier(0)
#define PIN(x) asm volatile("" : "+v"(x))
DI s16x8 ld128(lptr p) { return *(const LAS s16x8*)p; }
DI s16x4 ldtr(lptr p) { return __builtin_amdgcn_ds_read_tr16_b64_v4i16((LAS s16x4*)p); }
DI s16x8 cat(s16x4 a, s16x4 b) { return __builtin_shufflevector(a, b, 0, 1, 2, 3, 4, 5, 6, 7); }
DI f32x16 mfma32(s16x8 a, s16x8 b, f32x16 c) { return __builtin_amdgcn_mfma_f32_32x32x16_bf16(__builtin_bit_cast(bf16x8, a), __builtin_bit_cast(bf16x8, b), c, 0, 0, 0); }
DI f32x4 mfma16(s16x8 a, s16x8 b, f32x4 c) { return __builtin_amdgcn_mfma_f32_16x16x32_bf16(__builtin_bit_cast(bf16x8, a), __builtin_bit_cast(bf16x8, b), c, 0, 0, 0); }
DI float dot2(unsigned a, unsigned b, float c) { return __builtin_amdgcn_fdot2_f32_bf16(__builtin_bit_cast(bf16x2, a), __builtin_bit_cast(bf16x2, b), c, false); }
constexpr unsigned ONES2 = 0x3f803f80u;
DI float fsum8(s16x8 f, float c) { u32x4 u = __builtin_bit_cast(u32x4, f); for (int i = 0; i < 4; ++i) c = dot2(u[i], ONES2, c); return c; }
DI float fsq8(s16x8 f, float c) { u32x4 u = __builtin_bit_cast(u32x4, f); for (int i = 0; i < 4; ++i) c = dot2(u[i], u[i], c); return c; }
DI void st128_d(rsrc_t rs, unsigned off, u32x4 v) { __builtin_amdgcn_raw_buffer_store_b128(v, rs, off, 0, 16); }
DI void st64_d(rsrc_t rs, unsigned off, u32x2 v) { __builtin_amdgcn_raw_buffer_store_b64(v, rs, off, 0, 16); }
DI u32x4 ld128_d(rsrc_t rs, unsigned off) { return __builtin_amdgcn_raw_buffer_load_b128(rs, off, 0, 16); }
DI u32x2 ld64_d(rsrc_t rs, unsigned off) { return __builtin_amdgcn_raw_buffer_load_b64(rs, off, 0, 16); }
DI u64 ldflag(const char* ws, unsigned off) { return __hip_atomic_load((GAS u64*)(ws + off), __ATOMIC_RELAXED, __HIP_MEMORY_SCOPE_AGENT); }
DI void stflag(char* ws, unsigned off, u64 v) { __hip_atomic_store((GAS u64*)(ws + off), v, __ATOMIC_RELAXED, __HIP_MEMORY_SCOPE_AGENT); }
DI void poll(const char* ws, unsigned off0, unsigned off1, u64 tag) {
#pragma unroll 1
  for (unsigned n = 0; n < SPIN_CAP; ++n) {
    const u64 a = ldflag(ws, off0), b = ldflag(ws, off1);
    if (__all(a == tag && b == tag)) break;
    __builtin_amdgcn_s_sleep(1);
  }
}
DI void poll(const char* ws, unsigned off0, u64 tag) {
#pragma unroll 1
  for (unsigned n = 0; n < SPIN_CAP; ++n) {
    if (__all(ldflag(ws, off0) == tag)) break;
    __builtin_amdgcn_s_sleep(1);
  }
}

struct Ctx { Params p; lptr lds; unsigned lds0; rsrc_t rs; u64 tag; int tid, l, w, xcd, j; mutable unsigned st_lo, st_hi; };

struct F1 { s16x8 a0[4], a1[4], b[4]; };
template <bool VT>
DI void p1_body(const Ctx& c, int panel, int ct) {
  const int tid = c.tid, l = c.l, w = c.w, wm = w >> 2, wn = w & 3, r = l & 31, hh = l >> 5;
  lptr lds = c.lds;
  const unsigned voffA = (8 * w + (l >> 3)) * 1280 + (((l & 7) ^ ((4 * w + (l >> 4)) & 7)) << 4);
  const unsigned voffB = (4 * w + (l >> 4)) * 3840 + (((l & 15) ^ (((l >> 4) & 3) << 2)) << 4);
  const char* ga = c.p.x + (size_t)panel * 128 * 1280;
  const char* gb = c.p.wqkv + ct * 256;
  auto issue = [&](int s) {
    const unsigned dst = c.lds0 + (s & 3) * 32768 + w * 1024;
    const char* a = ga + s * 128; const char* b = gb + (size_t)s * 64 * 3840;
    glds(a, voffA, dst); glds(b, voffB, dst + 16384); glds(a + 64 * 1280, voffA, dst + 8192); glds(b + 32 * 3840, voffB, dst + 16384 + 8192);
  };
  issue(0); issue(1); issue(2); issue(3);
  STAMP(1);
  unsigned aoff[4], boff[4];
  const int q = (l & 15) >> 2, pp = l & 3;
#pragma unroll
  for (int i = 0; i < 4; ++i) {
    const int ks = (i + wn + 2 * wm) & 3;
    aoff[i] = (wm * 64 + r) * 128 + (((2 * ks + hh) ^ ((r >> 1) & 7)) << 4);
    boff[i] = 16384 + ks * 4096 + (8 * hh + q) * 256 + (((wn * 4 + 2 * ((l >> 4) & 1) + (pp >> 1)) ^ (q << 2)) << 4) + 8 * (pp & 1);
  }
  f32x16 acc[2] = {};
  float sa[2] = {}, sq[2] = {}, sb = 0.f;
  auto rd = [&](F1& f, int i, lptr sl) { f.b[i] = cat(ldtr(sl + boff[i]), ldtr(sl + boff[i] + 1024)); f.a0[i] = ld128(sl + aoff[i]); f.a1[i] = ld128(sl + aoff[i] + 4096); };
  auto mm = [&](const F1& f, int i) {
    if (VT) { acc[0] = mfma32(f.a0[i], f.b[i], acc[0]); acc[1] = mfma32(f.a1[i], f.b[i], acc[1]); }
    else    { acc[0] = mfma32(f.b[i], f.a0[i], acc[0]); acc[1] = mfma32(f.b[i], f.a1[i], acc[1]); }
    if (i == 0) { sa[0] = fsum8(f.a0[0], sa[0]); sq[0] = fsq8(f.a0[0], sq[0]); PIN(sa[0]); PIN(sq[0]); }
    if (i == 1) { sa[1] = fsum8(f.a1[0], sa[1]); sq[1] = fsq8(f.a1[0], sq[1]); PIN(sa[1]); PIN(sq[1]); }
    if (i >= 2) { sb = fsum8(f.b[i - 2], sb); PIN(sb); }
  };
  auto step = [&](F1& nx, const F1& cu, int s) {
    WAIT3_BAR(s, 8, 8, 4);
    if (s <= 6) issue(s + 3);
    lptr sl = lds + (s & 3) * 32768;
#pragma unroll
    for (int i = 0; i < 4; ++i) { SB(); rd(nx, i, sl); SB(); mm(cu, i); }
    SB();
  };
  F1 fa, fb;
  WAIT_BAR(12);
  STAMP(2);
#pragma unroll
  for (int i = 0; i < 4; ++i) rd(fa, i, lds);
#pragma unroll 1
  for (int s = 1; s < 9; s += 2) { step(fb, fa, s); step(fa, fb, s + 1); }
  step(fb, fa, 9);
#pragma unroll
  for (int i = 0; i < 4; ++i) mm(fb, i);
  LDS_BAR();
  STAMP(3);
  LAS f32x2* rp = (LAS f32x2*)lds;
  LAS float* cp = (LAS float*)(lds + 8192);
  LAS float* ra = (LAS float*)(lds + 10240); LAS float* rb = ra + 128; LAS float* cs = ra + 256;
  rp[(wn * 2 + hh) * 128 + wm * 64 + r] = f32x2{sa[0], sq[0]};
  rp[(wn * 2 + hh) * 128 + wm * 64 + 32 + r] = f32x2{sa[1], sq[1]};
  cp[(wm * 2 + hh) * 128 + wn * 32 + r] = sb;
  LDS_BAR();
  if (tid < 128) {
    f32x2 t = rp[tid];
#pragma unroll
    for (int i = 1; i < 8; ++i) t += rp[i * 128 + tid];
    const float mean = t[0] * (1.f / D), var = fmaxf(t[1] * (1.f / D) - mean * mean, 0.f);
    const float a = __builtin_amdgcn_rsqf(var + LN_EPS) * (ct < 5 ? QSCALE : 1.f);
    ra[tid] = a; rb[tid] = -mean * a;
  } else if (tid < 256) {
    const int n = tid - 128; cs[n] = cp[n] + cp[128 + n] + cp[256 + n] + cp[384 + n];
  }
  LDS_BAR();
#pragma unroll
  for (int mt = 0; mt < 2; ++mt) {
    unsigned pk[8];
    if (VT) {
      const float cn = cs[wn * 32 + r];
#pragma unroll
      for (int g = 0; g < 4; ++g) {
        const f32x4 a4 = *(LAS f32x4*)(ra + wm * 64 + mt * 32 + 8 * g + 4 * hh), b4 = *(LAS f32x4*)(rb + wm * 64 + mt * 32 + 8 * g + 4 * hh);
        float y[4];
#pragma unroll
        for (int i = 0; i < 4; ++i) y[i] = fmaf(acc[mt][4 * g + i], a4[i], b4[i] * cn);
        pk[2 * g] = cvtpk(y[0], y[1]); pk[2 * g + 1] = cvtpk(y[2], y[3]);
      }
    } else {
      const float a = ra[wm * 64 + mt * 32 + r], b = rb[wm * 64 + mt * 32 + r];
#pragma unroll
      for (int g = 0; g < 4; ++g) {
        const f32x4 c4 = *(LAS f32x4*)(cs + wn * 32 + 8 * g + 4 * hh);
        float y[4];
#pragma unroll
        for (int i = 0; i < 4; ++i) y[i] = fmaf(acc[mt][4 * g + i], a, b * c4[i]);
        pk[2 * g] = cvtpk(y[0], y[1]); pk[2 * g + 1] = cvtpk(y[2], y[3]);
      }
    }
#pragma unroll
    for (int gp = 0; gp < 2; ++gp) {
      const auto s0 = __builtin_amdgcn_permlane32_swap(pk[4 * gp], pk[4 * gp + 2], false, false);
      const auto s1 = __builtin_amdgcn_permlane32_swap(pk[4 * gp + 1], pk[4 * gp + 3], false, false);
      const u32x4 v = {s0[0], s1[0], s0[1], s1[1]};
      if (VT) st128_d(c.rs, (unsigned)(WS_V + ((panel * 16 + wm * 8 + mt * 4 + 2 * gp + hh) * 640 + (ct - 10) * 128 + wn * 32 + r) * 16), v);
      else    st128_d(c.rs, (unsigned)(WS_QK + ((ct * 16 + wn * 4 + 2 * gp + hh) * 2048 + panel * 128 + wm * 64 + mt * 32 + r) * 16), v);
    }
  }
  STAMP(4);
}

DI void w80_offsets(int l, int w, unsigned (&vo)[3]) {
#pragma unroll
  for (int i = 0; i < 3; ++i) {
    const int n = 64 * i + l, rr = n / 10, pos = n - 10 * rr, rho = 16 * w + rr;
    const int k = (rho & ~12) | ((rho & 4) << 1) | ((rho & 8) >> 1);
    vo[i] = k * 1280 + pos * 16;
  }
}
DI void w80_issue(const Ctx& c, const char* gb, const unsigned (&vo)[3], int ch) {
  const unsigned sb = c.lds0 + LDS_B + (ch & 3) * 20480 + c.w * 2560;
  const char* b = gb + (size_t)ch * 128 * 1280;
  glds(b, vo[0], sb); glds(b, vo[1], sb + 1024); glds_half(b, vo[2], sb + 2048);
}
DI void w192_offsets(int l, int w, unsigned (&vo)[3]) {
#pragma unroll
  for (int i = 0; i < 3; ++i) {
    const int n = 64 * i + l, rr = n / 24, pos = n - 24 * rr, rho = 8 * w + rr;
    const int k = (rho & ~12) | ((rho & 4) << 1) | ((rho & 8) >> 1);
    vo[i] = k * 3072 + ((((pos >> 1) ^ ((rho >> 1) & 3)) << 1) | (pos & 1)) * 16;
  }
}
DI void w192_issue(const Ctx& c, const char* gb, const unsigned (&vo)[3], int ch) {
  const unsigned sb = c.lds0 + LDS_B + (ch & 3) * 24576 + c.w * 3072;
  const char* b = gb + (size_t)ch * 64 * 3072;
  glds(b, vo[0], sb); glds(b, vo[1], sb + 1024); glds(b, vo[2], sb + 2048);
}

constexpr int KRING = 0, VRING = 24576;
#define STR_(x) #x
#define STR(x) STR_(x)
#define EX(b, i) "v_exp_f32 v[" STR(b) "+" STR(i) "], v[" STR(b) "+" STR(i) "]\n\t"
#define EX3(b, i) EX(b, i) EX(b, i + 1) EX(b, i + 2)
#define EX4(b, i) EX3(b, i) EX(b, i + 3)
#define VR(b, n) "v[" STR(b) ":" STR(b) "+" STR(n) "-1]"
#define R_SA 32
#define R_SB 64
#define R_O 96
#define R_NM 112
#define R_Q 128
#define R_QR 132
#define R_KA 134
#define R_KB 138
#define R_KR 142
#define R_V0 144
#define R_V1 148
#define R_V2 152
#define R_V3 156
#define R_IDX 160
#define R_KAD 161
#define R_KRAD 162
#define R_VAD 163
#define R_VO0 164
#define R_VO1 165
#define R_VO2 166
#define R_WO0 167
#define R_WO1 168
#define R_WO2 169
#define VREAD(reg, off) "s_mov_b64 exec, %[vmask]\n\tds_read_b128 " VR(reg, 4) ", v" STR(R_VAD) " offset:" STR(off) "\n\ts_mov_b64 exec, -1\n\t"
#define PV(vreg, sreg) "v_smfmac_f32_32x32x32_bf16 " VR(R_O, 16) ", " VR(vreg, 4) ", " VR(sreg, 8) ", v" STR(R_IDX) "\n\t"
#define QK16(dst, kreg) "v_mfma_f32_32x32x16_bf16 " VR(dst, 16) ", " VR(kreg, 4) ", " VR(R_Q, 4) ", " VR(R_NM, 16) "\n\t"
#define STEP(C, N, KO, VO, MID) \
  "s_waitcnt lgkmcnt(4)\n\t" \
  QK16(N, R_KA) \
  EX4(C, 0) \
  MID \
  "ds_read_b128 " VR(R_KA, 4) ", v" STR(R_KAD) " offset:" STR(KO) "\n\t" \
  PV(R_V3, N + 24) \
  VREAD(R_V3, VO) \
  EX3(C, 4) \
  QK16(N + 16, R_KB) \
  "ds_read_b128 " VR(R_KB, 4) ", v" STR(R_KAD) " offset:" STR(KO + 512) "\n\t" \
  EX4(C, 7) \
  "s_waitcnt lgkmcnt(3)\n\t" \
  PV(R_V0, C) \
  VREAD(R_V0, VO + 640) \
  EX4(C, 11) \
  "v_mfma_f32_32x32x4_2b_bf16 " VR(N, 32) ", " VR(R_KR, 2) ", " VR(R_QR, 2) ", " VR(N, 32) "\n\t" \
  "ds_read_b64 " VR(R_KR, 2) ", v" STR(R_KRAD) " offset:" STR(KO) "\n\t" \
  EX4(C, 15) EX4(C, 19) \
  PV(R_V1, C + 8) \
  VREAD(R_V1, VO + 1280) \
  EX4(C, 23) \
  PV(R_V2, C + 16) \
  VREAD(R_V2, VO + 1920) \
  EX4(C, 27) EX(C, 31)
#define KV_ISSUE(D0, D1, D2) \
  "v_add_u32 v" STR(R_VO0) ", %[st0], v" STR(R_VO0) "\n\tv_add_u32 v" STR(R_VO1) ", %[st1], v" STR(R_VO1) "\n\tv_add_u32 v" STR(R_VO2) ", %[st2], v" STR(R_VO2) "\n\t" \
  "s_mov_b32 m0, %[" D0 "]\n\ts_nop 0\n\tglobal_load_lds_dwordx4 v" STR(R_VO0) ", %[gb] sc1\n\t" \
  "s_mov_b32 m0, %[" D1 "]\n\ts_nop 0\n\tglobal_load_lds_dwordx4 v" STR(R_VO1) ", %[gb] sc1\n\t" \
  "s_mov_b32 m0, %[" D2 "]\n\ts_nop 0\n\tglobal_load_lds_dwordx4 v" STR(R_VO2) ", %[gb] sc1\n\t"
#define TOP_EVEN \
  "s_waitcnt vmcnt(1) lgkmcnt(0)\n\ts_barrier\n\t" KV_ISSUE("d10", "d11", "d12") \
  "s_mov_b32 m0, %[wd]\n\ts_add_u32 %[wd], %[wd], 1024\n\tglobal_load_lds_dwordx4 v" STR(R_WO0) ", %[wb]\n\t" \
  "s_mov_b32 m0, %[wd]\n\ts_add_u32 %[wd], %[wd], 1024\n\tglobal_load_lds_dwordx4 v" STR(R_WO1) ", %[wb]\n\t"
#define TOP_ODD \
  "s_waitcnt vmcnt(2) lgkmcnt(0)\n\ts_barrier\n\t" \
  "s_cmp_eq_u32 %[cnt], 1\n\ts_cbranch_scc1 2f\n\t" KV_ISSUE("d00", "d01", "d02") \
  "2:\n\ts_mov_b32 m0, %[wd]\n\ts_mov_b32 exec_hi, 0\n\tglobal_load_lds_dwordx4 v" STR(R_WO2) ", %[wb]\n\ts_mov_b32 exec_hi, -1\n\t" \
  "s_add_u32 %[wd], %[wd], 20480-2048\n\t" \
  "v_add_u32 v" STR(R_WO0) ", %[wst], v" STR(R_WO0) "\n\tv_add_u32 v" STR(R_WO1) ", %[wst], v" STR(R_WO1) "\n\tv_add_u32 v" STR(R_WO2) ", %[wst], v" STR(R_WO2) "\n\t"

DI void p2_body(const Ctx& c, int h, int qb, int cs) {
  const int l = c.l, w = c.w, r = l & 31, hh = l >> 5;
  const unsigned lds0 = c.lds0;
  const int e = h & 1, cb = 5 * (h >> 1) + 2 * e, q0 = qb * 256 + w * 32;
  unsigned vo[3], sstr[3], dst0[3], dst1[3], wo[3];
#pragma unroll
  for (int i = 0; i < 3; ++i) {
    int id = w + 8 * i; if (id >= 22) id -= 2;
    const bool isk = id < 12;
    const int blk = id / 3, ch = id - 3 * blk, pv = id - 12, n = 64 * pv + l, kg = n / 20, dd = n - 20 * kg, d = e ? (dd + 4 >= 20 ? dd - 16 : dd + 4) : dd;
    vo[i] = isk ? (unsigned)(WS_QK + ((80 + cb + ch) * 2048 + 128 - 256 + 64 * blk + l) * 16) : (unsigned)(WS_V + ((6 - 32 + kg) * 640 + 20 * h + d) * 16);
    sstr[i] = isk ? 4096u : 32u * 10240u;
    dst0[i] = lds0 + (isk ? KRING + id * 1024 : VRING + pv * 1024);
    dst1[i] = dst0[i] + (isk ? 12288 : 10240);
  }
  w80_offsets(l, w, wo);
  unsigned kad = lds0 + KRING + (e + hh) * 1024 + r * 16, krad = lds0 + KRING + (e ? 0 : 2048) + l * 16 + 8 * e;
  unsigned vad = lds0 + VRING + hh * 320 + (r < 20 ? r : 0) * 16;
  if (w == 0) {
    const int sel = l >> 4, f0 = (sel & 2) ? 20 * h : 8 * cb, f1 = f0 + ((sel & 1) ? ((sel & 2) ? 19 : 23) : 0);
    const int ctk = ((sel & 2) ? 10 : 5) + (f1 >> 7), ctq = (8 * cb + ((l & 2) ? 23 : 0)) >> 7;
    poll(c.p.ws, WS_F1 + (ctk * 16 + (l & 15)) * 8, WS_F1 + (ctq * 16 + 2 * qb + (l & 1)) * 8, c.tag);
  }
  LDS_BAR();
  STAMP(6);
#pragma unroll
  for (int i = 0; i < 3; ++i) glds_d(c.p.ws, vo[i], dst1[i]);
#pragma unroll
  for (int i = 0; i < 3; ++i) { vo[i] += sstr[i]; glds_d(c.p.ws, vo[i], dst0[i]); }
  s16x8 q16 = __builtin_bit_cast(s16x8, ld128_d(c.rs, WS_QK + ((cb + e + hh) * 2048 + q0 + r) * 16));
  s16x4 qr = __builtin_bit_cast(s16x4, ld64_d(c.rs, WS_QK + ((e ? cb : cb + 2) * 2048 + q0 + r) * 16 + 8 * e));
  asm volatile("" : "+v"(q16), "+v"(qr));
  WAIT_BAR(0);
  STAMP(7);
  lptr k0 = (lptr)(size_t)kad + 12288 + 2 * 3072;
  f32x32 sa;
  {
    const f32x32 z = {};
    sa = __builtin_amdgcn_mfma_f32_32x32x4bf16_1k(*(const LAS s16x4*)((lptr)(size_t)krad + 12288 + 2 * 3072), qr, z, 0, 0, 0);
    f32x16 lo = __builtin_shufflevector(sa, sa, 0, 1, 2, 3, 4, 5, 6, 7, 8, 9, 10, 11, 12, 13, 14, 15);
    f32x16 hi = __builtin_shufflevector(sa, sa, 16, 17, 18, 19, 20, 21, 22, 23, 24, 25, 26, 27, 28, 29, 30, 31);
    lo = mfma32(ld128(k0), q16, lo); hi = mfma32(ld128(k0 + 512), q16, hi);
#pragma unroll
    for (int i = 0; i < 16; ++i) { sa[i] = lo[i]; sa[16 + i] = hi[i]; }
  }
  float m = sa[0];
#pragma unroll
  for (int i = 1; i < 32; ++i) m = fmaxf(m, sa[i]);
  { const auto t = __builtin_amdgcn_permlane32_swap(__float_as_uint(m), __float_as_uint(m), false, false); m = fmaxf(__uint_as_float(t[0]), __uint_as_float(t[1])); }
  f32x16 nm, o = {};
#pragma unroll
  for (int i = 0; i < 16; ++i) nm[i] = -m;
#pragma unroll
  for (int i = 0; i < 32; ++i) sa[i] -= m;
  f32x32 sb = {};
  s16x8 ka = ld128(k0 + 3072), kb = ld128(k0 + 3072 + 512);
  s16x4 kr = *(const LAS s16x4*)((lptr)(size_t)krad + 12288 + 3 * 3072);
  const unsigned pad = r == 20 ? ONES2 : 0u;
  const u32x4 pad4 = {pad, pad, pad, pad};
  lptr vb = (lptr)(size_t)vad + 10240 + 3 * 2560;
  u32x4 v0 = __builtin_bit_cast(u32x4, ld128(vb + 640)), v1 = __builtin_bit_cast(u32x4, ld128(vb + 1280)), v2 = __builtin_bit_cast(u32x4, ld128(vb + 1920)), v3 = pad4;
  if (r >= 20) { v0 = pad4; v1 = pad4; v2 = pad4; }
  unsigned idx = 0xDDDDDDDDu, cnt = 4, wd = lds0 + LDS_B + w * 2560;
  const u64 vmask = 0x000FFFFF000FFFFFull;
  STAMP(8);
  asm volatile(
      "s_nop 4\n"
      "1:\n\t"
      STEP(R_SA, R_SB, 0, 0, TOP_EVEN)
      STEP(R_SB, R_SA, 3072, 2560, "")
      STEP(R_SA, R_SB, 6144, 5120, "")
      STEP(R_SB, R_SA, 9216, 7680, "")
      STEP(R_SA, R_SB, 12288, 10240, TOP_ODD)
      STEP(R_SB, R_SA, 12288 + 3072, 10240 + 2560, "")
      STEP(R_SA, R_SB, 12288 + 6144, 10240 + 5120, "")
      STEP(R_SB, R_SA, 12288 + 9216, 10240 + 7680, "")
      "s_sub_u32 %[cnt], %[cnt], 1\n\t"
      "s_cmp_lg_u32 %[cnt], 0\n\t"
      "s_cbranch_scc1 1b\n\t"
      "s_waitcnt lgkmcnt(0)\n\t"
      PV(R_V3, R_SB + 24)
      "s_nop 15\n\t"
      : "+{v[32:63]}"(sa), "+{v[64:95]}"(sb), "+{v[96:111]}"(o), "+{v[134:137]}"(ka), "+{v[138:141]}"(kb), "+{v[142:143]}"(kr),
        "+{v[144:147]}"(v0), "+{v[148:151]}"(v1), "+{v[152:155]}"(v2), "+{v[156:159]}"(v3),
        "+{v164}"(vo[0]), "+{v165}"(vo[1]), "+{v166}"(vo[2]), "+{v167}"(wo[0]), "+{v168}"(wo[1]), "+{v169}"(wo[2]), [cnt] "+s"(cnt), [wd] "+s"(wd)
      : "{v[112:127]}"(nm), "{v[128:131]}"(q16), "{v[132:133]}"(qr), "{v160}"(idx), "{v161}"(kad), "{v162}"(krad), "{v163}"(vad),
        [vmask] "s"(vmask), [gb] "s"(c.p.ws), [st0] "s"(sstr[0]), [st1] "s"(sstr[1]), [st2] "s"(sstr[2]),
        [d00] "s"(dst0[0]), [d01] "s"(dst0[1]), [d02] "s"(dst0[2]), [d10] "s"(dst1[0]), [d11] "s"(dst1[1]), [d12] "s"(dst1[2]),
        [wb] "s"(c.p.wo + cs * 160), [wst] "s"(128 * 1280)
      : "memory", "scc");
  STAMP(9);
  float lsum;
  { const auto t = __builtin_amdgcn_permlane32_swap(__float_as_uint(o[8]), __float_as_uint(o[8]), false, false); lsum = __uint_as_float(t[1]); }
  float chk = 0.f;
#pragma unroll
  for (int i = 0; i < 12; ++i) chk += fabsf(o[i]);
  if (__any(!(chk < 1e37f) || !(lsum > 0.f))) {
    const unsigned ko = WS_QK + ((80 + cb + e + hh) * 2048 + r) * 16, kro = WS_QK + ((80 + cb + (e ? 0 : 2)) * 2048 + l) * 16 + 8 * e;
    const int d = r < 20 ? (e ? (r + 4 >= 20 ? r - 16 : r + 4) : r) : 0;
    const unsigned vg = WS_V + (hh * 640 + 20 * h + d) * 16;
    float tm = -INFINITY;
#pragma unroll 1
    for (int pass = 0; pass < 2; ++pass) {
#pragma unroll
      for (int i = 0; i < 16; ++i) { nm[i] = pass ? -tm : 0.f; o[i] = 0.f; }
#pragma unroll 1
      for (int b = 0; b < 32; ++b) {
        f32x16 lo = mfma32(__builtin_bit_cast(s16x8, ld128_d(c.rs, ko + b * 1024)), q16, nm), hi = mfma32(__builtin_bit_cast(s16x8, ld128_d(c.rs, ko + b * 1024 + 512)), q16, nm);
        f32x32 s;
#pragma unroll
        for (int i = 0; i < 16; ++i) { s[i] = lo[i]; s[16 + i] = hi[i]; }
        s = __builtin_amdgcn_mfma_f32_32x32x4bf16_1k(__builtin_bit_cast(s16x4, ld64_d(c.rs, kro + b * 1024)), qr, s, 0, 0, 0);
        if (pass == 0) {
#pragma unroll
          for (int i = 0; i < 32; ++i) tm = fmaxf(tm, s[i]);
        } else {
#pragma unroll
          for (int g = 0; g < 4; ++g) {
            f32x8 pr;
#pragma unroll
            for (int i = 0; i < 8; ++i) pr[i] = __builtin_amdgcn_exp2f(s[8 * g + i]);
            u32x4 vf = ld128_d(c.rs, vg + (8 * b + 2 * g) * 10240);
            if (r >= 20) vf = pad4;
            o = __builtin_amdgcn_smfmac_f32_32x32x32_bf16(__builtin_bit_cast(bf16x8, vf), __builtin_bit_cast(bf16x16, pr), o, 0xDDDD, 0, 0);
          }
        }
      }
      if (pass == 0) { const auto t = __builtin_amdgcn_permlane32_swap(__float_as_uint(tm), __float_as_uint(tm), false, false); tm = fmaxf(__uint_as_float(t[0]), __uint_as_float(t[1])); }
    }
    const auto t = __builtin_amdgcn_permlane32_swap(__float_as_uint(o[8]), __float_as_uint(o[8]), false, false); lsum = __uint_as_float(t[1]);
  }
  const float rl = __builtin_amdgcn_rcpf(lsum);
  unsigned pk[6];
#pragma unroll
  for (int i = 0; i < 6; ++i) pk[i] = cvtpk(o[2 * i] * rl, o[2 * i + 1] * rl);
  const auto s0 = __builtin_amdgcn_permlane32_swap(pk[0], pk[2], false, false);
  const auto s1 = __builtin_amdgcn_permlane32_swap(pk[1], pk[3], false, false);
  const int row = q0 + r;
  const unsigned og = WS_O + (row >> 6) * 81920 + (row & 63) * 16;
  st128_d(c.rs, og + (cb + e + hh) * 1024, u32x4{s0[0], s1[0], s0[1], s1[1]});
  if (hh == 0) st64_d(c.rs, og + (e ? cb : cb + 2) * 1024 + 8 * e, u32x2{pk[4], pk[5]});
  STAMP(10);
}

template <int NT> struct F80 { s16x8 a0[2], a1[2], b[2][NT]; };
template <int NCH, bool LAST, int NT>
DI void g80_loop(const Ctx& c, int rt, int cs, f32x4 (&acc)[2][3]) {
  const int l = c.l, w = c.w, wm = w & 1, wk = (w >> 1) & 1, wn = w >> 2;
  lptr lds = c.lds;
  const char* ga = c.p.ws + (LAST ? WS_F : WS_O) + (size_t)rt * (NCH * 16384) + w * 2048;
  const char* gb = (LAST ? c.p.w2 : c.p.wo) + cs * 160;
  unsigned vo[3]; w80_offsets(l, w, vo);
  const unsigned voffA = l * 16;
  auto issueA = [&](int ch) { const unsigned sa = c.lds0 + (ch & 3) * 16384 + w * 2048; const char* a = ga + ch * 16384; glds_d(a, voffA, sa); glds_d(a + 1024, voffA, sa + 1024); };
  issueA(0); issueA(1); issueA(2); issueA(3);
  const int g = l >> 4, q = (l & 15) >> 2, pp = l & 3;
  const unsigned aoff = (8 * wk + g) * 1024 + (32 * wm + (l & 15)) * 16;
  const unsigned boff = LDS_B + (64 * wk + 16 * (g >> 1) + 4 * (g & 1) + q) * 160 + wn * 96 + pp * 8;
  auto rd = [&](F80<NT>& f, int ks, int ch) {
    lptr sa = lds + (ch & 3) * 16384 + aoff + ks * 4096, sb = lds + (ch & 3) * 20480 + boff + ks * 5120;
    f.a0[ks] = ld128(sa); f.a1[ks] = ld128(sa + 256);
#pragma unroll
    for (int nt = 0; nt < NT; ++nt) f.b[ks][nt] = cat(ldtr(sb + nt * 32), ldtr(sb + 1280 + nt * 32));
  };
  auto mm = [&](const F80<NT>& f, int ks) {
#pragma unroll
    for (int nt = 0; nt < NT; ++nt) { acc[0][nt] = mfma16(f.b[ks][nt], f.a0[ks], acc[0][nt]); acc[1][nt] = mfma16(f.b[ks][nt], f.a1[ks], acc[1][nt]); }
  };
  auto body = [&](F80<NT>& nx, const F80<NT>& cu, int ch) {
    if (ch + 3 < NCH) { w80_issue(c, gb, vo, ch + 3); issueA(ch + 3); }
    SB(); rd(nx, 0, ch); SB(); mm(cu, 0); SB(); rd(nx, 1, ch); SB(); mm(cu, 1); SB();
  };
  F80<NT> fa, fb;
  WAIT_BAR(6);
  STAMP(LAST ? 23 : 13);
  rd(fa, 0, 0); rd(fa, 1, 0);
  WAIT_BAR(4); body(fb, fa, 1);
  WAIT_BAR(7); body(fa, fb, 2);
  if constexpr (NCH == 5) {
    WAIT_BAR(5); body(fb, fa, 3);
    WAIT_BAR(0); body(fa, fb, 4);
    mm(fa, 0); mm(fa, 1);
  } else {
#pragma unroll 1
    for (int ch = 3; ch < NCH - 1; ch += 2) { WAIT3_BAR(ch, 10, 10, 5); body(fb, fa, ch); WAIT3_BAR(ch + 1, 10, 10, 5); body(fa, fb, ch + 1); }
    WAIT_BAR(0); body(fb, fa, NCH - 1);
    mm(fb, 0); mm(fb, 1);
  }
}
DI void g80_reduce(const Ctx& c, const f32x4 (&acc)[2][3], u32x4 (&res)[2]) {
  const int l = c.l, w = c.w, wm = w & 1, wk = (w >> 1) & 1, wn = w >> 2, g = l >> 4;
  lptr pw = c.lds + wk * 21504 + (32 * wm + (l & 15)) * 336 + (wn * 48 + 4 * g) * 4;
#pragma unroll
  for (int mt = 0; mt < 2; ++mt) {
    *(LAS f32x4*)(pw + mt * 16 * 336) = acc[mt][0]; *(LAS f32x4*)(pw + mt * 16 * 336 + 64) = acc[mt][1];
  }
  if (wn == 0) { *(LAS f32x4*)(pw + 128) = acc[0][2]; *(LAS f32x4*)(pw + 16 * 336 + 128) = acc[1][2]; }
  LDS_BAR();
#pragma unroll
  for (int rd = 0; rd < 2; ++rd) {
    if (rd == 0 || w < 2) {
      lptr pr = c.lds + l * 336 + (w + 8 * rd) * 32;
      const f32x4 u0 = *(LAS f32x4*)pr + *(LAS f32x4*)(pr + 21504), u1 = *(LAS f32x4*)(pr + 16) + *(LAS f32x4*)(pr + 21504 + 16);
      const u32x4 rs = res[rd];
      res[rd] = u32x4{cvtpk(u0[0] + bflo(rs[0]), u0[1] + bfhi(rs[0])), cvtpk(u0[2] + bflo(rs[1]), u0[3] + bfhi(rs[1])),
                      cvtpk(u1[0] + bflo(rs[2]), u1[1] + bfhi(rs[2])), cvtpk(u1[2] + bflo(rs[3]), u1[3] + bfhi(rs[3]))};
    }
  }
}

struct F4 { s16x8 a0[2], a1[2], b[2][3]; };
DI void p4_body(const Ctx& c, int rt, int cs, const unsigned (&vo)[3]) {
  const int tid = c.tid, l = c.l, w = c.w, wm = w >> 2, wq = w & 3;
  lptr lds = c.lds;
  const char* ga = c.p.ws + WS_H + (size_t)rt * 81920 + w * 1024;
  const char* gb = c.p.w1 + cs * 384;
  const unsigned voffA = l * 16;
  auto issueA = [&](int ch) { glds_d(ga + ch * 8192, voffA, c.lds0 + (ch & 3) * 8192 + w * 1024); };
  issueA(0); issueA(1); issueA(2); issueA(3);
  const int g = l >> 4, q = (l & 15) >> 2, pp = l & 3;
  const int kx = (wq & 1) ^ wm, mx = wq >> 1;
  unsigned aoff[2][2], boff[2][3];
#pragma unroll
  for (int i = 0; i < 2; ++i) {
#pragma unroll
    for (int m = 0; m < 2; ++m) aoff[i][m] = ((i ^ kx) * 4 + g) * 1024 + (32 * wm + 16 * (m ^ mx) + (l & 15)) * 16;
#pragma unroll
    for (int nt = 0; nt < 3; ++nt) boff[i][nt] = LDS_B + (32 * (i ^ kx) + 16 * (g >> 1) + 4 * (g & 1) + q) * 384 + (((3 * wq + nt) ^ (2 * (g & 1) + (q >> 1))) * 32) + pp * 8;
  }
  f32x4 acc[2][3] = {};
  float sa = 0.f, sq = 0.f, sb[3] = {};
  auto rd = [&](F4& f, int i, int ch) {
    lptr la = lds + (ch & 3) * 8192, lb = lds + (ch & 3) * 24576;
    f.a0[i] = ld128(la + aoff[i][0]); f.a1[i] = ld128(la + aoff[i][1]);
#pragma unroll
    for (int nt = 0; nt < 3; ++nt) f.b[i][nt] = cat(ldtr(lb + boff[i][nt]), ldtr(lb + boff[i][nt] + 3072));
  };
  auto mm = [&](const F4& f, int i) {
#pragma unroll
    for (int nt = 0; nt < 3; ++nt) { acc[0][nt] = mfma16(f.b[i][nt], f.a0[i], acc[0][nt]); acc[1][nt] = mfma16(f.b[i][nt], f.a1[i], acc[1][nt]); }
    if (i == 0) { sa = fsum8(f.a0[0], sa); sq = fsq8(f.a0[0], sq); sb[0] = fsum8(f.b[0][0], sb[0]); PIN(sa); PIN(sq); PIN(sb[0]); }
    else { sb[1] = fsum8(f.b[0][1], sb[1]); sb[2] = fsum8(f.b[0][2], sb[2]); PIN(sb[1]); PIN(sb[2]); }
  };
  auto body = [&](F4& nx, const F4& cu, int ch) {
    if (ch + 3 < 10) { w192_issue(c, gb, vo, ch + 3); issueA(ch + 3); }
    SB(); rd(nx, 0, ch); SB(); mm(cu, 0); SB(); rd(nx, 1, ch); SB(); mm(cu, 1); SB();
  };
  F4 fa, fb;
  WAIT_BAR(3);
  STAMP(18);
  rd(fa, 0, 0); rd(fa, 1, 0);
  WAIT_BAR(2); body(fb, fa, 1);
  WAIT_BAR(5); body(fa, fb, 2);
#pragma unroll 1
  for (int ch = 3; ch < 9; ch += 2) { WAIT3_BAR(ch, 8, 8, 4); body(fb, fa, ch); WAIT3_BAR(ch + 1, 8, 8, 4); body(fa, fb, ch + 1); }
  WAIT_BAR(0); body(fb, fa, 9);
  mm(fb, 0); mm(fb, 1);
  LDS_BAR();
  STAMP(19);
  LAS f32x2* rp = (LAS f32x2*)lds;
  LAS float* cp = (LAS float*)(lds + 8192);
  LAS float* ra = (LAS float*)(lds + 16384); LAS float* rb = ra + 64; LAS float* csum = ra + 128;
  rp[((wq & 1) * 4 + g) * 64 + wm * 32 + 16 * mx + (l & 15)] = f32x2{sa, sq};
#pragma unroll
  for (int nt = 0; nt < 3; ++nt) cp[(wm * 4 + g) * 192 + wq * 48 + nt * 16 + (l & 15)] = sb[nt];
  LDS_BAR();
  if (tid < 64) {
    f32x2 t = rp[tid];
#pragma unroll
    for (int i = 1; i < 8; ++i) t += rp[i * 64 + tid];
    const float mean = t[0] * (1.f / D), var = fmaxf(t[1] * (1.f / D) - mean * mean, 0.f);
    const float a = __builtin_amdgcn_rsqf(var + LN_EPS);
    ra[tid] = a; rb[tid] = -mean * a;
  } else if (tid < 256) {
    const int n = tid - 64; float t = cp[n];
#pragma unroll
    for (int i = 1; i < 8; ++i) t += cp[i * 192 + n];
    csum[n] = t;
  }
  LDS_BAR();
  float a[2], b[2];
#pragma unroll
  for (int m = 0; m < 2; ++m) { a[m] = ra[wm * 32 + 16 * (m ^ mx) + (l & 15)]; b[m] = rb[wm * 32 + 16 * (m ^ mx) + (l & 15)]; }
#pragma unroll
  for (int nt = 0; nt < 3; ++nt) {
    const f32x4 c4 = *(LAS f32x4*)(csum + wq * 48 + nt * 16 + 4 * g);
    unsigned pk[2][2];
#pragma unroll
    for (int m = 0; m < 2; ++m) {
      float y[4];
#pragma unroll
      for (int i = 0; i < 4; ++i) y[i] = fmaxf(fmaf(acc[m][nt][i], a[m], b[m] * c4[i]), 0.f);
      pk[m][0] = cvtpk(y[0], y[1]); pk[m][1] = cvtpk(y[2], y[3]);
    }
    const auto s0 = __builtin_amdgcn_permlane16_swap(pk[0][0], pk[1][0], false, false);
    const auto s1 = __builtin_amdgcn_permlane16_swap(pk[0][1], pk[1][1], false, false);
    st128_d(c.rs, (unsigned)(WS_F + rt * 196608 + (cs * 24 + wq * 6 + nt * 2 + (g >> 1)) * 1024 + (wm * 32 + ((g & 1) ^ mx) * 16 + (l & 15)) * 16), u32x4{s0[0], s1[0], s0[1], s1[1]});
  }
  STAMP(20);
}

__global__ void __launch_bounds__(512, 2) block_kernel(Params p) {
  extern __shared__ __attribute__((aligned(16))) char smem[];
  Ctx c;
  c.p = p; c.lds = (lptr)smem; c.lds0 = (unsigned)(size_t)c.lds; c.tid = threadIdx.x; c.l = c.tid & 63; c.w = rfl(c.tid >> 6);
  c.xcd = blockIdx.x & 7; c.j = blockIdx.x >> 3;
  c.st_lo = c.st_hi = 0;
  STAMP(0);
  c.rs = __builtin_amdgcn_make_buffer_rsrc(p.ws, 0, (unsigned)WS_END, 0x00020000);
  c.tag = ldflag(p.ws, WS_EPOCH) + 1;
  const int w = c.w, l = c.l, rt = 4 * c.xcd + (c.j >> 3), cs = c.j & 7;
  {
    const int panel = 4 * (c.xcd >> 1) + (c.j & 3), ct = 8 * (c.xcd & 1) + (c.j >> 2);
    if (ct < 10) p1_body<false>(c, panel, ct); else if (ct < 15) p1_body<true>(c, panel, ct);
    WAIT_BAR(0);
    if (c.tid == 0) stflag(p.ws, WS_F1 + (ct * 16 + panel) * 8, c.tag);
    STAMP(5);
  }
  {
    const int h = 4 * c.xcd + (c.j >> 3), qb = c.j & 7;
    p2_body(c, h, qb, cs);
    WAIT_BAR(0);
    if (c.tid == 0) stflag(p.ws, WS_F2 + (qb * 32 + h) * 8, c.tag);
    STAMP(11);
  }
  u32x4 res[2] = {};
  unsigned vo1[3];
  {
    const char* xg = p.x + (size_t)(rt * 64 + l) * 1280 + cs * 160 + w * 16;
    res[0] = *(const GAS u32x4*)xg; if (w < 2) res[1] = *(const GAS u32x4*)(xg + 128);
    if (w == 0) poll(p.ws, WS_F2 + (c.xcd * 32 + (l & 31)) * 8, c.tag);
    if (w == 1 && blockIdx.x == 0) {
      poll(p.ws, WS_F1 + l * 8, WS_F1 + 512 + l * 8, c.tag); poll(p.ws, WS_F1 + 1024 + l * 8, WS_F1 + 1536 + l * 8, c.tag);
      if (l == 0) stflag(p.ws, WS_EPOCH, c.tag);
    }
    asm volatile("" : "+v"(res[0]), "+v"(res[1]));
    WAIT_BAR(0);
    STAMP(12);
    f32x4 acc[2][3] = {};
    if (w < 4) g80_loop<5, false, 3>(c, rt, cs, acc); else g80_loop<5, false, 2>(c, rt, cs, acc);
    LDS_BAR();
    STAMP(14);
    g80_reduce(c, acc, res);
    const unsigned ho = WS_H + rt * 81920 + (cs * 10 + w) * 1024 + l * 16;
    st128_d(c.rs, ho, res[0]); if (w < 2) st128_d(c.rs, ho + 8192, res[1]);
    STAMP(15);
    w192_offsets(l, w, vo1);
    if (w != 0) { for (int ch = 0; ch < 4; ++ch) w192_issue(c, p.w1 + cs * 384, vo1, ch); WAIT_BAR(12); } else WAIT_BAR(0);
    if (c.tid == 0) stflag(p.ws, WS_F3 + rt * 128 + cs * 8, c.tag);
    STAMP(16);
  }
  {
    if (w == 0) { poll(p.ws, WS_F3 + rt * 128 + (l & 7) * 8, c.tag); for (int ch = 0; ch < 4; ++ch) w192_issue(c, p.w1 + cs * 384, vo1, ch); }
    LDS_BAR();
    STAMP(17);
    p4_body(c, rt, cs, vo1);
    unsigned vo2[3]; w80_offsets(l, w, vo2);
    if (w != 0) { for (int ch = 0; ch < 4; ++ch) w80_issue(c, p.w2 + cs * 160, vo2, ch); WAIT_BAR(12); } else WAIT_BAR(0);
    if (c.tid == 0) stflag(p.ws, WS_F4 + rt * 128 + cs * 8, c.tag);
    STAMP(21);
    if (w == 0) { poll(p.ws, WS_F4 + rt * 128 + (l & 7) * 8, c.tag); for (int ch = 0; ch < 4; ++ch) w80_issue(c, p.w2 + cs * 160, vo2, ch); }
    LDS_BAR();
    STAMP(22);
  }
  {
    f32x4 acc[2][3] = {};
    if (w < 4) g80_loop<12, true, 3>(c, rt, cs, acc); else g80_loop<12, true, 2>(c, rt, cs, acc);
    LDS_BAR();
    STAMP(24);
    g80_reduce(c, acc, res);
    char* og = p.out + (size_t)(rt * 64 + l) * 1280 + cs * 160 + w * 16;
    *(GAS u32x4*)og = res[0]; if (w < 2) *(GAS u32x4*)(og + 128) = res[1];
    STAMP(25);
  }
}

extern "C" void kernel_launch(void* const* d_in, const int* in_sizes, int n_in, void* d_out, int out_size, void* d_ws, size_t ws_size, hipStream_t stream) {
  Params p;
  p.x = (const char*)d_in[0]; p.wqkv = (const char*)d_in[1]; p.wo = (const char*)d_in[2]; p.w1 = (const char*)d_in[4]; p.w2 = (const char*)d_in[5];
  p.out = (char*)d_out; p.ws = (char*)d_ws;
  (void)hipFuncSetAttribute((const void*)block_kernel, hipFuncAttributeMaxDynamicSharedMemorySize, LDS_BYTES);
  block_kernel<<<256, 512, LDS_BYTES, stream>>>(p);
}
```

```cpp
#include <hip/hip_runtime.h>
#include <hip/hip_bf16.h>

#define DI __device__ __forceinline__
#define LAS __attribute__((address_space(3)))
#define GAS __attribute__((address_space(1)))
typedef unsigned short u16;
typedef unsigned long long u64;
typedef __attribute__((ext_vector_type(8))) short s16x8;
typedef __attribute__((ext_vector_type(4))) short s16x4;
typedef __attribute__((ext_vector_type(8))) __bf16 bf16x8;
typedef __attribute__((ext_vector_type(16))) __bf16 bf16x16;
typedef __attribute__((ext_vector_type(2))) __bf16 bf16x2;
typedef __attribute__((ext_vector_type(2))) float f32x2;
typedef __attribute__((ext_vector_type(4))) float f32x4;
typedef __attribute__((ext_vector_type(8))) float f32x8;
typedef __attribute__((ext_vector_type(16))) float f32x16;
typedef __attribute__((ext_vector_type(32))) float f32x32;
typedef __attribute__((ext_vector_type(4))) unsigned u32x4;
typedef __attribute__((ext_vector_type(2))) unsigned u32x2;
typedef LAS char* lptr;
typedef __amdgpu_buffer_rsrc_t rsrc_t;

constexpr int S = 2048, D = 640, NH = 32, DH = 20, FF = 1536;
constexpr float LN_EPS = 1e-5f;
constexpr float QSCALE = 0.22360679774997896f * 1.4426950408889634f;

constexpr size_t WS_EPOCH = 0, WS_F1 = 4096, WS_F2 = 8192, WS_F3 = 12288, WS_F4 = 16384;
constexpr size_t WS_RS1 = 65536;
constexpr size_t WS_CS1 = WS_RS1 + 2048 * 16;
constexpr size_t WS_CS2 = WS_CS1 + 8 * 1920 * 8;
constexpr size_t WS_RS2 = WS_CS2 + 8 * 1536 * 4;
constexpr size_t WS_W2T = WS_RS2 + 32 * 8 * 512;
constexpr size_t WS_QK = WS_W2T + 1536 * 1280;
constexpr size_t WS_V = WS_QK + (size_t)160 * 2048 * 16;
constexpr size_t WS_O = WS_V + (size_t)256 * 640 * 16;
constexpr size_t WS_H = WS_O + (size_t)S * D * 2;
constexpr size_t WS_F = WS_H + (size_t)S * D * 2;
constexpr size_t WS_END = WS_F + (size_t)S * FF * 2;
constexpr int LDS_BYTES = 163840;
constexpr int LDS_B = 65536;
constexpr unsigned SPIN_CAP = 1u << 18;

#define STAMP(k)
struct Params { const char* x; const char* wqkv; const char* wo; const char* w1; const char* w2; char* out; char* ws; };

DI unsigned rfl(unsigned v) { return __builtin_amdgcn_readfirstlane(v); }
DI unsigned cvtpk(float lo, float hi) { f32x2 v = {lo, hi}; return __builtin_bit_cast(unsigned, __builtin_convertvector(v, bf16x2)); }
DI float bflo(unsigned v) { return __uint_as_float(v << 16); }
DI float bfhi(unsigned v) { return __uint_as_float(v & 0xffff0000u); }
DI void glds(const char* sbase, unsigned voff, unsigned lds) {
  asm volatile("s_mov_b32 m0, %2\n\ts_nop 0\n\tglobal_load_lds_dwordx4 %0, %1" :: "v"(voff), "s"(sbase), "s"(lds) : "memory");
}
DI void glds_half(const char* sbase, unsigned voff, unsigned lds) {
  asm volatile("s_mov_b32 m0, %2\n\ts_mov_b32 exec_hi, 0\n\tglobal_load_lds_dwordx4 %0, %1\n\ts_mov_b32 exec_hi, -1" :: "v"(voff), "s"(sbase), "s"(lds) : "memory");
}
DI void glds_d(const char* sbase, unsigned voff, unsigned lds) {
  asm volatile("s_mov_b32 m0, %2\n\ts_nop 0\n\tglobal_load_lds_dwordx4 %0, %1 sc1" :: "v"(voff), "s"(sbase), "s"(lds) : "memory");
}
#define WAIT_BAR(N) asm volatile("s_waitcnt vmcnt(" #N ") lgkmcnt(0)\n\ts_barrier" ::: "memory")
#define LDS_BAR() asm volatile("s_waitcnt lgkmcnt(0)\n\ts_barrier" ::: "memory")
#define WAIT3_BAR(c, T, A, B) asm volatile("s_cmp_lt_u32 %0, " #T "\n\ts_cbranch_scc1 1f\n\ts_cmp_eq_u32 %0, " #T "\n\ts_cbranch_scc1 2f\n\ts_waitcnt vmcnt(0)\n" \
    "2:\n\ts_waitcnt vmcnt(" #B ")\n1:\n\ts_waitcnt vmcnt(" #A ") lgkmcnt(0)\n\ts_barrier" :: "s"(c) : "memory", "scc")
#define SB() __builtin_amdgcn_sched_barrier(0)
#define XD(x) x
#define XR(x) x
#define mfma32x mfma32
#define mfma16x mfma16
#define PIN(x) asm volatile("" : "+v"(x))
DI s16x8 ld128(lptr p) { return *(const LAS s16x8*)p; }
DI s16x4 ldtr(lptr p) { return __builtin_amdgcn_ds_read_tr16_b64_v4i16((LAS s16x4*)p); }
DI s16x8 cat(s16x4 a, s16x4 b) { return __builtin_shufflevector(a, b, 0, 1, 2, 3, 4, 5, 6, 7); }
DI f32x16 mfma32(s16x8 a, s16x8 b, f32x16 c) { return __builtin_amdgcn_mfma_f32_32x32x16_bf16(__builtin_bit_cast(bf16x8, a), __builtin_bit_cast(bf16x8, b), c, 0, 0, 0); }
DI f32x4 mfma16(s16x8 a, s16x8 b, f32x4 c) { return __builtin_amdgcn_mfma_f32_16x16x32_bf16(__builtin_bit_cast(bf16x8, a), __builtin_bit_cast(bf16x8, b), c, 0, 0, 0); }
DI float dot2(unsigned a, unsigned b, float c) { return __builtin_amdgcn_fdot2_f32_bf16(__builtin_bit_cast(bf16x2, a), __builtin_bit_cast(bf16x2, b), c, false); }
constexpr unsigned ONES2 = 0x3f803f80u;
DI float fsum8(s16x8 f, float c) { u32x4 u = __builtin_bit_cast(u32x4, f); for (int i = 0; i < 4; ++i) c = dot2(u[i], ONES2, c); return c; }
DI float fsq8(s16x8 f, float c) { u32x4 u = __builtin_bit_cast(u32x4, f); for (int i = 0; i < 4; ++i) c = dot2(u[i], u[i], c); return c; }
DI void st128_d(rsrc_t rs, unsigned off, u32x4 v) { __builtin_amdgcn_raw_buffer_store_b128(v, rs, off, 0, 16); }
DI void st64_d(rsrc_t rs, unsigned off, u32x2 v) { __builtin_amdgcn_raw_buffer_store_b64(v, rs, off, 0, 16); }
DI u32x4 ld128_d(rsrc_t rs, unsigned off) { return __builtin_amdgcn_raw_buffer_load_b128(rs, off, 0, 16); }
DI u32x2 ld64_d(rsrc_t rs, unsigned off) { return __builtin_amdgcn_raw_buffer_load_b64(rs, off, 0, 16); }
DI u64 ldflag(const char* ws, unsigned off) { return __hip_atomic_load((GAS u64*)(ws + off), __ATOMIC_RELAXED, __HIP_MEMORY_SCOPE_AGENT); }
DI void stflag(char* ws, unsigned off, u64 v) { __hip_atomic_store((GAS u64*)(ws + off), v, __ATOMIC_RELAXED, __HIP_MEMORY_SCOPE_AGENT); }
DI void poll(const char* ws, unsigned off0, unsigned off1, u64 tag) {
#pragma unroll 1
  for (unsigned n = 0; n < SPIN_CAP; ++n) {
    const u64 a = ldflag(ws, off0), b = ldflag(ws, off1);
    if (__all(a == tag && b == tag)) break;
    __builtin_amdgcn_s_sleep(1);
  }
}
DI void poll(const char* ws, unsigned off0, u64 tag) {
#pragma unroll 1
  for (unsigned n = 0; n < SPIN_CAP; ++n) {
    if (__all(ldflag(ws, off0) == tag)) break;
    __builtin_amdgcn_s_sleep(1);
  }
}

DI float dpp_add(float v, const int ctrl_sel) {
  int t;
  switch (ctrl_sel) {
    case 0: t = __builtin_amdgcn_update_dpp(0, __float_as_int(v), 0xB1, 0xf, 0xf, true); break;
    case 1: t = __builtin_amdgcn_update_dpp(0, __float_as_int(v), 0x4E, 0xf, 0xf, true); break;
    case 2: t = __builtin_amdgcn_update_dpp(0, __float_as_int(v), 0x141, 0xf, 0xf, true); break;
    default: t = __builtin_amdgcn_update_dpp(0, __float_as_int(v), 0x128, 0xf, 0xf, true); break;
  }
  return v + __int_as_float(t);
}
DI float swap16_add(float a, float b) { const auto t = __builtin_amdgcn_permlane16_swap(__float_as_uint(a), __float_as_uint(b), false, false); return __uint_as_float(t[0]) + __uint_as_float(t[1]); }
DI float swap32_add(float a, float b) { const auto t = __builtin_amdgcn_permlane32_swap(__float_as_uint(a), __float_as_uint(b), false, false); return __uint_as_float(t[0]) + __uint_as_float(t[1]); }
DI float wave_sum(float v) { v = dpp_add(v, 0); v = dpp_add(v, 1); v = dpp_add(v, 2); v = dpp_add(v, 3); v = swap16_add(v, v); return swap32_add(v, v); }
DI void colsum_step1(lptr part, int l, int w, u32x4 wa, u32x4 wb) {
  float v[8], u[4], t[2];
#pragma unroll
  for (int i = 0; i < 4; ++i) { v[2 * i] = bflo(wa[i]) + bflo(wb[i]); v[2 * i + 1] = bfhi(wa[i]) + bfhi(wb[i]); }
#pragma unroll
  for (int i = 0; i < 4; ++i) u[i] = swap32_add(v[i], v[i + 4]);
#pragma unroll
  for (int i = 0; i < 2; ++i) t[i] = dpp_add(swap16_add(u[i], u[i + 2]), 3);
  *(LAS f32x2*)(part + (w * 64 + (l & 7) * 8 + 4 * (l >> 5) + 2 * ((l >> 4) & 1)) * 4) = f32x2{t[0], t[1]};
}
DI float colsum_step2(lptr part, int l, int w) {
  float v = *(LAS float*)(part + ((l >> 3) * 64 + 8 * w + (l & 7)) * 4);
  v = dpp_add(v, 3); v = swap16_add(v, v); return swap32_add(v, v);
}
DI void glds_m(const char* sbase, unsigned voff, unsigned lds, u64 mask) {
  asm volatile("s_mov_b32 m0, %2\n\ts_mov_b64 exec, %3\n\tglobal_load_lds_dwordx4 %0, %1 sc1\n\ts_mov_b64 exec, -1" :: "v"(voff), "s"(sbase), "s"(lds), "s"(mask) : "memory");
}
struct Ctx { Params p; lptr lds; unsigned lds0; rsrc_t rs; u64 tag; int tid, l, w, xcd, j; mutable unsigned st_lo, st_hi; };

constexpr int L1_PART = 131072, L1_CG = L1_PART + 2048, L1_RG = L1_CG + 8192, L1_FIN = L1_RG + 2048, L1_TOUCH = L1_FIN + 2048;
struct F1 { s16x8 a0[4], a1[4], b[4]; };
struct P1 { unsigned voffA, voffB; const char* ga; const char* gb; int panel, ct; };
DI void p1_issue(const Ctx& c, const P1& t, int s) {
  const unsigned dst = c.lds0 + (s & 3) * 32768 + c.w * 1024;
  const char* a = t.ga + s * 128; const char* b = t.gb + (size_t)s * 64 * 3840;
  glds(a, t.voffA, dst); glds(b, t.voffB, dst + 16384); glds(a + 64 * 1280, t.voffA, dst + 8192); glds(b + 32 * 3840, t.voffB, dst + 16384 + 8192);
}
template <bool VT>
DI void p1_loop(const Ctx& c, const P1& t, f32x16 (&acc)[2]) {
  const int tid = c.tid, l = c.l, w = c.w, wm = w >> 2, wn = w & 3, r = l & 31, hh = l >> 5;
  lptr lds = c.lds;
  unsigned aoff[4];
  const int q = (l & 15) >> 2, pp = l & 3;
#pragma unroll
  for (int i = 0; i < 4; ++i) aoff[i] = (wm * 64 + r) * 128 + (((2 * i + hh) ^ ((r >> 1) & 7)) << 4);
  const unsigned boff = 16384 + (8 * hh + q) * 256 + (((wn * 4 + 2 * ((l >> 4) & 1) + (pp >> 1)) ^ (q << 2)) << 4) + 8 * (pp & 1);
  auto rd = [&](F1& f, int i, lptr sl) { f.b[i] = cat(ldtr(sl + boff + i * 4096), ldtr(sl + boff + i * 4096 + 1024)); f.a0[i] = ld128(sl + aoff[i]); f.a1[i] = ld128(sl + aoff[i] + 4096); };
  auto mm = [&](const F1& f, int i) {
    if (VT) { acc[0] = mfma32x(f.a0[i], f.b[i], acc[0]); acc[1] = mfma32x(f.a1[i], f.b[i], acc[1]); }
    else    { acc[0] = mfma32x(f.b[i], f.a0[i], acc[0]); acc[1] = mfma32x(f.b[i], f.a1[i], acc[1]); }
  };
  auto step = [&](F1& nx, const F1& cu, int s) {
    WAIT3_BAR(s, 8, 8, 4);
    if (s == 6) {
      glds_d(c.p.ws + WS_CS1 + (w * 1920 + t.ct * 128) * 8, l * 16, c.lds0 + L1_CG + w * 1024);
      glds_m(c.p.ws + WS_RS1 + (t.panel * 128 + 16 * w) * 16, l * 16, c.lds0 + L1_RG + w * 256, 0xFFFFull);
    }
    XD(if (s <= 6) p1_issue(c, t, s + 3);)
    lptr sl = lds + (s & 3) * 32768;
#pragma unroll
    for (int i = 0; i < 4; ++i) { SB(); XR(rd(nx, i, sl);) SB(); mm(cu, i); }
    SB();
  };
  F1 fa, fb;
#pragma unroll
  for (int i = 0; i < 4; ++i) rd(fa, i, lds);
#pragma unroll 1
  for (int s = 1; s < 9; s += 2) { step(fb, fa, s); step(fa, fb, s + 1); }
  step(fb, fa, 9);
  LAS float* ra = (LAS float*)(lds + L1_FIN);
  const unsigned tag = (unsigned)c.tag;
  if (tid < 128) {
    u32x4 g = *(LAS u32x4*)(lds + L1_RG + tid * 16);
    for (unsigned n = 0; (g[1] != tag || g[3] != tag) && n < SPIN_CAP; ++n) { asm volatile("" ::: "memory"); g = ld128_d(c.rs, WS_RS1 + (t.panel * 128 + tid) * 16); }
    const float sc = t.ct < 5 ? QSCALE : 1.f;
    ra[tid] = __uint_as_float(g[0]) * sc; ra[128 + tid] = __uint_as_float(g[2]) * sc;
  } else if (tid < 256) {
    const int n0 = tid - 128; float sum = 0.f; bool ok = true;
#pragma unroll
    for (int i = 0; i < 8; ++i) { const u32x2 g = *(LAS u32x2*)(lds + L1_CG + i * 1024 + n0 * 8); sum += __uint_as_float(g[0]); ok = ok && g[1] == tag; }
    for (unsigned n = 0; !ok && n < SPIN_CAP; ++n) {
      sum = 0.f; ok = true; asm volatile("" ::: "memory");
#pragma unroll 1
      for (int i = 0; i < 8; ++i) { const u32x2 g = ld64_d(c.rs, WS_CS1 + (i * 1920 + t.ct * 128 + n0) * 8); sum += __uint_as_float(g[0]); ok = ok && g[1] == tag; }
    }
    ra[256 + n0] = sum;
  }
#pragma unroll
  for (int i = 0; i < 4; ++i) mm(fb, i);
}
template <bool VT>
DI void p1_epilogue(const Ctx& c, const P1& t, const f32x16 (&acc)[2]) {
  const int l = c.l, w = c.w, wm = w >> 2, wn = w & 3, r = l & 31, hh = l >> 5;
  LAS float* ra = (LAS float*)(c.lds + L1_FIN); LAS float* rb = ra + 128; LAS float* cs = ra + 256;
#pragma unroll
  for (int mt = 0; mt < 2; ++mt) {
    unsigned pk[8];
    if (VT) {
      const float cn = cs[wn * 32 + r];
#pragma unroll
      for (int g = 0; g < 4; ++g) {
        const f32x4 a4 = *(LAS f32x4*)(ra + wm * 64 + mt * 32 + 8 * g + 4 * hh), b4 = *(LAS f32x4*)(rb + wm * 64 + mt * 32 + 8 * g + 4 * hh);
        float y[4];
#pragma unroll
        for (int i = 0; i < 4; ++i) y[i] = fmaf(acc[mt][4 * g + i], a4[i], b4[i] * cn);
        pk[2 * g] = cvtpk(y[0], y[1]); pk[2 * g + 1] = cvtpk(y[2], y[3]);
      }
    } else {
      const float a = ra[wm * 64 + mt * 32 + r], b = rb[wm * 64 + mt * 32 + r];
#pragma unroll
      for (int g = 0; g < 4; ++g) {
        const f32x4 c4 = *(LAS f32x4*)(cs + wn * 32 + 8 * g + 4 * hh);
        float y[4];
#pragma unroll
        for (int i = 0; i < 4; ++i) y[i] = fmaf(acc[mt][4 * g + i], a, b * c4[i]);
        pk[2 * g] = cvtpk(y[0], y[1]); pk[2 * g + 1] = cvtpk(y[2], y[3]);
      }
    }
#pragma unroll
    for (int gp = 0; gp < 2; ++gp) {
      const auto s0 = __builtin_amdgcn_permlane32_swap(pk[4 * gp], pk[4 * gp + 2], false, false);
      const auto s1 = __builtin_amdgcn_permlane32_swap(pk[4 * gp + 1], pk[4 * gp + 3], false, false);
      const u32x4 v = {s0[0], s1[0], s0[1], s1[1]};
      if (VT) st128_d(c.rs, (unsigned)(WS_V + ((t.panel * 16 + wm * 8 + mt * 4 + 2 * gp + hh) * 640 + (t.ct - 10) * 128 + wn * 32 + r) * 16), v);
      else    st128_d(c.rs, (unsigned)(WS_QK + ((t.ct * 16 + wn * 4 + 2 * gp + hh) * 2048 + t.panel * 128 + wm * 64 + mt * 32 + r) * 16), v);
    }
  }
}

DI void w80_offsets(int l, int w, unsigned (&vo)[3]) {
#pragma unroll
  for (int i = 0; i < 3; ++i) {
    const int n = 64 * i + l, rr = n / 10, pos = n - 10 * rr, rho = 16 * w + rr;
    const int k = (rho & ~12) | ((rho & 4) << 1) | ((rho & 8) >> 1);
    vo[i] = k * 1280 + pos * 16;
  }
}
DI void w2t_issue(const Ctx& c, int cs, int ch) {
  const unsigned sb = c.lds0 + LDS_B + (ch & 3) * 20480 + c.w * 2560, vo = c.l * 16;
  const char* b = c.p.ws + WS_W2T + cs * 245760 + ch * 20480 + c.w * 2560;
  glds_d(b, vo, sb); glds_d(b + 1024, vo, sb + 1024); glds_m(b + 2048, vo, sb + 2048, 0xFFFFFFFFull);
}
DI void w192_offsets(int l, int w, unsigned (&vo)[3]) {
#pragma unroll
  for (int i = 0; i < 3; ++i) {
    const int n = 64 * i + l, rr = n / 24, pos = n - 24 * rr, rho = 8 * w + rr;
    const int k = (rho & ~12) | ((rho & 4) << 1) | ((rho & 8) >> 1);
    vo[i] = k * 3072 + ((((pos >> 1) ^ ((rho >> 1) & 3)) << 1) | (pos & 1)) * 16;
  }
}
DI void w192_issue(const Ctx& c, const char* gb, const unsigned (&vo)[3], int ch) {
  const unsigned sb = c.lds0 + LDS_B + (ch & 3) * 24576 + c.w * 3072;
  const char* b = gb + (size_t)ch * 64 * 3072;
  glds(b, vo[0], sb); glds(b, vo[1], sb + 1024); glds(b, vo[2], sb + 2048);
}

constexpr int KRING = 0, VRING = 24576;
#define STR_(x) #x
#define STR(x) STR_(x)
#define EX(b, i) "v_exp_f32 v[" STR(b) "+" STR(i) "], v[" STR(b) "+" STR(i) "]\n\t"
#define EX3(b, i) EX(b, i) EX(b, i + 1) EX(b, i + 2)
#define EX4(b, i) EX3(b, i) EX(b, i + 3)
#define VR(b, n) "v[" STR(b) ":" STR(b) "+" STR(n) "-1]"
#define R_SA 32
#define R_SB 64
#define R_O 96
#define R_NM 112
#define R_Q 128
#define R_QR 132
#define R_KA 134
#define R_KB 138
#define R_KR 142
#define R_V0 144
#define R_V1 148
#define R_V2 152
#define R_V3 156
#define R_IDX 160
#define R_KAD 161
#define R_KRAD 162
#define R_VAD 163
#define R_VO0 164
#define R_VO1 165
#define R_VO2 166
#define R_WO0 167
#define R_WO1 168
#define R_WO2 169
#define VREAD(reg, off) "s_mov_b64 exec, %[vmask]\n\tds_read_b128 " VR(reg, 4) ", v" STR(R_VAD) " offset:" STR(off) "\n\ts_mov_b64 exec, -1\n\t"
#define PV(vreg, sreg) "v_smfmac_f32_32x32x32_bf16 " VR(R_O, 16) ", " VR(vreg, 4) ", " VR(sreg, 8) ", v" STR(R_IDX) "\n\t"
#define QK16(dst, kreg) "v_mfma_f32_32x32x16_bf16 " VR(dst, 16) ", " VR(kreg, 4) ", " VR(R_Q, 4) ", " VR(R_NM, 16) "\n\t"
#define STEP(C, N, KO, VO, MID) \
  "s_waitcnt lgkmcnt(4)\n\t" \
  QK16(N, R_KA) \
  EX4(C, 0) \
  MID \
  "ds_read_b128 " VR(R_KA, 4) ", v" STR(R_KAD) " offset:" STR(KO) "\n\t" \
  PV(R_V3, N + 24) \
  VREAD(R_V3, VO) \
  EX3(C, 4) \
  QK16(N + 16, R_KB) \
  "ds_read_b128 " VR(R_KB, 4) ", v" STR(R_KAD) " offset:" STR(KO + 512) "\n\t" \
  EX4(C, 7) \
  "s_waitcnt lgkmcnt(3)\n\t" \
  PV(R_V0, C) \
  VREAD(R_V0, VO + 640) \
  EX4(C, 11) \
  "v_mfma_f32_32x32x4_2b_bf16 " VR(N, 32) ", " VR(R_KR, 2) ", " VR(R_QR, 2) ", " VR(N, 32) "\n\t" \
  "ds_read_b64 " VR(R_KR, 2) ", v" STR(R_KRAD) " offset:" STR(KO) "\n\t" \
  EX4(C, 15) EX4(C, 19) \
  PV(R_V1, C + 8) \
  VREAD(R_V1, VO + 1280) \
  EX4(C, 23) \
  PV(R_V2, C + 16) \
  VREAD(R_V2, VO + 1920) \
  EX4(C, 27) EX(C, 31)
#define KV_ISSUE(D0, D1, D2) \
  "v_add_u32 v" STR(R_VO0) ", %[st0], v" STR(R_VO0) "\n\tv_add_u32 v" STR(R_VO1) ", %[st1], v" STR(R_VO1) "\n\tv_add_u32 v" STR(R_VO2) ", %[st2], v" STR(R_VO2) "\n\t" \
  "s_mov_b32 m0, %[" D0 "]\n\ts_nop 0\n\tglobal_load_lds_dwordx4 v" STR(R_VO0) ", %[gb] sc1\n\t" \
  "s_mov_b32 m0, %[" D1 "]\n\ts_nop 0\n\tglobal_load_lds_dwordx4 v" STR(R_VO1) ", %[gb] sc1\n\t" \
  "s_mov_b32 m0, %[" D2 "]\n\ts_nop 0\n\tglobal_load_lds_dwordx4 v" STR(R_VO2) ", %[gb] sc1\n\t"
#define TOP_EVEN \
  "s_waitcnt vmcnt(1) lgkmcnt(0)\n\ts_barrier\n\t" KV_ISSUE("d10", "d11", "d12") \
  "s_mov_b32 m0, %[wd]\n\ts_add_u32 %[wd], %[wd], 1024\n\tglobal_load_lds_dwordx4 v" STR(R_WO0) ", %[wb]\n\t" \
  "s_mov_b32 m0, %[wd]\n\ts_add_u32 %[wd], %[wd], 1024\n\tglobal_load_lds_dwordx4 v" STR(R_WO1) ", %[wb]\n\t"
#define TOP_ODD \
  "s_waitcnt vmcnt(2) lgkmcnt(0)\n\ts_barrier\n\t" \
  "s_cmp_eq_u32 %[cnt], 1\n\ts_cbranch_scc1 2f\n\t" KV_ISSUE("d00", "d01", "d02") \
  "2:\n\ts_mov_b32 m0, %[wd]\n\ts_mov_b32 exec_hi, 0\n\tglobal_load_lds_dwordx4 v" STR(R_WO2) ", %[wb]\n\ts_mov_b32 exec_hi, -1\n\t" \
  "s_add_u32 %[wd], %[wd], 20480-2048\n\t" \
  "v_add_u32 v" STR(R_WO0) ", %[wst], v" STR(R_WO0) "\n\tv_add_u32 v" STR(R_WO1) ", %[wst], v" STR(R_WO1) "\n\tv_add_u32 v" STR(R_WO2) ", %[wst], v" STR(R_WO2) "\n\t"

constexpr int L2_PART = 147456;
DI void p2_body(const Ctx& c, int h, int qb, int cs, unsigned (&wo)[3], u32x4 (&wo4)[3], float& cs2) {
  const int l = c.l, w = c.w, r = l & 31, hh = l >> 5;
  const unsigned lds0 = c.lds0;
  const int e = h & 1, cb = 5 * (h >> 1) + 2 * e, q0 = qb * 256 + w * 32;
  unsigned vo[3], sstr[3], dst0[3], dst1[3];
#pragma unroll
  for (int i = 0; i < 3; ++i) {
    int id = w + 8 * i; if (id >= 22) id -= 2;
    const bool isk = id < 12;
    const int blk = id / 3, ch = id - 3 * blk, pv = id - 12, n = 64 * pv + l, kg = n / 20, dd = n - 20 * kg, d = e ? (dd + 4 >= 20 ? dd - 16 : dd + 4) : dd;
    vo[i] = isk ? (unsigned)(WS_QK + ((80 + cb + ch) * 2048 + 128 - 256 + 64 * blk + l) * 16) : (unsigned)(WS_V + ((6 - 32 + kg) * 640 + 20 * h + d) * 16);
    sstr[i] = isk ? 4096u : 32u * 10240u;
    dst0[i] = lds0 + (isk ? KRING + id * 1024 : VRING + pv * 1024);
    dst1[i] = dst0[i] + (isk ? 12288 : 10240);
  }
  unsigned kad = lds0 + KRING + (e + hh) * 1024 + r * 16, krad = lds0 + KRING + (e ? 0 : 2048) + l * 16 + 8 * e;
  unsigned vad = lds0 + VRING + hh * 320 + (r < 20 ? r : 0) * 16;
  if (w == 0) {
    const int sel = l >> 4, f0 = (sel & 2) ? 20 * h : 8 * cb, f1 = f0 + ((sel & 1) ? ((sel & 2) ? 19 : 23) : 0);
    const int ctk = ((sel & 2) ? 10 : 5) + (f1 >> 7), ctq = (8 * cb + ((l & 2) ? 23 : 0)) >> 7;
    poll(c.p.ws, WS_F1 + (ctk * 16 + (l & 15)) * 8, WS_F1 + (ctq * 16 + 2 * qb + (l & 1)) * 8, c.tag);
  }
  LDS_BAR();
  STAMP(6);
#pragma unroll
  for (int i = 0; i < 3; ++i) glds_d(c.p.ws, vo[i], dst1[i]);
#pragma unroll
  for (int i = 0; i < 3; ++i) { vo[i] += sstr[i]; glds_d(c.p.ws, vo[i], dst0[i]); }
  s16x8 q16 = __builtin_bit_cast(s16x8, ld128_d(c.rs, WS_QK + ((cb + e + hh) * 2048 + q0 + r) * 16));
  s16x4 qr = __builtin_bit_cast(s16x4, ld64_d(c.rs, WS_QK + ((e ? cb : cb + 2) * 2048 + q0 + r) * 16 + 8 * e));
  cs2 = colsum_step2(c.lds + L2_PART, l, w);
  asm volatile("" : "+v"(q16), "+v"(qr), "+v"(wo4[0]), "+v"(wo4[1]), "+v"(wo4[2]), "+v"(cs2));
  WAIT_BAR(0);
  STAMP(7);
  lptr k0 = (lptr)(size_t)kad + 12288 + 2 * 3072;
  f32x32 sa;
  {
    const f32x32 z = {};
    sa = __builtin_amdgcn_mfma_f32_32x32x4bf16_1k(*(const LAS s16x4*)((lptr)(size_t)krad + 12288 + 2 * 3072), qr, z, 0, 0, 0);
    f32x16 lo = __builtin_shufflevector(sa, sa, 0, 1, 2, 3, 4, 5, 6, 7, 8, 9, 10, 11, 12, 13, 14, 15);
    f32x16 hi = __builtin_shufflevector(sa, sa, 16, 17, 18, 19, 20, 21, 22, 23, 24, 25, 26, 27, 28, 29, 30, 31);
    lo = mfma32(ld128(k0), q16, lo); hi = mfma32(ld128(k0 + 512), q16, hi);
#pragma unroll
    for (int i = 0; i < 16; ++i) { sa[i] = lo[i]; sa[16 + i] = hi[i]; }
  }
  float m = sa[0];
#pragma unroll
  for (int i = 1; i < 32; ++i) m = fmaxf(m, sa[i]);
  { const auto t = __builtin_amdgcn_permlane32_swap(__float_as_uint(m), __float_as_uint(m), false, false); m = fmaxf(__uint_as_float(t[0]), __uint_as_float(t[1])); }
  f32x16 nm, o = {};
#pragma unroll
  for (int i = 0; i < 16; ++i) nm[i] = -m;
#pragma unroll
  for (int i = 0; i < 32; ++i) sa[i] -= m;
  f32x32 sb = {};
  s16x8 ka = ld128(k0 + 3072), kb = ld128(k0 + 3072 + 512);
  s16x4 kr = *(const LAS s16x4*)((lptr)(size_t)krad + 12288 + 3 * 3072);
  const unsigned pad = r == 20 ? ONES2 : 0u;
  const u32x4 pad4 = {pad, pad, pad, pad};
  lptr vb = (lptr)(size_t)vad + 10240 + 3 * 2560;
  u32x4 v0 = __builtin_bit_cast(u32x4, ld128(vb + 640)), v1 = __builtin_bit_cast(u32x4, ld128(vb + 1280)), v2 = __builtin_bit_cast(u32x4, ld128(vb + 1920)), v3 = pad4;
  if (r >= 20) { v0 = pad4; v1 = pad4; v2 = pad4; }
  unsigned idx = 0xDDDDDDDDu, cnt = 4, wd = lds0 + LDS_B + w * 2560;
  const u64 vmask = 0x000FFFFF000FFFFFull;
  STAMP(8);
  asm volatile(
      "s_nop 4\n"
      "1:\n\t"
      STEP(R_SA, R_SB, 0, 0, TOP_EVEN)
      STEP(R_SB, R_SA, 3072, 2560, "")
      STEP(R_SA, R_SB, 6144, 5120, "")
      STEP(R_SB, R_SA, 9216, 7680, "")
      STEP(R_SA, R_SB, 12288, 10240, TOP_ODD)
      STEP(R_SB, R_SA, 12288 + 3072, 10240 + 2560, "")
      STEP(R_SA, R_SB, 12288 + 6144, 10240 + 5120, "")
      STEP(R_SB, R_SA, 12288 + 9216, 10240 + 7680, "")
      "s_sub_u32 %[cnt], %[cnt], 1\n\t"
      "s_cmp_lg_u32 %[cnt], 0\n\t"
      "s_cbranch_scc1 1b\n\t"
      "s_waitcnt lgkmcnt(0)\n\t"
      PV(R_V3, R_SB + 24)
      "s_nop 15\n\t"
      : "+{v[32:63]}"(sa), "+{v[64:95]}"(sb), "+{v[96:111]}"(o), "+{v[134:137]}"(ka), "+{v[138:141]}"(kb), "+{v[142:143]}"(kr),
        "+{v[144:147]}"(v0), "+{v[148:151]}"(v1), "+{v[152:155]}"(v2), "+{v[156:159]}"(v3),
        "+{v164}"(vo[0]), "+{v165}"(vo[1]), "+{v166}"(vo[2]), "+{v167}"(wo[0]), "+{v168}"(wo[1]), "+{v169}"(wo[2]), [cnt] "+s"(cnt), [wd] "+s"(wd)
      : "{v[112:127]}"(nm), "{v[128:131]}"(q16), "{v[132:133]}"(qr), "{v160}"(idx), "{v161}"(kad), "{v162}"(krad), "{v163}"(vad),
        [vmask] "s"(vmask), [gb] "s"(c.p.ws), [st0] "s"(sstr[0]), [st1] "s"(sstr[1]), [st2] "s"(sstr[2]),
        [d00] "s"(dst0[0]), [d01] "s"(dst0[1]), [d02] "s"(dst0[2]), [d10] "s"(dst1[0]), [d11] "s"(dst1[1]), [d12] "s"(dst1[2]),
        [wb] "s"(c.p.wo + cs * 160), [wst] "s"(128 * 1280)
      : "memory", "scc");
  STAMP(9);
  float lsum;
  { const auto t = __builtin_amdgcn_permlane32_swap(__float_as_uint(o[8]), __float_as_uint(o[8]), false, false); lsum = __uint_as_float(t[1]); }
  float chk = 0.f;
#pragma unroll
  for (int i = 0; i < 12; ++i) chk += fabsf(o[i]);
  if (__any(!(chk < 1e37f) || !(lsum > 0.f))) {
    const unsigned ko = WS_QK + ((80 + cb + e + hh) * 2048 + r) * 16, kro = WS_QK + ((80 + cb + (e ? 0 : 2)) * 2048 + l) * 16 + 8 * e;
    const int d = r < 20 ? (e ? (r + 4 >= 20 ? r - 16 : r + 4) : r) : 0;
    const unsigned vg = WS_V + (hh * 640 + 20 * h + d) * 16;
    float tm = -INFINITY;
#pragma unroll 1
    for (int pass = 0; pass < 2; ++pass) {
#pragma unroll
      for (int i = 0; i < 16; ++i) { nm[i] = pass ? -tm : 0.f; o[i] = 0.f; }
#pragma unroll 1
      for (int b = 0; b < 32; ++b) {
        f32x16 lo = mfma32(__builtin_bit_cast(s16x8, ld128_d(c.rs, ko + b * 1024)), q16, nm), hi = mfma32(__builtin_bit_cast(s16x8, ld128_d(c.rs, ko + b * 1024 + 512)), q16, nm);
        f32x32 s;
#pragma unroll
        for (int i = 0; i < 16; ++i) { s[i] = lo[i]; s[16 + i] = hi[i]; }
        s = __builtin_amdgcn_mfma_f32_32x32x4bf16_1k(__builtin_bit_cast(s16x4, ld64_d(c.rs, kro + b * 1024)), qr, s, 0, 0, 0);
        if (pass == 0) {
#pragma unroll
          for (int i = 0; i < 32; ++i) tm = fmaxf(tm, s[i]);
        } else {
#pragma unroll
          for (int g = 0; g < 4; ++g) {
            f32x8 pr;
#pragma unroll
            for (int i = 0; i < 8; ++i) pr[i] = __builtin_amdgcn_exp2f(s[8 * g + i]);
            u32x4 vf = ld128_d(c.rs, vg + (8 * b + 2 * g) * 10240);
            if (r >= 20) vf = pad4;
            o = __builtin_amdgcn_smfmac_f32_32x32x32_bf16(__builtin_bit_cast(bf16x8, vf), __builtin_bit_cast(bf16x16, pr), o, 0xDDDD, 0, 0);
          }
        }
      }
      if (pass == 0) { const auto t = __builtin_amdgcn_permlane32_swap(__float_as_uint(tm), __float_as_uint(tm), false, false); tm = fmaxf(__uint_as_float(t[0]), __uint_as_float(t[1])); }
    }
    const auto t = __builtin_amdgcn_permlane32_swap(__float_as_uint(o[8]), __float_as_uint(o[8]), false, false); lsum = __uint_as_float(t[1]);
  }
  const float rl = __builtin_amdgcn_rcpf(lsum);
  unsigned pk[6];
#pragma unroll
  for (int i = 0; i < 6; ++i) pk[i] = cvtpk(o[2 * i] * rl, o[2 * i + 1] * rl);
  const auto s0 = __builtin_amdgcn_permlane32_swap(pk[0], pk[2], false, false);
  const auto s1 = __builtin_amdgcn_permlane32_swap(pk[1], pk[3], false, false);
  const int row = q0 + r;
  const unsigned og = WS_O + (row >> 6) * 81920 + (row & 63) * 16;
  st128_d(c.rs, og + (cb + e + hh) * 1024, u32x4{s0[0], s1[0], s0[1], s1[1]});
  if (hh == 0) st64_d(c.rs, og + (e ? cb : cb + 2) * 1024 + 8 * e, u32x2{pk[4], pk[5]});
  STAMP(10);
}

template <int NT> struct F80 { s16x8 a0[2], a1[2], b[2][NT]; };
template <bool LAST, int NT>
DI void g80_loop(const Ctx& c, int rt, int cs, f32x4 (&acc)[2][3], const u32x4 (&wo4)[3]) {
  constexpr int NCH = LAST ? 12 : 5;
  const int l = c.l, w = c.w, wm = w & 1, wk = (w >> 1) & 1, wn = w >> 2;
  lptr lds = c.lds;
  const char* ga = c.p.ws + (LAST ? WS_F : WS_O) + (size_t)rt * (NCH * 16384) + w * 2048;
  const unsigned voffA = l * 16;
  auto aslot = [&](int ch) { return (!LAST && ch == 4) ? 147456 : (ch & 3) * 16384; };
  auto issueA = [&](int ch) { const unsigned sa = c.lds0 + aslot(ch) + w * 2048; const char* a = ga + ch * 16384; glds_d(a, voffA, sa); glds_d(a + 1024, voffA, sa + 1024); };
  issueA(0); issueA(1); issueA(2); issueA(3);
  if (!LAST) issueA(4);
  const int g = l >> 4, q = (l & 15) >> 2, pp = l & 3;
  const unsigned aoff = (8 * wk + g) * 1024 + (32 * wm + (l & 15)) * 16;
  const unsigned boff = LDS_B + (64 * wk + 16 * (g >> 1) + 4 * (g & 1) + q) * 160 + wn * 96 + pp * 8;
  auto rd = [&](F80<NT>& f, int ks, int ch) {
    lptr sa = lds + aslot(ch) + aoff + ks * 4096, sb = lds + (ch & 3) * 20480 + boff + ks * 5120;
    f.a0[ks] = ld128(sa); f.a1[ks] = ld128(sa + 256);
#pragma unroll
    for (int nt = 0; nt < NT; ++nt) f.b[ks][nt] = cat(ldtr(sb + nt * 32), ldtr(sb + 1280 + nt * 32));
  };
  auto mm = [&](const F80<NT>& f, int ks) {
#pragma unroll
    for (int nt = 0; nt < NT; ++nt) { acc[0][nt] = mfma16x(f.b[ks][nt], f.a0[ks], acc[0][nt]); acc[1][nt] = mfma16x(f.b[ks][nt], f.a1[ks], acc[1][nt]); }
  };
  auto body = [&](F80<NT>& nx, const F80<NT>& cu, int ch) {
    XD(if (LAST && ch + 3 < NCH) { w2t_issue(c, cs, ch + 3); issueA(ch + 3); })
    SB(); XR(rd(nx, 0, ch);) SB(); mm(cu, 0); SB(); XR(rd(nx, 1, ch);) SB(); mm(cu, 1); SB();
  };
  F80<NT> fa, fb;
  if constexpr (!LAST) {
    WAIT_BAR(8);
    STAMP(13);
    rd(fa, 0, 0); rd(fa, 1, 0);
    WAIT_BAR(6);
    lptr b4 = lds + LDS_B + w * 2560 + l * 16;
    *(LAS u32x4*)b4 = wo4[0]; *(LAS u32x4*)(b4 + 1024) = wo4[1]; if (l < 32) *(LAS u32x4*)(b4 + 2048) = wo4[2];
    body(fb, fa, 1);
    WAIT_BAR(4); body(fa, fb, 2);
    WAIT_BAR(2); body(fb, fa, 3);
    WAIT_BAR(0); body(fa, fb, 4);
    mm(fa, 0); mm(fa, 1);
  } else {
    WAIT_BAR(6);
    STAMP(23);
    rd(fa, 0, 0); rd(fa, 1, 0);
    WAIT_BAR(4); body(fb, fa, 1);
    WAIT_BAR(7); body(fa, fb, 2);
#pragma unroll 1
    for (int ch = 3; ch < NCH - 1; ch += 2) { WAIT3_BAR(ch, 10, 10, 5); body(fb, fa, ch); WAIT3_BAR(ch + 1, 10, 10, 5); body(fa, fb, ch + 1); }
    WAIT_BAR(0); body(fb, fa, NCH - 1);
    mm(fb, 0); mm(fb, 1);
  }
}
DI void g80_reduce(const Ctx& c, const f32x4 (&acc)[2][3], u32x4 (&res)[2]) {
  const int l = c.l, w = c.w, wm = w & 1, wk = (w >> 1) & 1, wn = w >> 2, g = l >> 4;
  lptr pw = c.lds + wk * 21504 + (32 * wm + (l & 15)) * 336 + (wn * 48 + 4 * g) * 4;
#pragma unroll
  for (int mt = 0; mt < 2; ++mt) {
    *(LAS f32x4*)(pw + mt * 16 * 336) = acc[mt][0]; *(LAS f32x4*)(pw + mt * 16 * 336 + 64) = acc[mt][1];
  }
  if (wn == 0) { *(LAS f32x4*)(pw + 128) = acc[0][2]; *(LAS f32x4*)(pw + 16 * 336 + 128) = acc[1][2]; }
  LDS_BAR();
#pragma unroll
  for (int rd = 0; rd < 2; ++rd) {
    if (rd == 0 || w < 2) {
      lptr pr = c.lds + l * 336 + (w + 8 * rd) * 32;
      const f32x4 u0 = *(LAS f32x4*)pr + *(LAS f32x4*)(pr + 21504), u1 = *(LAS f32x4*)(pr + 16) + *(LAS f32x4*)(pr + 21504 + 16);
      const u32x4 rs = res[rd];
      res[rd] = u32x4{cvtpk(u0[0] + bflo(rs[0]), u0[1] + bfhi(rs[0])), cvtpk(u0[2] + bflo(rs[1]), u0[3] + bfhi(rs[1])),
                      cvtpk(u1[0] + bflo(rs[2]), u1[1] + bfhi(rs[2])), cvtpk(u1[2] + bflo(rs[3]), u1[3] + bfhi(rs[3]))};
    }
  }
}

constexpr int L4_RP = 32768, L4_CP = L4_RP + 4096, L4_FIN = L4_CP + 6144;
struct F4 { s16x8 a0[2], a1[2], b[2][3]; };
DI void p4_body(const Ctx& c, int rt, int cs, const unsigned (&vo)[3]) {
  const int tid = c.tid, l = c.l, w = c.w, wm = w >> 2, wq = w & 3;
  lptr lds = c.lds;
  const char* ga = c.p.ws + WS_H + (size_t)rt * 81920 + w * 1024;
  const char* gb = c.p.w1 + cs * 384;
  const unsigned voffA = l * 16;
  auto issueA = [&](int ch) { glds_d(ga + ch * 8192, voffA, c.lds0 + (ch & 3) * 8192 + w * 1024); };
  glds_m(c.p.ws + WS_RS2 + (rt * 8 + w) * 512, voffA, c.lds0 + L4_RP + w * 512, 0xFFFFFFFFull);
  glds_m(c.p.ws + WS_CS2 + (w * 1536 + cs * 192) * 4, voffA, c.lds0 + L4_CP + w * 768, 0xFFFFFFFFFFFFull);
  issueA(0); issueA(1); issueA(2); issueA(3);
  const int g = l >> 4, q = (l & 15) >> 2, pp = l & 3;
  const unsigned aoff = g * 1024 + (32 * wm + (l & 15)) * 16;
  unsigned boff[3];
#pragma unroll
  for (int nt = 0; nt < 3; ++nt) boff[nt] = LDS_B + (16 * (g >> 1) + 4 * (g & 1) + q) * 384 + (((3 * wq + nt) ^ (2 * (g & 1) + (q >> 1))) * 32) + pp * 8;
  f32x4 acc[2][3] = {};
  auto rd = [&](F4& f, int i, int ch) {
    lptr la = lds + (ch & 3) * 8192 + aoff + i * 4096, lb = lds + (ch & 3) * 24576 + i * 12288;
    f.a0[i] = ld128(la); f.a1[i] = ld128(la + 256);
#pragma unroll
    for (int nt = 0; nt < 3; ++nt) f.b[i][nt] = cat(ldtr(lb + boff[nt]), ldtr(lb + boff[nt] + 3072));
  };
  auto mm = [&](const F4& f, int i) {
#pragma unroll
    for (int nt = 0; nt < 3; ++nt) { acc[0][nt] = mfma16x(f.b[i][nt], f.a0[i], acc[0][nt]); acc[1][nt] = mfma16x(f.b[i][nt], f.a1[i], acc[1][nt]); }
  };
  auto body = [&](F4& nx, const F4& cu, int ch) {
    XD(if (ch + 3 < 10) { w192_issue(c, gb, vo, ch + 3); issueA(ch + 3); })
    SB(); XR(rd(nx, 0, ch);) SB(); mm(cu, 0); SB(); XR(rd(nx, 1, ch);) SB(); mm(cu, 1); SB();
  };
  F4 fa, fb;
  WAIT_BAR(3);
  STAMP(18);
  rd(fa, 0, 0); rd(fa, 1, 0);
  LAS float* ra = (LAS float*)(lds + L4_FIN); LAS float* rb = ra + 64; LAS float* csum = ra + 128;
  if (tid < 64) {
    f32x2 t = *(LAS f32x2*)(lds + L4_RP + tid * 8);
#pragma unroll
    for (int i = 1; i < 8; ++i) t += *(LAS f32x2*)(lds + L4_RP + i * 512 + tid * 8);
    const float mean = t[0] * (1.f / D), var = fmaxf(t[1] * (1.f / D) - mean * mean, 0.f);
    const float a = __builtin_amdgcn_rsqf(var + LN_EPS);
    ra[tid] = a; rb[tid] = -mean * a;
  } else if (tid < 256) {
    const int n = tid - 64; float t = *(LAS float*)(lds + L4_CP + n * 4);
#pragma unroll
    for (int i = 1; i < 8; ++i) t += *(LAS float*)(lds + L4_CP + i * 768 + n * 4);
    csum[n] = t;
  }
  WAIT_BAR(2); body(fb, fa, 1);
  WAIT_BAR(5); body(fa, fb, 2);
#pragma unroll 1
  for (int ch = 3; ch < 9; ch += 2) { WAIT3_BAR(ch, 8, 8, 4); body(fb, fa, ch); WAIT3_BAR(ch + 1, 8, 8, 4); body(fa, fb, ch + 1); }
  WAIT_BAR(0); body(fb, fa, 9);
  float a[2], b[2];
#pragma unroll
  for (int m = 0; m < 2; ++m) { a[m] = ra[wm * 32 + 16 * m + (l & 15)]; b[m] = rb[wm * 32 + 16 * m + (l & 15)]; }
  f32x4 c4[3];
#pragma unroll
  for (int nt = 0; nt < 3; ++nt) c4[nt] = *(LAS f32x4*)(csum + wq * 48 + nt * 16 + 4 * g);
  LDS_BAR();
  if (w != 0) { for (int ch = 0; ch < 4; ++ch) w2t_issue(c, cs, ch); }
  mm(fb, 0); mm(fb, 1);
  STAMP(19);
#pragma unroll
  for (int nt = 0; nt < 3; ++nt) {
    unsigned pk[2][2];
#pragma unroll
    for (int m = 0; m < 2; ++m) {
      float y[4];
#pragma unroll
      for (int i = 0; i < 4; ++i) y[i] = fmaxf(fmaf(acc[m][nt][i], a[m], b[m] * c4[nt][i]), 0.f);
      pk[m][0] = cvtpk(y[0], y[1]); pk[m][1] = cvtpk(y[2], y[3]);
    }
    const auto s0 = __builtin_amdgcn_permlane16_swap(pk[0][0], pk[1][0], false, false);
    const auto s1 = __builtin_amdgcn_permlane16_swap(pk[0][1], pk[1][1], false, false);
    st128_d(c.rs, (unsigned)(WS_F + rt * 196608 + (cs * 24 + wq * 6 + nt * 2 + (g >> 1)) * 1024 + (wm * 32 + (g & 1) * 16 + (l & 15)) * 16), u32x4{s0[0], s1[0], s0[1], s1[1]});
  }
  STAMP(20);
}

__global__ void __launch_bounds__(512, 2) block_kernel(Params p) {
  extern __shared__ __attribute__((aligned(16))) char smem[];
  Ctx c;
  c.p = p; c.lds = (lptr)smem; c.lds0 = (unsigned)(size_t)c.lds; c.tid = threadIdx.x; c.l = c.tid & 63; c.w = rfl(c.tid >> 6);
  c.xcd = blockIdx.x & 7; c.j = blockIdx.x >> 3;
  c.st_lo = c.st_hi = 0;
  STAMP(0);
  c.rs = __builtin_amdgcn_make_buffer_rsrc(p.ws, 0, (unsigned)WS_END, 0x00020000);
  const int w = c.w, l = c.l, tid = c.tid, b = blockIdx.x, rt = 4 * c.xcd + (c.j >> 3), cs = c.j & 7;
  unsigned wo[3]; u32x4 wo4[3]; float cs2;
  {
    P1 t;
    t.panel = 4 * (c.xcd >> 1) + (c.j & 3); t.ct = 8 * (c.xcd & 1) + (c.j >> 2);
    u32x4 xa, xb = {}, wa = {}, wb = {}; u64 ep;
    const int rg = b / 30, cg = b - 30 * rg;
    {
      const char* wsrc = p.wqkv + (size_t)(b < 240 ? rg * 80 * 3840 + cg * 128 : 0);
      const int m1 = (b - 240) >> 31, m2 = m1 & ((w - 2) >> 31);
      asm volatile("global_load_dwordx4 %0, %5, %7\n\tglobal_load_dwordx2 %4, %12, %13 sc1\n\t"
                   "s_mov_b64 exec, 0xffff\n\tglobal_load_dwordx4 %1, %5, %7 offset:1024\n\t"
                   "s_mov_b32 exec_lo, %10\n\ts_mov_b32 exec_hi, %10\n\ts_cbranch_execz 1f\n\tglobal_load_dwordx4 %2, %6, %8\n\t"
                   "s_mov_b32 exec_lo, %11\n\ts_mov_b32 exec_hi, %11\n\ts_cbranch_execz 1f\n\tglobal_load_dwordx4 %3, %6, %9\n1:\n\ts_mov_b64 exec, -1"
                   : "=&v"(xa), "+v"(xb), "+v"(wa), "+v"(wb), "=&v"(ep)
                   : "v"(l * 16), "v"((tid >> 3) * 3840 + (tid & 7) * 16), "s"(p.x + (size_t)(8 * b + w) * 1280), "s"(wsrc), "s"(wsrc + 64 * 3840), "s"(m1), "s"(m2), "v"(0), "s"(p.ws) : "memory");
    }
    {
      const char* pc; unsigned len;
      asm volatile("s_getpc_b64 %0\n.Lblk_pc:\n\ts_mov_b32 %1, .Lblk_end-.Lblk_pc" : "=s"(pc), "=s"(len));
      const unsigned off = (c.j * 64 + l) * 128;
      if (w == 7 && off < len) asm volatile("s_mov_b32 m0, %2\n\ts_nop 0\n\tglobal_load_lds_dword %0, %1" :: "v"(off & ~3u), "s"(pc), "s"(c.lds0 + L1_TOUCH) : "memory");
    }
    t.voffA = (8 * w + (l >> 3)) * 1280 + (((l & 7) ^ ((4 * w + (l >> 4)) & 7)) << 4);
    t.voffB = (4 * w + (l >> 4)) * 3840 + (((l & 15) ^ (((l >> 4) & 3) << 2)) << 4);
    t.ga = p.x + (size_t)t.panel * 128 * 1280; t.gb = p.wqkv + t.ct * 256;
    if (t.ct < 15) { p1_issue(c, t, 0); p1_issue(c, t, 1); p1_issue(c, t, 2); p1_issue(c, t, 3); }
    STAMP(1);
    asm volatile("s_cmp_lt_u32 %5, 15\n\ts_cbranch_scc1 1f\n\ts_waitcnt vmcnt(0)\n1:\n\ts_waitcnt vmcnt(16)" : "+v"(xa), "+v"(xb), "+v"(wa), "+v"(wb), "+v"(ep) : "s"(t.ct) : "memory", "scc");
    c.tag = ep + 1;
    {
      const float sum = wave_sum(fsum8(__builtin_bit_cast(s16x8, xb), fsum8(__builtin_bit_cast(s16x8, xa), 0.f)));
      const float sq = wave_sum(fsq8(__builtin_bit_cast(s16x8, xb), fsq8(__builtin_bit_cast(s16x8, xa), 0.f)));
      const float mean = sum * (1.f / D), var = fmaxf(sq * (1.f / D) - mean * mean, 0.f), a = __builtin_amdgcn_rsqf(var + LN_EPS);
      if (l == 0) st128_d(c.rs, WS_RS1 + (8 * b + w) * 16, u32x4{__float_as_uint(a), (unsigned)c.tag, __float_as_uint(-mean * a), (unsigned)c.tag});
      colsum_step1(c.lds + L1_PART, l, w, wa, wb);
    }
    WAIT_BAR(13);
    STAMP(2);
    {
      const float v = colsum_step2(c.lds + L1_PART, l, w);
      if (l < 8 && b < 240) st64_d(c.rs, WS_CS1 + (rg * 1920 + cg * 64 + 8 * w + l) * 8, u32x2{__float_as_uint(v), (unsigned)c.tag});
    }
    f32x16 acc[2] = {};
    if (t.ct < 10) p1_loop<false>(c, t, acc); else if (t.ct < 15) p1_loop<true>(c, t, acc);
    const int rg1 = b / 24, cg1 = b - 24 * rg1, k2 = 6 * b + tid / 80, c2 = tid % 80;
    u32x4 w1a = {}, w1b = {}, w2r = {};
    if (b < 192) {
      const char* w1s = p.w1 + (size_t)(rg1 * 80 + (tid >> 3)) * 3072 + cg1 * 128 + (tid & 7) * 16;
      w1a = *(const GAS u32x4*)w1s; if (w < 2) w1b = *(const GAS u32x4*)(w1s + 64 * 3072);
    }
    if (tid < 480) w2r = *(const GAS u32x4*)(p.w2 + (size_t)k2 * 1280 + c2 * 16);
    w80_offsets(l, w, wo);
#pragma unroll
    for (int i = 0; i < 3; ++i) wo4[i] = *(const GAS u32x4*)(p.wo + cs * 160 + 512 * 1280 + ((i < 2 || l < 32) ? wo[i] : wo[0]));
    LDS_BAR();
    STAMP(3);
    if (t.ct < 10) p1_epilogue<false>(c, t, acc); else if (t.ct < 15) p1_epilogue<true>(c, t, acc);
    if (tid < 480) {
      const int k7 = k2 & 127, rho = (k7 & ~12) | ((k7 & 4) << 1) | ((k7 & 8) >> 1), sl = c2 / 10;
      st128_d(c.rs, WS_W2T + sl * 245760 + (k2 >> 7) * 20480 + rho * 160 + (c2 - 10 * sl) * 16, w2r);
    }
    STAMP(4);
    WAIT_BAR(0);
    if (tid == 0) stflag(p.ws, WS_F1 + (t.ct * 16 + t.panel) * 8, c.tag);
    STAMP(5);
    colsum_step1(c.lds + L2_PART, l, w, w1a, w1b);
  }
  {
    const int h = 4 * c.xcd + (c.j >> 3), qb = c.j & 7;
    p2_body(c, h, qb, cs, wo, wo4, cs2);
    if (l < 8 && b < 192) __builtin_amdgcn_raw_buffer_store_b32(__float_as_uint(cs2), c.rs, WS_CS2 + ((b / 24) * 1536 + (b % 24) * 64 + 8 * w + l) * 4, 0, 16);
    WAIT_BAR(0);
    if (tid == 0) stflag(p.ws, WS_F2 + (qb * 32 + h) * 8, c.tag);
    STAMP(11);
  }
  u32x4 res[2] = {};
  unsigned vo1[3];
  {
    const char* xg = p.x + (size_t)(rt * 64 + l) * 1280 + cs * 160 + w * 16;
    res[0] = *(const GAS u32x4*)xg; if (w < 2) res[1] = *(const GAS u32x4*)(xg + 128);
    if (w == 0) poll(p.ws, WS_F2 + (c.xcd * 32 + (l & 31)) * 8, c.tag);
    asm volatile("" : "+v"(res[0]), "+v"(res[1]));
    WAIT_BAR(0);
    STAMP(12);
    f32x4 acc[2][3] = {};
    if (w < 4) g80_loop<false, 3>(c, rt, cs, acc, wo4); else g80_loop<false, 2>(c, rt, cs, acc, wo4);
    LDS_BAR();
    STAMP(14);
    g80_reduce(c, acc, res);
    const unsigned ho = WS_H + rt * 81920 + (cs * 10 + w) * 1024 + l * 16;
    st128_d(c.rs, ho, res[0]); if (w < 2) st128_d(c.rs, ho + 8192, res[1]);
    LAS f32x2* sp = (LAS f32x2*)(c.lds + 45056);
    sp[w * 64 + l] = f32x2{fsum8(__builtin_bit_cast(s16x8, res[1]), fsum8(__builtin_bit_cast(s16x8, res[0]), 0.f)), fsq8(__builtin_bit_cast(s16x8, res[1]), fsq8(__builtin_bit_cast(s16x8, res[0]), 0.f))};
    w192_offsets(l, w, vo1);
    LDS_BAR();
    STAMP(15);
    if (w != 0) { for (int ch = 0; ch < 4; ++ch) w192_issue(c, p.w1 + cs * 384, vo1, ch); WAIT_BAR(12); }
    else {
      f32x2 t = sp[l];
#pragma unroll
      for (int i = 1; i < 8; ++i) t += sp[i * 64 + l];
      st64_d(c.rs, WS_RS2 + (rt * 8 + cs) * 512 + l * 8, __builtin_bit_cast(u32x2, t));
      WAIT_BAR(0);
    }
    if (tid == 0) stflag(p.ws, WS_F3 + rt * 128 + cs * 8, c.tag);
    STAMP(16);
  }
  {
    if (w == 0) { poll(p.ws, WS_F3 + rt * 128 + (l & 7) * 8, c.tag); for (int ch = 0; ch < 4; ++ch) w192_issue(c, p.w1 + cs * 384, vo1, ch); }
    if (w == 1) {
      poll(p.ws, WS_F2 + l * 8, WS_F2 + 512 + l * 8, c.tag); poll(p.ws, WS_F2 + 1024 + l * 8, WS_F2 + 1536 + l * 8, c.tag);
      if (b == 0 && l == 0) stflag(p.ws, WS_EPOCH, c.tag);
    }
    LDS_BAR();
    STAMP(17);
    p4_body(c, rt, cs, vo1);
    WAIT_BAR(0);
    if (tid == 0) stflag(p.ws, WS_F4 + rt * 128 + cs * 8, c.tag);
    STAMP(21);
    if (w == 0) { poll(p.ws, WS_F4 + rt * 128 + (l & 7) * 8, c.tag); for (int ch = 0; ch < 4; ++ch) w2t_issue(c, cs, ch); }
    LDS_BAR();
    STAMP(22);
  }
  {
    f32x4 acc[2][3] = {};
    if (w < 4) g80_loop<true, 3>(c, rt, cs, acc, wo4); else g80_loop<true, 2>(c, rt, cs, acc, wo4);
    LDS_BAR();
    STAMP(24);
    g80_reduce(c, acc, res);
    char* og = p.out + (size_t)(rt * 64 + l) * 1280 + cs * 160 + w * 16;
    *(GAS u32x4*)og = res[0]; if (w < 2) *(GAS u32x4*)(og + 128) = res[1];
    STAMP(25);
  }
  asm volatile(".Lblk_end:");
}

extern "C" void kernel_launch(void* const* d_in, const int* in_sizes, int n_in, void* d_out, int out_size, void* d_ws, size_t ws_size, hipStream_t stream) {
  Params p;
  p.x = (const char*)d_in[0]; p.wqkv = (const char*)d_in[1]; p.wo = (const char*)d_in[2]; p.w1 = (const char*)d_in[4]; p.w2 = (const char*)d_in[5];
  p.out = (char*)d_out; p.ws = (char*)d_ws;
  (void)hipFuncSetAttribute((const void*)block_kernel, hipFuncAttributeMaxDynamicSharedMemorySize, LDS_BYTES);
  block_kernel<<<256, 512, LDS_BYTES, stream>>>(p);
}
```

```cpp
#define PK 3
#define PX 2000
#include <hip/hip_runtime.h>
#include <hip/hip_bf16.h>

#define DI __device__ __forceinline__
#define LAS __attribute__((address_space(3)))
#define GAS __attribute__((address_space(1)))
typedef unsigned short u16;
typedef unsigned long long u64;
typedef __attribute__((ext_vector_type(8))) short s16x8;
typedef __attribute__((ext_vector_type(4))) short s16x4;
typedef __attribute__((ext_vector_type(8))) __bf16 bf16x8;
typedef __attribute__((ext_vector_type(16))) __bf16 bf16x16;
typedef __attribute__((ext_vector_type(2))) __bf16 bf16x2;
typedef __attribute__((ext_vector_type(2))) float f32x2;
typedef __attribute__((ext_vector_type(4))) float f32x4;
typedef __attribute__((ext_vector_type(8))) float f32x8;
typedef __attribute__((ext_vector_type(16))) float f32x16;
typedef __attribute__((ext_vector_type(32))) float f32x32;
typedef __attribute__((ext_vector_type(4))) unsigned u32x4;
typedef __attribute__((ext_vector_type(2))) unsigned u32x2;
typedef LAS char* lptr;
typedef __amdgpu_buffer_rsrc_t rsrc_t;

constexpr int S = 2048, D = 640, NH = 32, DH = 20, FF = 1536;
constexpr float LN_EPS = 1e-5f;
constexpr float QSCALE = 0.22360679774997896f * 1.4426950408889634f;

constexpr size_t WS_EPOCH = 0, WS_F1 = 4096, WS_F2 = 8192, WS_F3 = 12288, WS_F4 = 16384;
constexpr size_t WS_RS1 = 65536;
constexpr size_t WS_CS1 = WS_RS1 + 2048 * 16;
constexpr size_t WS_CS2 = WS_CS1 + 8 * 1920 * 8;
constexpr size_t WS_RS2 = WS_CS2 + 8 * 1536 * 4;
constexpr size_t WS_W2T = WS_RS2 + 32 * 8 * 512;
constexpr size_t WS_QK = WS_W2T + 1536 * 1280;
constexpr size_t WS_V = WS_QK + (size_t)160 * 2048 * 16;
constexpr size_t WS_O = WS_V + (size_t)256 * 640 * 16;
constexpr size_t WS_H = WS_O + (size_t)S * D * 2;
constexpr size_t WS_F = WS_H + (size_t)S * D * 2;
constexpr size_t WS_END = WS_F + (size_t)S * FF * 2;
constexpr int LDS_BYTES = 163840;
constexpr int LDS_B = 65536;
constexpr unsigned SPIN_CAP = 1u << 18;

#define STAMP(k) do { if ((k) == 0) { asm volatile("s_memrealtime %0\n\ts_waitcnt lgkmcnt(0)" : "=s"(c.t0) :: "memory"); } \
  if ((k) == PK) { u64 t_; do { asm volatile("s_memrealtime %0\n\ts_waitcnt lgkmcnt(0)" : "=s"(t_) :: "memory"); } while (t_ - c.t0 < PX); } } while (0)
struct Params { const char* x; const char* wqkv; const char* wo; const char* w1; const char* w2; char* out; char* ws; };

DI unsigned rfl(unsigned v) { return __builtin_amdgcn_readfirstlane(v); }
DI unsigned cvtpk(float lo, float hi) { f32x2 v = {lo, hi}; return __builtin_bit_cast(unsigned, __builtin_convertvector(v, bf16x2)); }
DI float bflo(unsigned v) { return __uint_as_float(v << 16); }
DI float bfhi(unsigned v) { return __uint_as_float(v & 0xffff0000u); }
DI void glds(const char* sbase, unsigned voff, unsigned lds) {
  asm volatile("s_mov_b32 m0, %2\n\ts_nop 0\n\tglobal_load_lds_dwordx4 %0, %1" :: "v"(voff), "s"(sbase), "s"(lds) : "memory");
}
DI void glds_half(const char* sbase, unsigned voff, unsigned lds) {
  asm volatile("s_mov_b32 m0, %2\n\ts_mov_b32 exec_hi, 0\n\tglobal_load_lds_dwordx4 %0, %1\n\ts_mov_b32 exec_hi, -1" :: "v"(voff), "s"(sbase), "s"(lds) : "memory");
}
DI void glds_d(const char* sbase, unsigned voff, unsigned lds) {
  asm volatile("s_mov_b32 m0, %2\n\ts_nop 0\n\tglobal_load_lds_dwordx4 %0, %1 sc1" :: "v"(voff), "s"(sbase), "s"(lds) : "memory");
}
#define WAIT_BAR(N) asm volatile("s_waitcnt vmcnt(" #N ") lgkmcnt(0)\n\ts_barrier" ::: "memory")
#define LDS_BAR() asm volatile("s_waitcnt lgkmcnt(0)\n\ts_barrier" ::: "memory")
#define WAIT3_BAR(c, T, A, B) asm volatile("s_cmp_lt_u32 %0, " #T "\n\ts_cbranch_scc1 1f\n\ts_cmp_eq_u32 %0, " #T "\n\ts_cbranch_scc1 2f\n\ts_waitcnt vmcnt(0)\n" \
    "2:\n\ts_waitcnt vmcnt(" #B ")\n1:\n\ts_waitcnt vmcnt(" #A ") lgkmcnt(0)\n\ts_barrier" :: "s"(c) : "memory", "scc")
#define SB() __builtin_amdgcn_sched_barrier(0)
#define XD(x) x
#define XR(x) x
#define mfma32x mfma32
#define mfma16x mfma16
#define PIN(x) asm volatile("" : "+v"(x))
DI s16x8 ld128(lptr p) { return *(const LAS s16x8*)p; }
DI s16x4 ldtr(lptr p) { return __builtin_amdgcn_ds_read_tr16_b64_v4i16((LAS s16x4*)p); }
DI s16x8 cat(s16x4 a, s16x4 b) { return __builtin_shufflevector(a, b, 0, 1, 2, 3, 4, 5, 6, 7); }
DI f32x16 mfma32(s16x8 a, s16x8 b, f32x16 c) { return __builtin_amdgcn_mfma_f32_32x32x16_bf16(__builtin_bit_cast(bf16x8, a), __builtin_bit_cast(bf16x8, b), c, 0, 0, 0); }
DI f32x4 mfma16(s16x8 a, s16x8 b, f32x4 c) { return __builtin_amdgcn_mfma_f32_16x16x32_bf16(__builtin_bit_cast(bf16x8, a), __builtin_bit_cast(bf16x8, b), c, 0, 0, 0); }
DI float dot2(unsigned a, unsigned b, float c) { return __builtin_amdgcn_fdot2_f32_bf16(__builtin_bit_cast(bf16x2, a), __builtin_bit_cast(bf16x2, b), c, false); }
constexpr unsigned ONES2 = 0x3f803f80u;
DI float fsum8(s16x8 f, float c) { u32x4 u = __builtin_bit_cast(u32x4, f); for (int i = 0; i < 4; ++i) c = dot2(u[i], ONES2, c); return c; }
DI float fsq8(s16x8 f, float c) { u32x4 u = __builtin_bit_cast(u32x4, f); for (int i = 0; i < 4; ++i) c = dot2(u[i], u[i], c); return c; }
DI void st128_d(rsrc_t rs, unsigned off, u32x4 v) { __builtin_amdgcn_raw_buffer_store_b128(v, rs, off, 0, 16); }
DI void st64_d(rsrc_t rs, unsigned off, u32x2 v) { __builtin_amdgcn_raw_buffer_store_b64(v, rs, off, 0, 16); }
DI u32x4 ld128_d(rsrc_t rs, unsigned off) { return __builtin_amdgcn_raw_buffer_load_b128(rs, off, 0, 16); }
DI u32x2 ld64_d(rsrc_t rs, unsigned off) { return __builtin_amdgcn_raw_buffer_load_b64(rs, off, 0, 16); }
DI u64 ldflag(const char* ws, unsigned off) { return __hip_atomic_load((GAS u64*)(ws + off), __ATOMIC_RELAXED, __HIP_MEMORY_SCOPE_AGENT); }
DI void stflag(char* ws, unsigned off, u64 v) { __hip_atomic_store((GAS u64*)(ws + off), v, __ATOMIC_RELAXED, __HIP_MEMORY_SCOPE_AGENT); }
DI void poll(const char* ws, unsigned off0, unsigned off1, u64 tag) {
#pragma unroll 1
  for (unsigned n = 0; n < SPIN_CAP; ++n) {
    const u64 a = ldflag(ws, off0), b = ldflag(ws, off1);
    if (__all(a == tag && b == tag)) break;
    __builtin_amdgcn_s_sleep(1);
  }
}
DI void poll(const char* ws, unsigned off0, u64 tag) {
#pragma unroll 1
  for (unsigned n = 0; n < SPIN_CAP; ++n) {
    if (__all(ldflag(ws, off0) == tag)) break;
    __builtin_amdgcn_s_sleep(1);
  }
}

DI float dpp_add(float v, const int ctrl_sel) {
  int t;
  switch (ctrl_sel) {
    case 0: t = __builtin_amdgcn_update_dpp(0, __float_as_int(v), 0xB1, 0xf, 0xf, true); break;
    case 1: t = __builtin_amdgcn_update_dpp(0, __float_as_int(v), 0x4E, 0xf, 0xf, true); break;
    case 2: t = __builtin_amdgcn_update_dpp(0, __float_as_int(v), 0x141, 0xf, 0xf, true); break;
    default: t = __builtin_amdgcn_update_dpp(0, __float_as_int(v), 0x128, 0xf, 0xf, true); break;
  }
  return v + __int_as_float(t);
}
DI float swap16_add(float a, float b) { const auto t = __builtin_amdgcn_permlane16_swap(__float_as_uint(a), __float_as_uint(b), false, false); return __uint_as_float(t[0]) + __uint_as_float(t[1]); }
DI float swap32_add(float a, float b) { const auto t = __builtin_amdgcn_permlane32_swap(__float_as_uint(a), __float_as_uint(b), false, false); return __uint_as_float(t[0]) + __uint_as_float(t[1]); }
DI float wave_sum(float v) { v = dpp_add(v, 0); v = dpp_add(v, 1); v = dpp_add(v, 2); v = dpp_add(v, 3); v = swap16_add(v, v); return swap32_add(v, v); }
DI void colsum_step1(lptr part, int l, int w, u32x4 wa, u32x4 wb) {
  float v[8], u[4], t[2];
#pragma unroll
  for (int i = 0; i < 4; ++i) { v[2 * i] = bflo(wa[i]) + bflo(wb[i]); v[2 * i + 1] = bfhi(wa[i]) + bfhi(wb[i]); }
#pragma unroll
  for (int i = 0; i < 4; ++i) u[i] = swap32_add(v[i], v[i + 4]);
#pragma unroll
  for (int i = 0; i < 2; ++i) t[i] = dpp_add(swap16_add(u[i], u[i + 2]), 3);
  *(LAS f32x2*)(part + (w * 64 + (l & 7) * 8 + 4 * (l >> 5) + 2 * ((l >> 4) & 1)) * 4) = f32x2{t[0], t[1]};
}
DI float colsum_step2(lptr part, int l, int w) {
  float v = *(LAS float*)(part + ((l >> 3) * 64 + 8 * w + (l & 7)) * 4);
  v = dpp_add(v, 3); v = swap16_add(v, v); return swap32_add(v, v);
}
DI void glds_m(const char* sbase, unsigned voff, unsigned lds, u64 mask) {
  asm volatile("s_mov_b32 m0, %2\n\ts_mov_b64 exec, %3\n\tglobal_load_lds_dwordx4 %0, %1 sc1\n\ts_mov_b64 exec, -1" :: "v"(voff), "s"(sbase), "s"(lds), "s"(mask) : "memory");
}
struct Ctx { Params p; lptr lds; unsigned lds0; rsrc_t rs; u64 tag; int tid, l, w, xcd, j; mutable unsigned st_lo, st_hi; mutable u64 t0; };

constexpr int L1_PART = 131072, L1_CG = L1_PART + 2048, L1_RG = L1_CG + 8192, L1_FIN = L1_RG + 2048, L1_TOUCH = L1_FIN + 2048;
struct F1 { s16x8 a0[4], a1[4], b[4]; };
struct P1 { unsigned voffA, voffB; const char* ga; const char* gb; int panel, ct; };
DI void p1_issue(const Ctx& c, const P1& t, int s) {
  const unsigned dst = c.lds0 + (s & 3) * 32768 + c.w * 1024;
  const char* a = t.ga + s * 128; const char* b = t.gb + (size_t)s * 64 * 3840;
  glds(a, t.voffA, dst); glds(b, t.voffB, dst + 16384); glds(a + 64 * 1280, t.voffA, dst + 8192); glds(b + 32 * 3840, t.voffB, dst + 16384 + 8192);
}
template <bool VT>
DI void p1_loop(const Ctx& c, const P1& t, f32x16 (&acc)[2]) {
  const int tid = c.tid, l = c.l, w = c.w, wm = w >> 2, wn = w & 3, r = l & 31, hh = l >> 5;
  lptr lds = c.lds;
  unsigned aoff[4];
  const int q = (l & 15) >> 2, pp = l & 3;
#pragma unroll
  for (int i = 0; i < 4; ++i) aoff[i] = (wm * 64 + r) * 128 + (((2 * i + hh) ^ ((r >> 1) & 7)) << 4);
  const unsigned boff = 16384 + (8 * hh + q) * 256 + (((wn * 4 + 2 * ((l >> 4) & 1) + (pp >> 1)) ^ (q << 2)) << 4) + 8 * (pp & 1);
  auto rd = [&](F1& f, int i, lptr sl) { f.b[i] = cat(ldtr(sl + boff + i * 4096), ldtr(sl + boff + i * 4096 + 1024)); f.a0[i] = ld128(sl + aoff[i]); f.a1[i] = ld128(sl + aoff[i] + 4096); };
  auto mm = [&](const F1& f, int i) {
    if (VT) { acc[0] = mfma32x(f.a0[i], f.b[i], acc[0]); acc[1] = mfma32x(f.a1[i], f.b[i], acc[1]); }
    else    { acc[0] = mfma32x(f.b[i], f.a0[i], acc[0]); acc[1] = mfma32x(f.b[i], f.a1[i], acc[1]); }
  };
  auto step = [&](F1& nx, const F1& cu, int s) {
    WAIT3_BAR(s, 8, 8, 4);
    if (s == 6) {
      glds_d(c.p.ws + WS_CS1 + (w * 1920 + t.ct * 128) * 8, l * 16, c.lds0 + L1_CG + w * 1024);
      glds_m(c.p.ws + WS_RS1 + (t.panel * 128 + 16 * w) * 16, l * 16, c.lds0 + L1_RG + w * 256, 0xFFFFull);
    }
    XD(if (s <= 6) p1_issue(c, t, s + 3);)
    lptr sl = lds + (s & 3) * 32768;
#pragma unroll
    for (int i = 0; i < 4; ++i) { SB(); XR(rd(nx, i, sl);) SB(); mm(cu, i); }
    SB();
  };
  F1 fa, fb;
#pragma unroll
  for (int i = 0; i < 4; ++i) rd(fa, i, lds);
#pragma unroll 1
  for (int s = 1; s < 9; s += 2) { step(fb, fa, s); step(fa, fb, s + 1); }
  step(fb, fa, 9);
  LAS float* ra = (LAS float*)(lds + L1_FIN);
  const unsigned tag = (unsigned)c.tag;
  if (tid < 128) {
    u32x4 g = *(LAS u32x4*)(lds + L1_RG + tid * 16);
    for (unsigned n = 0; (g[1] != tag || g[3] != tag) && n < SPIN_CAP; ++n) { asm volatile("" ::: "memory"); g = ld128_d(c.rs, WS_RS1 + (t.panel * 128 + tid) * 16); }
    const float sc = t.ct < 5 ? QSCALE : 1.f;
    ra[tid] = __uint_as_float(g[0]) * sc; ra[128 + tid] = __uint_as_float(g[2]) * sc;
  } else if (tid < 256) {
    const int n0 = tid - 128; float sum = 0.f; bool ok = true;
#pragma unroll
    for (int i = 0; i < 8; ++i) { const u32x2 g = *(LAS u32x2*)(lds + L1_CG + i * 1024 + n0 * 8); sum += __uint_as_float(g[0]); ok = ok && g[1] == tag; }
    for (unsigned n = 0; !ok && n < SPIN_CAP; ++n) {
      sum = 0.f; ok = true; asm volatile("" ::: "memory");
#pragma unroll 1
      for (int i = 0; i < 8; ++i) { const u32x2 g = ld64_d(c.rs, WS_CS1 + (i * 1920 + t.ct * 128 + n0) * 8); sum += __uint_as_float(g[0]); ok = ok && g[1] == tag; }
    }
    ra[256 + n0] = sum;
  }
#pragma unroll
  for (int i = 0; i < 4; ++i) mm(fb, i);
}
template <bool VT>
DI void p1_epilogue(const Ctx& c, const P1& t, const f32x16 (&acc)[2]) {
  const int l = c.l, w = c.w, wm = w >> 2, wn = w & 3, r = l & 31, hh = l >> 5;
  LAS float* ra = (LAS float*)(c.lds + L1_FIN); LAS float* rb = ra + 128; LAS float* cs = ra + 256;
#pragma unroll
  for (int mt = 0; mt < 2; ++mt) {
    unsigned pk[8];
    if (VT) {
      const float cn = cs[wn * 32 + r];
#pragma unroll
      for (int g = 0; g < 4; ++g) {
        const f32x4 a4 = *(LAS f32x4*)(ra + wm * 64 + mt * 32 + 8 * g + 4 * hh), b4 = *(LAS f32x4*)(rb + wm * 64 + mt * 32 + 8 * g + 4 * hh);
        float y[4];
#pragma unroll
        for (int i = 0; i < 4; ++i) y[i] = fmaf(acc[mt][4 * g + i], a4[i], b4[i] * cn);
        pk[2 * g] = cvtpk(y[0], y[1]); pk[2 * g + 1] = cvtpk(y[2], y[3]);
      }
    } else {
      const float a = ra[wm * 64 + mt * 32 + r], b = rb[wm * 64 + mt * 32 + r];
#pragma unroll
      for (int g = 0; g < 4; ++g) {
        const f32x4 c4 = *(LAS f32x4*)(cs + wn * 32 + 8 * g + 4 * hh);
        float y[4];
#pragma unroll
        for (int i = 0; i < 4; ++i) y[i] = fmaf(acc[mt][4 * g + i], a, b * c4[i]);
        pk[2 * g] = cvtpk(y[0], y[1]); pk[2 * g + 1] = cvtpk(y[2], y[3]);
      }
    }
#pragma unroll
    for (int gp = 0; gp < 2; ++gp) {
      const auto s0 = __builtin_amdgcn_permlane32_swap(pk[4 * gp], pk[4 * gp + 2], false, false);
      const auto s1 = __builtin_amdgcn_permlane32_swap(pk[4 * gp + 1], pk[4 * gp + 3], false, false);
      const u32x4 v = {s0[0], s1[0], s0[1], s1[1]};
      if (VT) st128_d(c.rs, (unsigned)(WS_V + ((t.panel * 16 + wm * 8 + mt * 4 + 2 * gp + hh) * 640 + (t.ct - 10) * 128 + wn * 32 + r) * 16), v);
      else    st128_d(c.rs, (unsigned)(WS_QK + ((t.ct * 16 + wn * 4 + 2 * gp + hh) * 2048 + t.panel * 128 + wm * 64 + mt * 32 + r) * 16), v);
    }
  }
}

DI void w80_offsets(int l, int w, unsigned (&vo)[3]) {
#pragma unroll
  for (int i = 0; i < 3; ++i) {
    const int n = 64 * i + l, rr = n / 10, pos = n - 10 * rr, rho = 16 * w + rr;
    const int k = (rho & ~12) | ((rho & 4) << 1) | ((rho & 8) >> 1);
    vo[i] = k * 1280 + pos * 16;
  }
}
DI void w2t_issue(const Ctx& c, int cs, int ch) {
  const unsigned sb = c.lds0 + LDS_B + (ch & 3) * 20480 + c.w * 2560, vo = c.l * 16;
  const char* b = c.p.ws + WS_W2T + cs * 245760 + ch * 20480 + c.w * 2560;
  glds_d(b, vo, sb); glds_d(b + 1024, vo, sb + 1024); glds_m(b + 2048, vo, sb + 2048, 0xFFFFFFFFull);
}
DI void w192_offsets(int l, int w, unsigned (&vo)[3]) {
#pragma unroll
  for (int i = 0; i < 3; ++i) {
    const int n = 64 * i + l, rr = n / 24, pos = n - 24 * rr, rho = 8 * w + rr;
    const int k = (rho & ~12) | ((rho & 4) << 1) | ((rho & 8) >> 1);
    vo[i] = k * 3072 + ((((pos >> 1) ^ ((rho >> 1) & 3)) << 1) | (pos & 1)) * 16;
  }
}
DI void w192_issue(const Ctx& c, const char* gb, const unsigned (&vo)[3], int ch) {
  const unsigned sb = c.lds0 + LDS_B + (ch & 3) * 24576 + c.w * 3072;
  const char* b = gb + (size_t)ch * 64 * 3072;
  glds(b, vo[0], sb); glds(b, vo[1], sb + 1024); glds(b, vo[2], sb + 2048);
}

constexpr int KRING = 0, VRING = 24576;
#define STR_(x) #x
#define STR(x) STR_(x)
#define EX(b, i) "v_exp_f32 v[" STR(b) "+" STR(i) "], v[" STR(b) "+" STR(i) "]\n\t"
#define EX3(b, i) EX(b, i) EX(b, i + 1) EX(b, i + 2)
#define EX4(b, i) EX3(b, i) EX(b, i + 3)
#define VR(b, n) "v[" STR(b) ":" STR(b) "+" STR(n) "-1]"
#define R_SA 32
#define R_SB 64
#define R_O 96
#define R_NM 112
#define R_Q 128
#define R_QR 132
#define R_KA 134
#define R_KB 138
#define R_KR 142
#define R_V0 144
#define R_V1 148
#define R_V2 152
#define R_V3 156
#define R_IDX 160
#define R_KAD 161
#define R_KRAD 162
#define R_VAD 163
#define R_VO0 164
#define R_VO1 165
#define R_VO2 166
#define R_WO0 167
#define R_WO1 168
#define R_WO2 169
#define VREAD(reg, off) "s_mov_b64 exec, %[vmask]\n\tds_read_b128 " VR(reg, 4) ", v" STR(R_VAD) " offset:" STR(off) "\n\ts_mov_b64 exec, -1\n\t"
#define PV(vreg, sreg) "v_smfmac_f32_32x32x32_bf16 " VR(R_O, 16) ", " VR(vreg, 4) ", " VR(sreg, 8) ", v" STR(R_IDX) "\n\t"
#define QK16(dst, kreg) "v_mfma_f32_32x32x16_bf16 " VR(dst, 16) ", " VR(kreg, 4) ", " VR(R_Q, 4) ", " VR(R_NM, 16) "\n\t"
#define STEP(C, N, KO, VO, MID) \
  "s_waitcnt lgkmcnt(4)\n\t" \
  QK16(N, R_KA) \
  EX4(C, 0) \
  MID \
  "ds_read_b128 " VR(R_KA, 4) ", v" STR(R_KAD) " offset:" STR(KO) "\n\t" \
  PV(R_V3, N + 24) \
  VREAD(R_V3, VO) \
  EX3(C, 4) \
  QK16(N + 16, R_KB) \
  "ds_read_b128 " VR(R_KB, 4) ", v" STR(R_KAD) " offset:" STR(KO + 512) "\n\t" \
  EX4(C, 7) \
  "s_waitcnt lgkmcnt(3)\n\t" \
  PV(R_V0, C) \
  VREAD(R_V0, VO + 640) \
  EX4(C, 11) \
  "v_mfma_f32_32x32x4_2b_bf16 " VR(N, 32) ", " VR(R_KR, 2) ", " VR(R_QR, 2) ", " VR(N, 32) "\n\t" \
  "ds_read_b64 " VR(R_KR, 2) ", v" STR(R_KRAD) " offset:" STR(KO) "\n\t" \
  EX4(C, 15) EX4(C, 19) \
  PV(R_V1, C + 8) \
  VREAD(R_V1, VO + 1280) \
  EX4(C, 23) \
  PV(R_V2, C + 16) \
  VREAD(R_V2, VO + 1920) \
  EX4(C, 27) EX(C, 31)
#define KV_ISSUE(D0, D1, D2) \
  "v_add_u32 v" STR(R_VO0) ", %[st0], v" STR(R_VO0) "\n\tv_add_u32 v" STR(R_VO1) ", %[st1], v" STR(R_VO1) "\n\tv_add_u32 v" STR(R_VO2) ", %[st2], v" STR(R_VO2) "\n\t" \
  "s_mov_b32 m0, %[" D0 "]\n\ts_nop 0\n\tglobal_load_lds_dwordx4 v" STR(R_VO0) ", %[gb] sc1\n\t" \
  "s_mov_b32 m0, %[" D1 "]\n\ts_nop 0\n\tglobal_load_lds_dwordx4 v" STR(R_VO1) ", %[gb] sc1\n\t" \
  "s_mov_b32 m0, %[" D2 "]\n\ts_nop 0\n\tglobal_load_lds_dwordx4 v" STR(R_VO2) ", %[gb] sc1\n\t"
#define TOP_EVEN \
  "s_waitcnt vmcnt(1) lgkmcnt(0)\n\ts_barrier\n\t" KV_ISSUE("d10", "d11", "d12") \
  "s_mov_b32 m0, %[wd]\n\ts_add_u32 %[wd], %[wd], 1024\n\tglobal_load_lds_dwordx4 v" STR(R_WO0) ", %[wb]\n\t" \
  "s_mov_b32 m0, %[wd]\n\ts_add_u32 %[wd], %[wd], 1024\n\tglobal_load_lds_dwordx4 v" STR(R_WO1) ", %[wb]\n\t"
#define TOP_ODD \
  "s_waitcnt vmcnt(2) lgkmcnt(0)\n\ts_barrier\n\t" \
  "s_cmp_eq_u32 %[cnt], 1\n\ts_cbranch_scc1 2f\n\t" KV_ISSUE("d00", "d01", "d02") \
  "2:\n\ts_mov_b32 m0, %[wd]\n\ts_mov_b32 exec_hi, 0\n\tglobal_load_lds_dwordx4 v" STR(R_WO2) ", %[wb]\n\ts_mov_b32 exec_hi, -1\n\t" \
  "s_add_u32 %[wd], %[wd], 20480-2048\n\t" \
  "v_add_u32 v" STR(R_WO0) ", %[wst], v" STR(R_WO0) "\n\tv_add_u32 v" STR(R_WO1) ", %[wst], v" STR(R_WO1) "\n\tv_add_u32 v" STR(R_WO2) ", %[wst], v" STR(R_WO2) "\n\t"

constexpr int L2_PART = 147456;
DI void p2_body(const Ctx& c, int h, int qb, int cs, unsigned (&wo)[3], u32x4 (&wo4)[3], float& cs2) {
  const int l = c.l, w = c.w, r = l & 31, hh = l >> 5;
  const unsigned lds0 = c.lds0;
  const int e = h & 1, cb = 5 * (h >> 1) + 2 * e, q0 = qb * 256 + w * 32;
  unsigned vo[3], sstr[3], dst0[3], dst1[3];
#pragma unroll
  for (int i = 0; i < 3; ++i) {
    int id = w + 8 * i; if (id >= 22) id -= 2;
    const bool isk = id < 12;
    const int blk = id / 3, ch = id - 3 * blk, pv = id - 12, n = 64 * pv + l, kg = n / 20, dd = n - 20 * kg, d = e ? (dd + 4 >= 20 ? dd - 16 : dd + 4) : dd;
    vo[i] = isk ? (unsigned)(WS_QK + ((80 + cb + ch) * 2048 + 128 - 256 + 64 * blk + l) * 16) : (unsigned)(WS_V + ((6 - 32 + kg) * 640 + 20 * h + d) * 16);
    sstr[i] = isk ? 4096u : 32u * 10240u;
    dst0[i] = lds0 + (isk ? KRING + id * 1024 : VRING + pv * 1024);
    dst1[i] = dst0[i] + (isk ? 12288 : 10240);
  }
  unsigned kad = lds0 + KRING + (e + hh) * 1024 + r * 16, krad = lds0 + KRING + (e ? 0 : 2048) + l * 16 + 8 * e;
  unsigned vad = lds0 + VRING + hh * 320 + (r < 20 ? r : 0) * 16;
  if (w == 0) {
    const int sel = l >> 4, f0 = (sel & 2) ? 20 * h : 8 * cb, f1 = f0 + ((sel & 1) ? ((sel & 2) ? 19 : 23) : 0);
    const int ctk = ((sel & 2) ? 10 : 5) + (f1 >> 7), ctq = (8 * cb + ((l & 2) ? 23 : 0)) >> 7;
    poll(c.p.ws, WS_F1 + (ctk * 16 + (l & 15)) * 8, WS_F1 + (ctq * 16 + 2 * qb + (l & 1)) * 8, c.tag);
  }
  LDS_BAR();
  STAMP(6);
#pragma unroll
  for (int i = 0; i < 3; ++i) glds_d(c.p.ws, vo[i], dst1[i]);
#pragma unroll
  for (int i = 0; i < 3; ++i) { vo[i] += sstr[i]; glds_d(c.p.ws, vo[i], dst0[i]); }
  s16x8 q16 = __builtin_bit_cast(s16x8, ld128_d(c.rs, WS_QK + ((cb + e + hh) * 2048 + q0 + r) * 16));
  s16x4 qr = __builtin_bit_cast(s16x4, ld64_d(c.rs, WS_QK + ((e ? cb : cb + 2) * 2048 + q0 + r) * 16 + 8 * e));
  cs2 = colsum_step2(c.lds + L2_PART, l, w);
  asm volatile("" : "+v"(q16), "+v"(qr), "+v"(wo4[0]), "+v"(wo4[1]), "+v"(wo4[2]), "+v"(cs2));
  WAIT_BAR(0);
  STAMP(7);
  lptr k0 = (lptr)(size_t)kad + 12288 + 2 * 3072;
  f32x32 sa;
  {
    const f32x32 z = {};
    sa = __builtin_amdgcn_mfma_f32_32x32x4bf16_1k(*(const LAS s16x4*)((lptr)(size_t)krad + 12288 + 2 * 3072), qr, z, 0, 0, 0);
    f32x16 lo = __builtin_shufflevector(sa, sa, 0, 1, 2, 3, 4, 5, 6, 7, 8, 9, 10, 11, 12, 13, 14, 15);
    f32x16 hi = __builtin_shufflevector(sa, sa, 16, 17, 18, 19, 20, 21, 22, 23, 24, 25, 26, 27, 28, 29, 30, 31);
    lo = mfma32(ld128(k0), q16, lo); hi = mfma32(ld128(k0 + 512), q16, hi);
#pragma unroll
    for (int i = 0; i < 16; ++i) { sa[i] = lo[i]; sa[16 + i] = hi[i]; }
  }
  float m = sa[0];
#pragma unroll
  for (int i = 1; i < 32; ++i) m = fmaxf(m, sa[i]);
  { const auto t = __builtin_amdgcn_permlane32_swap(__float_as_uint(m), __float_as_uint(m), false, false); m = fmaxf(__uint_as_float(t[0]), __uint_as_float(t[1])); }
  f32x16 nm, o = {};
#pragma unroll
  for (int i = 0; i < 16; ++i) nm[i] = -m;
#pragma unroll
  for (int i = 0; i < 32; ++i) sa[i] -= m;
  f32x32 sb = {};
  s16x8 ka = ld128(k0 + 3072), kb = ld128(k0 + 3072 + 512);
  s16x4 kr = *(const LAS s16x4*)((lptr)(size_t)krad + 12288 + 3 * 3072);
  const unsigned pad = r == 20 ? ONES2 : 0u;
  const u32x4 pad4 = {pad, pad, pad, pad};
  lptr vb = (lptr)(size_t)vad + 10240 + 3 * 2560;
  u32x4 v0 = __builtin_bit_cast(u32x4, ld128(vb + 640)), v1 = __builtin_bit_cast(u32x4, ld128(vb + 1280)), v2 = __builtin_bit_cast(u32x4, ld128(vb + 1920)), v3 = pad4;
  if (r >= 20) { v0 = pad4; v1 = pad4; v2 = pad4; }
  unsigned idx = 0xDDDDDDDDu, cnt = 4, wd = lds0 + LDS_B + w * 2560;
  const u64 vmask = 0x000FFFFF000FFFFFull;
  STAMP(8);
  asm volatile(
      "s_nop 4\n"
      "1:\n\t"
      STEP(R_SA, R_SB, 0, 0, TOP_EVEN)
      STEP(R_SB, R_SA, 3072, 2560, "")
      STEP(R_SA, R_SB, 6144, 5120, "")
      STEP(R_SB, R_SA, 9216, 7680, "")
      STEP(R_SA, R_SB, 12288, 10240, TOP_ODD)
      STEP(R_SB, R_SA, 12288 + 3072, 10240 + 2560, "")
      STEP(R_SA, R_SB, 12288 + 6144, 10240 + 5120, "")
      STEP(R_SB, R_SA, 12288 + 9216, 10240 + 7680, "")
      "s_sub_u32 %[cnt], %[cnt], 1\n\t"
      "s_cmp_lg_u32 %[cnt], 0\n\t"
      "s_cbranch_scc1 1b\n\t"
      "s_waitcnt lgkmcnt(0)\n\t"
      PV(R_V3, R_SB + 24)
      "s_nop 15\n\t"
      : "+{v[32:63]}"(sa), "+{v[64:95]}"(sb), "+{v[96:111]}"(o), "+{v[134:137]}"(ka), "+{v[138:141]}"(kb), "+{v[142:143]}"(kr),
        "+{v[144:147]}"(v0), "+{v[148:151]}"(v1), "+{v[152:155]}"(v2), "+{v[156:159]}"(v3),
        "+{v164}"(vo[0]), "+{v165}"(vo[1]), "+{v166}"(vo[2]), "+{v167}"(wo[0]), "+{v168}"(wo[1]), "+{v169}"(wo[2]), [cnt] "+s"(cnt), [wd] "+s"(wd)
      : "{v[112:127]}"(nm), "{v[128:131]}"(q16), "{v[132:133]}"(qr), "{v160}"(idx), "{v161}"(kad), "{v162}"(krad), "{v163}"(vad),
        [vmask] "s"(vmask), [gb] "s"(c.p.ws), [st0] "s"(sstr[0]), [st1] "s"(sstr[1]), [st2] "s"(sstr[2]),
        [d00] "s"(dst0[0]), [d01] "s"(dst0[1]), [d02] "s"(dst0[2]), [d10] "s"(dst1[0]), [d11] "s"(dst1[1]), [d12] "s"(dst1[2]),
        [wb] "s"(c.p.wo + cs * 160), [wst] "s"(128 * 1280)
      : "memory", "scc");
  STAMP(9);
  float lsum;
  { const auto t = __builtin_amdgcn_permlane32_swap(__float_as_uint(o[8]), __float_as_uint(o[8]), false, false); lsum = __uint_as_float(t[1]); }
  float chk = 0.f;
#pragma unroll
  for (int i = 0; i < 12; ++i) chk += fabsf(o[i]);
  if (__any(!(chk < 1e37f) || !(lsum > 0.f))) {
    const unsigned ko = WS_QK + ((80 + cb + e + hh) * 2048 + r) * 16, kro = WS_QK + ((80 + cb + (e ? 0 : 2)) * 2048 + l) * 16 + 8 * e;
    const int d = r < 20 ? (e ? (r + 4 >= 20 ? r - 16 : r + 4) : r) : 0;
    const unsigned vg = WS_V + (hh * 640 + 20 * h + d) * 16;
    float tm = -INFINITY;
#pragma unroll 1
    for (int pass = 0; pass < 2; ++pass) {
#pragma unroll
      for (int i = 0; i < 16; ++i) { nm[i] = pass ? -tm : 0.f; o[i] = 0.f; }
#pragma unroll 1
      for (int b = 0; b < 32; ++b) {
        f32x16 lo = mfma32(__builtin_bit_cast(s16x8, ld128_d(c.rs, ko + b * 1024)), q16, nm), hi = mfma32(__builtin_bit_cast(s16x8, ld128_d(c.rs, ko + b * 1024 + 512)), q16, nm);
        f32x32 s;
#pragma unroll
        for (int i = 0; i < 16; ++i) { s[i] = lo[i]; s[16 + i] = hi[i]; }
        s = __builtin_amdgcn_mfma_f32_32x32x4bf16_1k(__builtin_bit_cast(s16x4, ld64_d(c.rs, kro + b * 1024)), qr, s, 0, 0, 0);
        if (pass == 0) {
#pragma unroll
          for (int i = 0; i < 32; ++i) tm = fmaxf(tm, s[i]);
        } else {
#pragma unroll
          for (int g = 0; g < 4; ++g) {
            f32x8 pr;
#pragma unroll
            for (int i = 0; i < 8; ++i) pr[i] = __builtin_amdgcn_exp2f(s[8 * g + i]);
            u32x4 vf = ld128_d(c.rs, vg + (8 * b + 2 * g) * 10240);
            if (r >= 20) vf = pad4;
            o = __builtin_amdgcn_smfmac_f32_32x32x32_bf16(__builtin_bit_cast(bf16x8, vf), __builtin_bit_cast(bf16x16, pr), o, 0xDDDD, 0, 0);
          }
        }
      }
      if (pass == 0) { const auto t = __builtin_amdgcn_permlane32_swap(__float_as_uint(tm), __float_as_uint(tm), false, false); tm = fmaxf(__uint_as_float(t[0]), __uint_as_float(t[1])); }
    }
    const auto t = __builtin_amdgcn_permlane32_swap(__float_as_uint(o[8]), __float_as_uint(o[8]), false, false); lsum = __uint_as_float(t[1]);
  }
  const float rl = __builtin_amdgcn_rcpf(lsum);
  unsigned pk[6];
#pragma unroll
  for (int i = 0; i < 6; ++i) pk[i] = cvtpk(o[2 * i] * rl, o[2 * i + 1] * rl);
  const auto s0 = __builtin_amdgcn_permlane32_swap(pk[0], pk[2], false, false);
  const auto s1 = __builtin_amdgcn_permlane32_swap(pk[1], pk[3], false, false);
  const int row = q0 + r;
  const unsigned og = WS_O + (row >> 6) * 81920 + (row & 63) * 16;
  st128_d(c.rs, og + (cb + e + hh) * 1024, u32x4{s0[0], s1[0], s0[1], s1[1]});
  if (hh == 0) st64_d(c.rs, og + (e ? cb : cb + 2) * 1024 + 8 * e, u32x2{pk[4], pk[5]});
  STAMP(10);
}

template <int NT> struct F80 { s16x8 a0[2], a1[2], b[2][NT]; };
template <bool LAST, int NT>
DI void g80_loop(const Ctx& c, int rt, int cs, f32x4 (&acc)[2][3], const u32x4 (&wo4)[3]) {
  constexpr int NCH = LAST ? 12 : 5;
  const int l = c.l, w = c.w, wm = w & 1, wk = (w >> 1) & 1, wn = w >> 2;
  lptr lds = c.lds;
  const char* ga = c.p.ws + (LAST ? WS_F : WS_O) + (size_t)rt * (NCH * 16384) + w * 2048;
  const unsigned voffA = l * 16;
  auto aslot = [&](int ch) { return (!LAST && ch == 4) ? 147456 : (ch & 3) * 16384; };
  auto issueA = [&](int ch) { const unsigned sa = c.lds0 + aslot(ch) + w * 2048; const char* a = ga + ch * 16384; glds_d(a, voffA, sa); glds_d(a + 1024, voffA, sa + 1024); };
  issueA(0); issueA(1); issueA(2); issueA(3);
  if (!LAST) issueA(4);
  const int g = l >> 4, q = (l & 15) >> 2, pp = l & 3;
  const unsigned aoff = (8 * wk + g) * 1024 + (32 * wm + (l & 15)) * 16;
  const unsigned boff = LDS_B + (64 * wk + 16 * (g >> 1) + 4 * (g & 1) + q) * 160 + wn * 96 + pp * 8;
  auto rd = [&](F80<NT>& f, int ks, int ch) {
    lptr sa = lds + aslot(ch) + aoff + ks * 4096, sb = lds + (ch & 3) * 20480 + boff + ks * 5120;
    f.a0[ks] = ld128(sa); f.a1[ks] = ld128(sa + 256);
#pragma unroll
    for (int nt = 0; nt < NT; ++nt) f.b[ks][nt] = cat(ldtr(sb + nt * 32), ldtr(sb + 1280 + nt * 32));
  };
  auto mm = [&](const F80<NT>& f, int ks) {
#pragma unroll
    for (int nt = 0; nt < NT; ++nt) { acc[0][nt] = mfma16x(f.b[ks][nt], f.a0[ks], acc[0][nt]); acc[1][nt] = mfma16x(f.b[ks][nt], f.a1[ks], acc[1][nt]); }
  };
  auto body = [&](F80<NT>& nx, const F80<NT>& cu, int ch) {
    XD(if (LAST && ch + 3 < NCH) { w2t_issue(c, cs, ch + 3); issueA(ch + 3); })
    SB(); XR(rd(nx, 0, ch);) SB(); mm(cu, 0); SB(); XR(rd(nx, 1, ch);) SB(); mm(cu, 1); SB();
  };
  F80<NT> fa, fb;
  if constexpr (!LAST) {
    WAIT_BAR(8);
    STAMP(13);
    rd(fa, 0, 0); rd(fa, 1, 0);
    WAIT_BAR(6);
    lptr b4 = lds + LDS_B + w * 2560 + l * 16;
    *(LAS u32x4*)b4 = wo4[0]; *(LAS u32x4*)(b4 + 1024) = wo4[1]; if (l < 32) *(LAS u32x4*)(b4 + 2048) = wo4[2];
    body(fb, fa, 1);
    WAIT_BAR(4); body(fa, fb, 2);
    WAIT_BAR(2); body(fb, fa, 3);
    WAIT_BAR(0); body(fa, fb, 4);
    mm(fa, 0); mm(fa, 1);
  } else {
    WAIT_BAR(6);
    STAMP(23);
    rd(fa, 0, 0); rd(fa, 1, 0);
    WAIT_BAR(4); body(fb, fa, 1);
    WAIT_BAR(7); body(fa, fb, 2);
#pragma unroll 1
    for (int ch = 3; ch < NCH - 1; ch += 2) { WAIT3_BAR(ch, 10, 10, 5); body(fb, fa, ch); WAIT3_BAR(ch + 1, 10, 10, 5); body(fa, fb, ch + 1); }
    WAIT_BAR(0); body(fb, fa, NCH - 1);
    mm(fb, 0); mm(fb, 1);
  }
}
DI void g80_reduce(const Ctx& c, const f32x4 (&acc)[2][3], u32x4 (&res)[2]) {
  const int l = c.l, w = c.w, wm = w & 1, wk = (w >> 1) & 1, wn = w >> 2, g = l >> 4;
  lptr pw = c.lds + wk * 21504 + (32 * wm + (l & 15)) * 336 + (wn * 48 + 4 * g) * 4;
#pragma unroll
  for (int mt = 0; mt < 2; ++mt) {
    *(LAS f32x4*)(pw + mt * 16 * 336) = acc[mt][0]; *(LAS f32x4*)(pw + mt * 16 * 336 + 64) = acc[mt][1];
  }
  if (wn == 0) { *(LAS f32x4*)(pw + 128) = acc[0][2]; *(LAS f32x4*)(pw + 16 * 336 + 128) = acc[1][2]; }
  LDS_BAR();
#pragma unroll
  for (int rd = 0; rd < 2; ++rd) {
    if (rd == 0 || w < 2) {
      lptr pr = c.lds + l * 336 + (w + 8 * rd) * 32;
      const f32x4 u0 = *(LAS f32x4*)pr + *(LAS f32x4*)(pr + 21504), u1 = *(LAS f32x4*)(pr + 16) + *(LAS f32x4*)(pr + 21504 + 16);
      const u32x4 rs = res[rd];
      res[rd] = u32x4{cvtpk(u0[0] + bflo(rs[0]), u0[1] + bfhi(rs[0])), cvtpk(u0[2] + bflo(rs[1]), u0[3] + bfhi(rs[1])),
                      cvtpk(u1[0] + bflo(rs[2]), u1[1] + bfhi(rs[2])), cvtpk(u1[2] + bflo(rs[3]), u1[3] + bfhi(rs[3]))};
    }
  }
}

constexpr int L4_RP = 32768, L4_CP = L4_RP + 4096, L4_FIN = L4_CP + 6144;
struct F4 { s16x8 a0[2], a1[2], b[2][3]; };
DI void p4_body(const Ctx& c, int rt, int cs, const unsigned (&vo)[3]) {
  const int tid = c.tid, l = c.l, w = c.w, wm = w >> 2, wq = w & 3;
  lptr lds = c.lds;
  const char* ga = c.p.ws + WS_H + (size_t)rt * 81920 + w * 1024;
  const char* gb = c.p.w1 + cs * 384;
  const unsigned voffA = l * 16;
  auto issueA = [&](int ch) { glds_d(ga + ch * 8192, voffA, c.lds0 + (ch & 3) * 8192 + w * 1024); };
  glds_m(c.p.ws + WS_RS2 + (rt * 8 + w) * 512, voffA, c.lds0 + L4_RP + w * 512, 0xFFFFFFFFull);
  glds_m(c.p.ws + WS_CS2 + (w * 1536 + cs * 192) * 4, voffA, c.lds0 + L4_CP + w * 768, 0xFFFFFFFFFFFFull);
  issueA(0); issueA(1); issueA(2); issueA(3);
  const int g = l >> 4, q = (l & 15) >> 2, pp = l & 3;
  const unsigned aoff = g * 1024 + (32 * wm + (l & 15)) * 16;
  unsigned boff[3];
#pragma unroll
  for (int nt = 0; nt < 3; ++nt) boff[nt] = LDS_B + (16 * (g >> 1) + 4 * (g & 1) + q) * 384 + (((3 * wq + nt) ^ (2 * (g & 1) + (q >> 1))) * 32) + pp * 8;
  f32x4 acc[2][3] = {};
  auto rd = [&](F4& f, int i, int ch) {
    lptr la = lds + (ch & 3) * 8192 + aoff + i * 4096, lb = lds + (ch & 3) * 24576 + i * 12288;
    f.a0[i] = ld128(la); f.a1[i] = ld128(la + 256);
#pragma unroll
    for (int nt = 0; nt < 3; ++nt) f.b[i][nt] = cat(ldtr(lb + boff[nt]), ldtr(lb + boff[nt] + 3072));
  };
  auto mm = [&](const F4& f, int i) {
#pragma unroll
    for (int nt = 0; nt < 3; ++nt) { acc[0][nt] = mfma16x(f.b[i][nt], f.a0[i], acc[0][nt]); acc[1][nt] = mfma16x(f.b[i][nt], f.a1[i], acc[1][nt]); }
  };
  auto body = [&](F4& nx, const F4& cu, int ch) {
    XD(if (ch + 3 < 10) { w192_issue(c, gb, vo, ch + 3); issueA(ch + 3); })
    SB(); XR(rd(nx, 0, ch);) SB(); mm(cu, 0); SB(); XR(rd(nx, 1, ch);) SB(); mm(cu, 1); SB();
  };
  F4 fa, fb;
  WAIT_BAR(3);
  STAMP(18);
  rd(fa, 0, 0); rd(fa, 1, 0);
  LAS float* ra = (LAS float*)(lds + L4_FIN); LAS float* rb = ra + 64; LAS float* csum = ra + 128;
  if (tid < 64) {
    f32x2 t = *(LAS f32x2*)(lds + L4_RP + tid * 8);
#pragma unroll
    for (int i = 1; i < 8; ++i) t += *(LAS f32x2*)(lds + L4_RP + i * 512 + tid * 8);
    const float mean = t[0] * (1.f / D), var = fmaxf(t[1] * (1.f / D) - mean * mean, 0.f);
    const float a = __builtin_amdgcn_rsqf(var + LN_EPS);
    ra[tid] = a; rb[tid] = -mean * a;
  } else if (tid < 256) {
    const int n = tid - 64; float t = *(LAS float*)(lds + L4_CP + n * 4);
#pragma unroll
    for (int i = 1; i < 8; ++i) t += *(LAS float*)(lds + L4_CP + i * 768 + n * 4);
    csum[n] = t;
  }
  WAIT_BAR(2); body(fb, fa, 1);
  WAIT_BAR(5); body(fa, fb, 2);
#pragma unroll 1
  for (int ch = 3; ch < 9; ch += 2) { WAIT3_BAR(ch, 8, 8, 4); body(fb, fa, ch); WAIT3_BAR(ch + 1, 8, 8, 4); body(fa, fb, ch + 1); }
  WAIT_BAR(0); body(fb, fa, 9);
  float a[2], b[2];
#pragma unroll
  for (int m = 0; m < 2; ++m) { a[m] = ra[wm * 32 + 16 * m + (l & 15)]; b[m] = rb[wm * 32 + 16 * m + (l & 15)]; }
  f32x4 c4[3];
#pragma unroll
  for (int nt = 0; nt < 3; ++nt) c4[nt] = *(LAS f32x4*)(csum + wq * 48 + nt * 16 + 4 * g);
  LDS_BAR();
  if (w != 0) { for (int ch = 0; ch < 4; ++ch) w2t_issue(c, cs, ch); }
  mm(fb, 0); mm(fb, 1);
  STAMP(19);
#pragma unroll
  for (int nt = 0; nt < 3; ++nt) {
    unsigned pk[2][2];
#pragma unroll
    for (int m = 0; m < 2; ++m) {
      float y[4];
#pragma unroll
      for (int i = 0; i < 4; ++i) y[i] = fmaxf(fmaf(acc[m][nt][i], a[m], b[m] * c4[nt][i]), 0.f);
      pk[m][0] = cvtpk(y[0], y[1]); pk[m][1] = cvtpk(y[2], y[3]);
    }
    const auto s0 = __builtin_amdgcn_permlane16_swap(pk[0][0], pk[1][0], false, false);
    const auto s1 = __builtin_amdgcn_permlane16_swap(pk[0][1], pk[1][1], false, false);
    st128_d(c.rs, (unsigned)(WS_F + rt * 196608 + (cs * 24 + wq * 6 + nt * 2 + (g >> 1)) * 1024 + (wm * 32 + (g & 1) * 16 + (l & 15)) * 16), u32x4{s0[0], s1[0], s0[1], s1[1]});
  }
  STAMP(20);
}

__global__ void __launch_bounds__(512, 2) block_kernel(Params p) {
  extern __shared__ __attribute__((aligned(16))) char smem[];
  Ctx c;
  c.p = p; c.lds = (lptr)smem; c.lds0 = (unsigned)(size_t)c.lds; c.tid = threadIdx.x; c.l = c.tid & 63; c.w = rfl(c.tid >> 6);
  c.xcd = blockIdx.x & 7; c.j = blockIdx.x >> 3;
  c.st_lo = c.st_hi = 0;
  STAMP(0);
  c.rs = __builtin_amdgcn_make_buffer_rsrc(p.ws, 0, (unsigned)WS_END, 0x00020000);
  const int w = c.w, l = c.l, tid = c.tid, b = blockIdx.x, rt = 4 * c.xcd + (c.j >> 3), cs = c.j & 7;
  unsigned wo[3]; u32x4 wo4[3]; float cs2;
  {
    P1 t;
    t.panel = 4 * (c.xcd >> 1) + (c.j & 3); t.ct = 8 * (c.xcd & 1) + (c.j >> 2);
    u32x4 xa, xb = {}, wa = {}, wb = {}; u64 ep;
    const int rg = b / 30, cg = b - 30 * rg;
    {
      const char* wsrc = p.wqkv + (size_t)(b < 240 ? rg * 80 * 3840 + cg * 128 : 0);
      const int m1 = (b - 240) >> 31, m2 = m1 & ((w - 2) >> 31);
      asm volatile("global_load_dwordx4 %0, %5, %7\n\tglobal_load_dwordx2 %4, %12, %13 sc1\n\t"
                   "s_mov_b64 exec, 0xffff\n\tglobal_load_dwordx4 %1, %5, %7 offset:1024\n\t"
                   "s_mov_b32 exec_lo, %10\n\ts_mov_b32 exec_hi, %10\n\ts_cbranch_execz 1f\n\tglobal_load_dwordx4 %2, %6, %8\n\t"
                   "s_mov_b32 exec_lo, %11\n\ts_mov_b32 exec_hi, %11\n\ts_cbranch_execz 1f\n\tglobal_load_dwordx4 %3, %6, %9\n1:\n\ts_mov_b64 exec, -1"
                   : "=&v"(xa), "+v"(xb), "+v"(wa), "+v"(wb), "=&v"(ep)
                   : "v"(l * 16), "v"((tid >> 3) * 3840 + (tid & 7) * 16), "s"(p.x + (size_t)(8 * b + w) * 1280), "s"(wsrc), "s"(wsrc + 64 * 3840), "s"(m1), "s"(m2), "v"(0), "s"(p.ws) : "memory");
    }
    {
      const char* pc; unsigned len;
      asm volatile("s_getpc_b64 %0\n.Lblk_pc:\n\ts_mov_b32 %1, .Lblk_end-.Lblk_pc" : "=s"(pc), "=s"(len));
      const unsigned off = (c.j * 64 + l) * 128;
      if (w == 7 && off < len) asm volatile("s_mov_b32 m0, %2\n\ts_nop 0\n\tglobal_load_lds_dword %0, %1" :: "v"(off & ~3u), "s"(pc), "s"(c.lds0 + L1_TOUCH) : "memory");
    }
    t.voffA = (8 * w + (l >> 3)) * 1280 + (((l & 7) ^ ((4 * w + (l >> 4)) & 7)) << 4);
    t.voffB = (4 * w + (l >> 4)) * 3840 + (((l & 15) ^ (((l >> 4) & 3) << 2)) << 4);
    t.ga = p.x + (size_t)t.panel * 128 * 1280; t.gb = p.wqkv + t.ct * 256;
    if (t.ct < 15) { p1_issue(c, t, 0); p1_issue(c, t, 1); p1_issue(c, t, 2); p1_issue(c, t, 3); }
    STAMP(1);
    asm volatile("s_cmp_lt_u32 %5, 15\n\ts_cbranch_scc1 1f\n\ts_waitcnt vmcnt(0)\n1:\n\ts_waitcnt vmcnt(16)" : "+v"(xa), "+v"(xb), "+v"(wa), "+v"(wb), "+v"(ep) : "s"(t.ct) : "memory", "scc");
    c.tag = ep + 1;
    {
      const float sum = wave_sum(fsum8(__builtin_bit_cast(s16x8, xb), fsum8(__builtin_bit_cast(s16x8, xa), 0.f)));
      const float sq = wave_sum(fsq8(__builtin_bit_cast(s16x8, xb), fsq8(__builtin_bit_cast(s16x8, xa), 0.f)));
      const float mean = sum * (1.f / D), var = fmaxf(sq * (1.f / D) - mean * mean, 0.f), a = __builtin_amdgcn_rsqf(var + LN_EPS);
      if (l == 0) st128_d(c.rs, WS_RS1 + (8 * b + w) * 16, u32x4{__float_as_uint(a), (unsigned)c.tag, __float_as_uint(-mean * a), (unsigned)c.tag});
      colsum_step1(c.lds + L1_PART, l, w, wa, wb);
    }
    WAIT_BAR(13);
    STAMP(2);
    {
      const float v = colsum_step2(c.lds + L1_PART, l, w);
      if (l < 8 && b < 240) st64_d(c.rs, WS_CS1 + (rg * 1920 + cg * 64 + 8 * w + l) * 8, u32x2{__float_as_uint(v), (unsigned)c.tag});
    }
    f32x16 acc[2] = {};
    if (t.ct < 10) p1_loop<false>(c, t, acc); else if (t.ct < 15) p1_loop<true>(c, t, acc);
    const int rg1 = b / 24, cg1 = b - 24 * rg1, k2 = 6 * b + tid / 80, c2 = tid % 80;
    u32x4 w1a = {}, w1b = {}, w2r = {};
    if (b < 192) {
      const char* w1s = p.w1 + (size_t)(rg1 * 80 + (tid >> 3)) * 3072 + cg1 * 128 + (tid & 7) * 16;
      w1a = *(const GAS u32x4*)w1s; if (w < 2) w1b = *(const GAS u32x4*)(w1s + 64 * 3072);
    }
    if (tid < 480) w2r = *(const GAS u32x4*)(p.w2 + (size_t)k2 * 1280 + c2 * 16);
    w80_offsets(l, w, wo);
#pragma unroll
    for (int i = 0; i < 3; ++i) wo4[i] = *(const GAS u32x4*)(p.wo + cs * 160 + 512 * 1280 + ((i < 2 || l < 32) ? wo[i] : wo[0]));
    LDS_BAR();
    STAMP(3);
    if (t.ct < 10) p1_epilogue<false>(c, t, acc); else if (t.ct < 15) p1_epilogue<true>(c, t, acc);
    if (tid < 480) {
      const int k7 = k2 & 127, rho = (k7 & ~12) | ((k7 & 4) << 1) | ((k7 & 8) >> 1), sl = c2 / 10;
      st128_d(c.rs, WS_W2T + sl * 245760 + (k2 >> 7) * 20480 + rho * 160 + (c2 - 10 * sl) * 16, w2r);
    }
    STAMP(4);
    WAIT_BAR(0);
    if (tid == 0) stflag(p.ws, WS_F1 + (t.ct * 16 + t.panel) * 8, c.tag);
    STAMP(5);
    colsum_step1(c.lds + L2_PART, l, w, w1a, w1b);
  }
  {
    const int h = 4 * c.xcd + (c.j >> 3), qb = c.j & 7;
    p2_body(c, h, qb, cs, wo, wo4, cs2);
    if (l < 8 && b < 192) __builtin_amdgcn_raw_buffer_store_b32(__float_as_uint(cs2), c.rs, WS_CS2 + ((b / 24) * 1536 + (b % 24) * 64 + 8 * w + l) * 4, 0, 16);
    WAIT_BAR(0);
    if (tid == 0) stflag(p.ws, WS_F2 + (qb * 32 + h) * 8, c.tag);
    STAMP(11);
  }
  u32x4 res[2] = {};
  unsigned vo1[3];
  {
    const char* xg = p.x + (size_t)(rt * 64 + l) * 1280 + cs * 160 + w * 16;
    res[0] = *(const GAS u32x4*)xg; if (w < 2) res[1] = *(const GAS u32x4*)(xg + 128);
    if (w == 0) poll(p.ws, WS_F2 + (c.xcd * 32 + (l & 31)) * 8, c.tag);
    asm volatile("" : "+v"(res[0]), "+v"(res[1]));
    WAIT_BAR(0);
    STAMP(12);
    f32x4 acc[2][3] = {};
    if (w < 4) g80_loop<false, 3>(c, rt, cs, acc, wo4); else g80_loop<false, 2>(c, rt, cs, acc, wo4);
    LDS_BAR();
    STAMP(14);
    g80_reduce(c, acc, res);
    const unsigned ho = WS_H + rt * 81920 + (cs * 10 + w) * 1024 + l * 16;
    st128_d(c.rs, ho, res[0]); if (w < 2) st128_d(c.rs, ho + 8192, res[1]);
    LAS f32x2* sp = (LAS f32x2*)(c.lds + 45056);
    sp[w * 64 + l] = f32x2{fsum8(__builtin_bit_cast(s16x8, res[1]), fsum8(__builtin_bit_cast(s16x8, res[0]), 0.f)), fsq8(__builtin_bit_cast(s16x8, res[1]), fsq8(__builtin_bit_cast(s16x8, res[0]), 0.f))};
    w192_offsets(l, w, vo1);
    LDS_BAR();
    STAMP(15);
    if (w != 0) { for (int ch = 0; ch < 4; ++ch) w192_issue(c, p.w1 + cs * 384, vo1, ch); WAIT_BAR(12); }
    else {
      f32x2 t = sp[l];
#pragma unroll
      for (int i = 1; i < 8; ++i) t += sp[i * 64 + l];
      st64_d(c.rs, WS_RS2 + (rt * 8 + cs) * 512 + l * 8, __builtin_bit_cast(u32x2, t));
      WAIT_BAR(0);
    }
    if (tid == 0) stflag(p.ws, WS_F3 + rt * 128 + cs * 8, c.tag);
    STAMP(16);
  }
  {
    if (w == 0) { poll(p.ws, WS_F3 + rt * 128 + (l & 7) * 8, c.tag); for (int ch = 0; ch < 4; ++ch) w192_issue(c, p.w1 + cs * 384, vo1, ch); }
    if (w == 1) {
      poll(p.ws, WS_F2 + l * 8, WS_F2 + 512 + l * 8, c.tag); poll(p.ws, WS_F2 + 1024 + l * 8, WS_F2 + 1536 + l * 8, c.tag);
      if (b == 0 && l == 0) stflag(p.ws, WS_EPOCH, c.tag);
    }
    LDS_BAR();
    STAMP(17);
    p4_body(c, rt, cs, vo1);
    WAIT_BAR(0);
    if (tid == 0) stflag(p.ws, WS_F4 + rt * 128 + cs * 8, c.tag);
    STAMP(21);
    if (w == 0) { poll(p.ws, WS_F4 + rt * 128 + (l & 7) * 8, c.tag); for (int ch = 0; ch < 4; ++ch) w2t_issue(c, cs, ch); }
    LDS_BAR();
    STAMP(22);
  }
  {
    f32x4 acc[2][3] = {};
    if (w < 4) g80_loop<true, 3>(c, rt, cs, acc, wo4); else g80_loop<true, 2>(c, rt, cs, acc, wo4);
    LDS_BAR();
    STAMP(24);
    g80_reduce(c, acc, res);
    char* og = p.out + (size_t)(rt * 64 + l) * 1280 + cs * 160 + w * 16;
    *(GAS u32x4*)og = res[0]; if (w < 2) *(GAS u32x4*)(og + 128) = res[1];
    STAMP(25);
  }
  asm volatile(".Lblk_end:");
}

extern "C" void kernel_launch(void* const* d_in, const int* in_sizes, int n_in, void* d_out, int out_size, void* d_ws, size_t ws_size, hipStream_t stream) {
  Params p;
  p.x = (const char*)d_in[0]; p.wqkv = (const char*)d_in[1]; p.wo = (const char*)d_in[2]; p.w1 = (const char*)d_in[4]; p.w2 = (const char*)d_in[5];
  p.out = (char*)d_out; p.ws = (char*)d_ws;
  (void)hipFuncSetAttribute((const void*)block_kernel, hipFuncAttributeMaxDynamicSharedMemorySize, LDS_BYTES);
  block_kernel<<<256, 512, LDS_BYTES, stream>>>(p);
}
```

```cpp
#include <hip/hip_runtime.h>
#include <hip/hip_bf16.h>

#define DI __device__ __forceinline__
#define LAS __attribute__((address_space(3)))
#define GAS __attribute__((address_space(1)))
typedef unsigned long long u64;
typedef __attribute__((ext_vector_type(8))) short s16x8;
typedef __attribute__((ext_vector_type(4))) short s16x4;
typedef __attribute__((ext_vector_type(8))) __bf16 bf16x8;
typedef __attribute__((ext_vector_type(16))) __bf16 bf16x16;
typedef __attribute__((ext_vector_type(2))) __bf16 bf16x2;
typedef __attribute__((ext_vector_type(2))) float f32x2;
typedef __attribute__((ext_vector_type(4))) float f32x4;
typedef __attribute__((ext_vector_type(8))) float f32x8;
typedef __attribute__((ext_vector_type(16))) float f32x16;
typedef __attribute__((ext_vector_type(32))) float f32x32;
typedef __attribute__((ext_vector_type(4))) unsigned u32x4;
typedef __attribute__((ext_vector_type(2))) unsigned u32x2;
typedef LAS char* lptr;
typedef __amdgpu_buffer_rsrc_t rsrc_t;

#define V_BY_HEAD
constexpr int S = 2048, D = 640, FF = 1536;
constexpr float LN_EPS = 1e-5f;
constexpr float QSCALE = 0.22360679774997896f * 1.4426950408889634f;

constexpr unsigned WS_EPOCH = 0, WS_F0 = 4096, WS_F1 = 6144, WS_FP = 8192;
constexpr unsigned WS_RS1 = 65536;
constexpr unsigned WS_CS1 = WS_RS1 + 2048 * 16;
constexpr unsigned WS_CS2 = WS_CS1 + 8 * 1920 * 8;
constexpr unsigned WS_QK = WS_CS2 + 8 * 1536 * 4;
constexpr unsigned WS_V = WS_QK + 160 * 2048 * 16;
constexpr unsigned WS_RS2 = WS_V + 256 * 640 * 16;
constexpr unsigned WS_O = WS_RS2 + 32 * 8 * 512;
constexpr unsigned WS_H = WS_O + S * D * 2;
constexpr unsigned WS_F = WS_H + S * D * 2;
constexpr unsigned WS_END = WS_RS2 + 1024 * 12288;
static_assert(WS_F + S * FF * 2 <= WS_END && WS_END <= 384 * 65536, "");
constexpr int LDS_BYTES = 163840;
constexpr unsigned SPIN_CAP = 1u << 18;
constexpr unsigned POISON = 0xFFFFFFFFu;
constexpr int L1_CG = 131072, L1_RG = L1_CG + 8192, L1_FIN = L1_RG + 2048;
constexpr int KRING = 0, VRING = 24576, L_W1B = 47104, L_WO = 57344;
constexpr int L4_A = 122880, L_EX = 98304;
constexpr int L_CNT0 = 159744, L_CNT1 = L_CNT0 + 4, L_CNTP = L_CNT0 + 8, L_CSUM = L_CNT0 + 256, L_RA = L_CSUM + 768, L_TOUCH = L_RA + 512, L_PART = L_TOUCH + 256;

#define STAMP(k)
#define STAMPV(k)
struct Params { const char* x; const char* wqkv; const char* wo; const char* w1; const char* w2; char* out; char* ws; };
struct Ctx { Params p; lptr lds; unsigned lds0; rsrc_t rs; u64 tag; int l, w, b, panel, ct, h, qb, rt, cs; mutable unsigned st_lo, st_hi; };

DI unsigned rfl(unsigned v) { return __builtin_amdgcn_readfirstlane(v); }
DI unsigned cvtpk(float lo, float hi) { f32x2 v = {lo, hi}; return __builtin_bit_cast(unsigned, __builtin_convertvector(v, bf16x2)); }
DI float bflo(unsigned v) { return __uint_as_float(v << 16); }
DI float bfhi(unsigned v) { return __uint_as_float(v & 0xffff0000u); }
DI void glds(const char* sbase, unsigned voff, unsigned lds) {
  asm volatile("s_mov_b32 m0, %2\n\ts_nop 0\n\tglobal_load_lds_dwordx4 %0, %1" :: "v"(voff), "s"(sbase), "s"(lds) : "memory");
}
DI void glds_d(const char* sbase, unsigned voff, unsigned lds) {
  asm volatile("s_mov_b32 m0, %2\n\ts_nop 0\n\tglobal_load_lds_dwordx4 %0, %1 sc1" :: "v"(voff), "s"(sbase), "s"(lds) : "memory");
}
DI void glds_m(const char* sbase, unsigned voff, unsigned lds, u64 mask) {
  asm volatile("s_mov_b32 m0, %2\n\ts_mov_b64 exec, %3\n\tglobal_load_lds_dwordx4 %0, %1 sc1\n\ts_mov_b64 exec, -1" :: "v"(voff), "s"(sbase), "s"(lds), "s"(mask) : "memory");
}
DI void touch(const char* sbase, unsigned voff, unsigned lds) {
  asm volatile("s_mov_b32 m0, %2\n\ts_nop 0\n\tglobal_load_lds_dword %0, %1" :: "v"(voff), "s"(sbase), "s"(lds) : "memory");
}
template <int N> DI void wait_vm() { asm volatile("s_waitcnt vmcnt(%0)" :: "i"(N) : "memory"); }
template <int N> DI void wait_bar() { asm volatile("s_waitcnt vmcnt(%0) lgkmcnt(0)\n\ts_barrier" :: "i"(N) : "memory"); }
#define LDS_BAR() asm volatile("s_waitcnt lgkmcnt(0)\n\ts_barrier" ::: "memory")
#define DRAIN() asm volatile("s_waitcnt vmcnt(0)" ::: "memory")
#define STR_(x) #x
#define STR(x) STR_(x)
#define VR(b, n) "v[" STR(b) ":" STR(b) "+" STR(n) "-1]"
#define WL(n) "s_waitcnt lgkmcnt(" #n ")\n\t"
#define CL8(a, b, c, d, e, f, g, h) "v" #a, "v" #b, "v" #c, "v" #d, "v" #e, "v" #f, "v" #g, "v" #h
DI s16x8 ld128(lptr p) { return *(const LAS s16x8*)p; }
DI s16x4 ldtr(lptr p) { return __builtin_amdgcn_ds_read_tr16_b64_v4i16((LAS s16x4*)p); }
DI s16x8 ldtr2(lptr p, int d) { return __builtin_shufflevector(ldtr(p), ldtr(p + d), 0, 1, 2, 3, 4, 5, 6, 7); }
DI f32x16 mfma32(s16x8 a, s16x8 b, f32x16 c) { return __builtin_amdgcn_mfma_f32_32x32x16_bf16(__builtin_bit_cast(bf16x8, a), __builtin_bit_cast(bf16x8, b), c, 0, 0, 0); }
DI f32x4 mfma16(s16x8 a, s16x8 b, f32x4 c) { return __builtin_amdgcn_mfma_f32_16x16x32_bf16(__builtin_bit_cast(bf16x8, a), __builtin_bit_cast(bf16x8, b), c, 0, 0, 0); }
DI float dot2(unsigned a, unsigned b, float c) { return __builtin_amdgcn_fdot2_f32_bf16(__builtin_bit_cast(bf16x2, a), __builtin_bit_cast(bf16x2, b), c, false); }
constexpr unsigned ONES2 = 0x3f803f80u;
DI void st128_d(rsrc_t rs, unsigned off, u32x4 v) { __builtin_amdgcn_raw_buffer_store_b128(v, rs, off, 0, 16); }
DI void st_poison(rsrc_t rs, unsigned off, unsigned soff) {
  asm volatile("buffer_store_dwordx4 %0, %1, %2, %3 offen sc1\n\ts_nop 1" :: "v"(u32x4{POISON, POISON, POISON, POISON}), "v"(off), "s"(rs), "s"(soff) : "memory");
}
DI void st64_d(rsrc_t rs, unsigned off, u32x2 v) { __builtin_amdgcn_raw_buffer_store_b64(v, rs, off, 0, 16); }
DI void st32_d(rsrc_t rs, unsigned off, unsigned v) { __builtin_amdgcn_raw_buffer_store_b32(v, rs, off, 0, 16); }
DI u32x4 ld128_d(rsrc_t rs, unsigned off) { return __builtin_amdgcn_raw_buffer_load_b128(rs, off, 0, 16); }
DI u32x2 ld64_d(rsrc_t rs, unsigned off) { return __builtin_amdgcn_raw_buffer_load_b64(rs, off, 0, 16); }
DI unsigned ld32_d(rsrc_t rs, unsigned off) { return __builtin_amdgcn_raw_buffer_load_b32(rs, off, 0, 16); }
DI u64 ldflag(const char* ws, unsigned off) { return __hip_atomic_load((GAS u64*)(ws + off), __ATOMIC_RELAXED, __HIP_MEMORY_SCOPE_AGENT); }
DI void stflag(char* ws, unsigned off, u64 v) { __hip_atomic_store((GAS u64*)(ws + off), v, __ATOMIC_RELAXED, __HIP_MEMORY_SCOPE_AGENT); }
DI void poll256(const char* ws, unsigned off, u64 tag) {
#pragma unroll 1
  for (unsigned n = 0; n < SPIN_CAP; ++n) {
    const u64 a = ldflag(ws, off), b = ldflag(ws, off + 512), c = ldflag(ws, off + 1024), d = ldflag(ws, off + 1536);
    if (__all(a == tag && b == tag && c == tag && d == tag)) break;
    __builtin_amdgcn_s_sleep(4);
  }
}
template <int YOUNGER> DI void publish(const Ctx& c, int cnt_off, unsigned n, unsigned flag_off) {
  wait_vm<YOUNGER>();
  if (c.l == 0) {
    const unsigned old = __hip_atomic_fetch_add((LAS unsigned*)(c.lds + cnt_off), 1u, __ATOMIC_RELAXED, __HIP_MEMORY_SCOPE_WORKGROUP);
    if (old == n - 1) stflag(c.p.ws, flag_off, c.tag);
  }
}
template <int CTRL> DI float dpp_add(float v) { return v + __int_as_float(__builtin_amdgcn_update_dpp(0, __float_as_int(v), CTRL, 0xf, 0xf, true)); }
DI float swap16_add(float a, float b) { const auto t = __builtin_amdgcn_permlane16_swap(__float_as_uint(a), __float_as_uint(b), false, false); return __uint_as_float(t[0]) + __uint_as_float(t[1]); }
DI float swap32_add(float a, float b) { const auto t = __builtin_amdgcn_permlane32_swap(__float_as_uint(a), __float_as_uint(b), false, false); return __uint_as_float(t[0]) + __uint_as_float(t[1]); }
DI float swap32_max(float a) { const auto t = __builtin_amdgcn_permlane32_swap(__float_as_uint(a), __float_as_uint(a), false, false); return fmaxf(__uint_as_float(t[0]), __uint_as_float(t[1])); }
DI float upper_half(float a) { const auto t = __builtin_amdgcn_permlane32_swap(__float_as_uint(a), __float_as_uint(a), false, false); return __uint_as_float(t[1]); }
DI u32x4 swap32_pair(unsigned a0, unsigned a1, unsigned b0, unsigned b1) {
  const auto s0 = __builtin_amdgcn_permlane32_swap(a0, b0, false, false), s1 = __builtin_amdgcn_permlane32_swap(a1, b1, false, false);
  return u32x4{s0[0], s1[0], s0[1], s1[1]};
}
DI u32x4 swap16_pair(unsigned a0, unsigned a1, unsigned b0, unsigned b1) {
  const auto s0 = __builtin_amdgcn_permlane16_swap(a0, b0, false, false), s1 = __builtin_amdgcn_permlane16_swap(a1, b1, false, false);
  return u32x4{s0[0], s1[0], s0[1], s1[1]};
}
DI void colsum_load(const char* blk, int stride, int l, int w4, u32x4 (&m)[3]) {
  const char* src = blk + (unsigned)((8 * w4 + (l >> 3)) * stride + (l & 7) * 16);
  m[0] = *(const GAS u32x4*)src; m[1] = *(const GAS u32x4*)(src + 32 * stride); m[2] = u32x4{};
  if (w4 < 2) m[2] = *(const GAS u32x4*)(src + 64 * stride);
}
DI void colsum_step1(lptr part, int l, int w4, const u32x4 (&m)[3]) {
  float v[8], u[4], t[2];
#pragma unroll
  for (int i = 0; i < 4; ++i) { v[2 * i] = bflo(m[0][i]) + bflo(m[1][i]) + bflo(m[2][i]); v[2 * i + 1] = bfhi(m[0][i]) + bfhi(m[1][i]) + bfhi(m[2][i]); }
#pragma unroll
  for (int i = 0; i < 4; ++i) u[i] = swap32_add(v[i], v[i + 4]);
#pragma unroll
  for (int i = 0; i < 2; ++i) t[i] = dpp_add<0x128>(swap16_add(u[i], u[i + 2]));
  *(LAS f32x2*)(part + (w4 * 64 + (l & 7) * 8 + 4 * (l >> 5) + 2 * ((l >> 4) & 1)) * 4) = f32x2{t[0], t[1]};
}
DI float colsum_step2(lptr part, int l, int w4) {
  float v = *(LAS float*)(part + ((l >> 4) * 64 + 16 * w4 + (l & 15)) * 4);
  v = swap16_add(v, v); return swap32_add(v, v);
}
DI unsigned np(unsigned v) { return v < POISON - 1 ? v : POISON - 1; }
DI u32x4 np(u32x4 v) { return u32x4{np(v[0]), np(v[1]), np(v[2]), np(v[3])}; }
DI unsigned umax(unsigned a, unsigned b) { return a > b ? a : b; }
DI unsigned umin(unsigned a, unsigned b) { return a < b ? a : b; }
DI unsigned umax4(u32x4 v) { return umax(umax(v[0], v[1]), umax(v[2], v[3])); }
#define FENCE() asm volatile("" ::: "memory")
DI f32x16 lo16(const f32x32& s) { return __builtin_shufflevector(s, s, 0, 1, 2, 3, 4, 5, 6, 7, 8, 9, 10, 11, 12, 13, 14, 15); }
DI f32x16 hi16(const f32x32& s) { return __builtin_shufflevector(s, s, 16, 17, 18, 19, 20, 21, 22, 23, 24, 25, 26, 27, 28, 29, 30, 31); }
DI int swap23(int k) { return (k & ~12) | ((k & 4) << 1) | ((k & 8) >> 1); }

DI void row_stats(const Ctx& c, const u32x4 (&xv)[3], int xrow) {
  const int r = c.l & 31;
  float s1 = 0.f, s2 = 0.f;
#pragma unroll
  for (int i = 0; i < 3; ++i)
#pragma unroll
    for (int k = 0; k < 4; ++k) { const unsigned v = (i < 2 || r < 16) ? xv[i][k] : 0u; s1 = dot2(v, ONES2, s1); s2 = dot2(v, v, s2); }
  s1 = dpp_add<0x128>(dpp_add<0x141>(dpp_add<0x4E>(dpp_add<0xB1>(s1)))); s2 = dpp_add<0x128>(dpp_add<0x141>(dpp_add<0x4E>(dpp_add<0xB1>(s2))));
  s1 = swap16_add(s1, s1); s2 = swap16_add(s2, s2);
  const float mean = s1 * (1.f / D), var = fmaxf(s2 * (1.f / D) - mean * mean, 0.f), a = __builtin_amdgcn_rsqf(var + LN_EPS);
  const unsigned tag = (unsigned)c.tag;
  if (r == 0) st128_d(c.rs, WS_RS1 + xrow * 16, u32x4{__float_as_uint(a), tag, __float_as_uint(-mean * a), tag});
}
struct O1 { unsigned base[2], sm, sg; };
DI O1 p1_places(const Ctx& c, int w) {
  const int wm = w >> 1, wn = w & 1, r = c.l & 31, hh = c.l >> 5; const bool vt = c.ct >= 10;
  O1 o;
#pragma unroll
  for (int n = 0; n < 2; ++n) {
    const int f = (c.ct - 10) * 128 + wn * 64 + (n ^ wm) * 32 + r, h = f / 20, d = f - 20 * h, dd = (h & 1) ? (d < 4 ? d + 16 : d - 4) : d;
    o.base[n] = vt ? WS_V + (h * 256 + c.panel * 16 + wm * 8 + hh) * 320 + dd * 16 : WS_QK + ((c.ct * 16 + wn * 8 + (n ^ wm) * 4 + hh) * 2048 + c.panel * 128 + wm * 64 + r) * 16;
  }
  o.sm = vt ? 4 * 320 : 512; o.sg = vt ? 2 * 320 : 2 * 32768;
  return o;
}
#define Q_X 96
#define Q_Y 128
#define QMK(S, k, m, n) "v_mfma_f32_32x32x16_bf16 " VR(32 + m * 32 + n * 16, 16) ", " VR(S + 16 + k * 8 + n * 4, 4) ", " VR(S + k * 8 + m * 4, 4) ", " VR(32 + m * 32 + n * 16, 16) "\n\t"
#define QMV(S, k, m, n) "v_mfma_f32_32x32x16_bf16 " VR(32 + m * 32 + n * 16, 16) ", " VR(S + k * 8 + m * 4, 4) ", " VR(S + 16 + k * 8 + n * 4, 4) ", " VR(32 + m * 32 + n * 16, 16) "\n\t"
#define QRA(S, k, m, ks) "ds_read_b128 " VR(S + k * 8 + m * 4, 4) ", v[160+" STR(ks) "] offset:" STR(m * 4096) "\n\t"
#define QRB(S, k, n, h, ks) "ds_read_b64_tr_b16 " VR(S + 16 + k * 8 + n * 4 + h * 2, 2) ", v[164+" STR(n) "] offset:" STR((ks) * 4096 + h * 1024) "\n\t"
#define QR0(S, k, ks) QRA(S, k, 0, ks) QRB(S, k, 0, 0, ks)
#define QR1(S, k, ks) QRB(S, k, 0, 1, ks) QRB(S, k, 1, 0, ks)
#define QR2(S, k, ks) QRB(S, k, 1, 1, ks) QRA(S, k, 1, ks)
#define QAD(i) "v_add_u32 v[160+" #i "], %[dl], v[160+" #i "]\n\t"
#define P1_LOOP(MM) \
  QR0(Q_Y, 0, 0) QR1(Q_Y, 0, 0) QR2(Q_Y, 0, 0) QR0(Q_Y, 1, 1) QR1(Q_Y, 1, 1) QR2(Q_Y, 1, 1) \
  "s_branch 2f\n" \
  "1:\n\t" \
  MM(Q_X, 0, 0, 1) QR0(Q_Y, 0, 0) MM(Q_X, 0, 1, 0) QR1(Q_Y, 0, 0) MM(Q_X, 0, 1, 1) QR2(Q_Y, 0, 0) \
  MM(Q_X, 1, 0, 0) QR0(Q_Y, 1, 1) MM(Q_X, 1, 0, 1) QR1(Q_Y, 1, 1) MM(Q_X, 1, 1, 0) QR2(Q_Y, 1, 1) MM(Q_X, 1, 1, 1) \
  "2:\n\t" \
  WL(9) MM(Q_Y, 0, 0, 0) QR0(Q_X, 0, 2) WL(9) MM(Q_Y, 0, 0, 1) QR1(Q_X, 0, 2) WL(10) MM(Q_Y, 0, 1, 0) QR2(Q_X, 0, 2) \
  MM(Q_Y, 0, 1, 1) QR0(Q_X, 1, 3) WL(11) MM(Q_Y, 1, 0, 0) QR1(Q_X, 1, 3) WL(11) MM(Q_Y, 1, 0, 1) QR2(Q_X, 1, 3) \
  WL(12) MM(Q_Y, 1, 1, 0) "s_add_u32 %[st], %[st], 1\n\ts_and_b32 %[dl], %[st], 3\n\ts_cselect_b32 %[dl], 32768, %[wr]\n\t" \
  MM(Q_Y, 1, 1, 1) QAD(0) QAD(1) \
  WL(9) MM(Q_X, 0, 0, 0) QAD(2) QAD(3) QAD(4) QAD(5) \
  "s_cmp_eq_u32 %[st], 12\n\ts_cbranch_scc1 3f\n\t" \
  WL(0) "s_barrier\n\ts_branch 1b\n" \
  "3:\n\t" WL(0) \
  MM(Q_X, 0, 0, 1) MM(Q_X, 0, 1, 0) MM(Q_X, 0, 1, 1) \
  MM(Q_X, 1, 0, 0) MM(Q_X, 1, 0, 1) MM(Q_X, 1, 1, 0) MM(Q_X, 1, 1, 1) \
  "s_nop 15\n\ts_nop 3"
#define P1_OPERANDS \
  : "+{v[32:63]}"(ac0), "+{v[64:95]}"(ac1), "+{v160}"(aoff[0]), "+{v161}"(aoff[1]), "+{v162}"(aoff[2]), "+{v163}"(aoff[3]), "+{v164}"(boff[0]), "+{v165}"(boff[1]), \
    [st] "+s"(st), [dl] "=&s"(dl) \
  : [wr] "s"(-98304) \
  : "memory", "scc", CL8(96, 97, 98, 99, 100, 101, 102, 103), CL8(104, 105, 106, 107, 108, 109, 110, 111), CL8(112, 113, 114, 115, 116, 117, 118, 119), CL8(120, 121, 122, 123, 124, 125, 126, 127), \
    CL8(128, 129, 130, 131, 132, 133, 134, 135), CL8(136, 137, 138, 139, 140, 141, 142, 143), CL8(144, 145, 146, 147, 148, 149, 150, 151), CL8(152, 153, 154, 155, 156, 157, 158, 159)
template <bool VT>
DI void p1_compute(const Ctx& c) {
  const int l = c.l, w = c.w, wm = w >> 1, wn = w & 1, r = l & 31, hh = l >> 5, q = (l & 15) >> 2, pp = l & 3;
  unsigned aoff[4], boff[2];
#pragma unroll
  for (int i = 0; i < 4; ++i) aoff[i] = c.lds0 + 65536 + (wm * 64 + r) * 128 + (((2 * i + hh) ^ ((r >> 1) & 7)) << 4);
#pragma unroll
  for (int n = 0; n < 2; ++n) boff[n] = c.lds0 + 65536 + 16384 + (8 * hh + q) * 256 + (((wn * 8 + (n ^ wm) * 4 + 2 * ((l >> 4) & 1) + (pp >> 1)) ^ (q << 2)) << 4) + 8 * (pp & 1);
  f32x32 ac0 = {}, ac1 = {};
  unsigned st = 2, dl;
  if (VT) asm volatile(P1_LOOP(QMV) P1_OPERANDS); else asm volatile(P1_LOOP(QMK) P1_OPERANDS);
  const f32x16 acc[2][2] = {{lo16(ac0), hi16(ac0)}, {lo16(ac1), hi16(ac1)}};
  LAS float* ra = (LAS float*)(c.lds + L1_FIN); LAS float* rb = ra + 128; LAS float* cs = ra + 256;
  const O1 o1 = p1_places(c, w);
  LDS_BAR();
  STAMP(3);
#pragma unroll
  for (int m = 0; m < 2; ++m)
#pragma unroll
    for (int n = 0; n < 2; ++n) {
      const int row0 = wm * 64 + m * 32, col0 = wn * 64 + (n ^ wm) * 32;
      unsigned pk[8];
      const float s0 = VT ? cs[col0 + r] : ra[row0 + r], s1 = VT ? 0.f : rb[row0 + r];
#pragma unroll
      for (int g = 0; g < 4; ++g) {
        float y[4];
        if (VT) {
          const f32x4 a4 = *(LAS f32x4*)(ra + row0 + 8 * g + 4 * hh), b4 = *(LAS f32x4*)(rb + row0 + 8 * g + 4 * hh);
#pragma unroll
          for (int i = 0; i < 4; ++i) y[i] = fmaf(acc[m][n][4 * g + i], a4[i], b4[i] * s0);
        } else {
          const f32x4 c4 = *(LAS f32x4*)(cs + col0 + 8 * g + 4 * hh);
#pragma unroll
          for (int i = 0; i < 4; ++i) y[i] = fmaf(acc[m][n][4 * g + i], s0, s1 * c4[i]);
        }
        pk[2 * g] = cvtpk(y[0], y[1]); pk[2 * g + 1] = cvtpk(y[2], y[3]);
      }
#pragma unroll
      for (int gp = 0; gp < 2; ++gp) {
        const u32x4 v = swap32_pair(pk[4 * gp], pk[4 * gp + 1], pk[4 * gp + 2], pk[4 * gp + 3]);
        st128_d(c.rs, o1.base[n] + (m * o1.sm + gp * o1.sg), np(v));
      }
    }
}

#define EX(b, i) "v_exp_f32 v[" STR(b) "+" STR(i) "], v[" STR(b) "+" STR(i) "]\n\t"
#define EX3(b, i) EX(b, i) EX(b, i + 1) EX(b, i + 2)
#define EX4(b, i) EX3(b, i) EX(b, i + 3)
#define R_SA 32
#define R_SB 64
#define R_OA 96
#define R_OB 112
#define R_NA 128
#define R_NB 144
#define R_QA 160
#define R_QB 164
#define R_QR 168
#define R_KR 170
#define R_K 172
#define R_VA 176
#define R_VB 184
#define R_IDX 192
#define R_KAD 193
#define R_KRAD 194
#define R_VAD 195
#define PV(o, vreg, sreg) "v_smfmac_f32_32x32x32_bf16 " VR(o, 16) ", " VR(vreg, 4) ", " VR(sreg, 8) ", v" STR(R_IDX) "\n\t"
#define QK16(dst, q, nm) "v_mfma_f32_32x32x16_bf16 " VR(dst, 16) ", " VR(R_K, 4) ", " VR(q, 4) ", " VR(nm, 16) "\n\t"
#define STEP(C, N, VC, VN, AB, i, MID, KRLOAD) \
  QK16(N, R_QA, R_NA) \
  MID \
  EX4(C, 0) \
  PV(R_OB, VN + 4, N + 24) \
  "ds_read_b128 " VR(VN, 4) ", v" STR(R_VAD) " offset:" STR(i * 1408) "\n\t" \
  "ds_read_b128 " VR(VN + 4, 4) ", v" STR(R_VAD) " offset:" STR(i * 1408 + 704) "\n\t" \
  EX3(C, 4) \
  QK16(N + 16, R_QB, R_NB) \
  "ds_read_b128 " VR(R_K, 4) ", v" STR(R_KAD) " offset:" STR((i / 2) * 3072 + (i & 1) * 512) "\n\t" \
  EX4(C, 7) \
  PV(R_OA, VC, C) \
  "s_waitcnt lgkmcnt(3)\n\t" \
  EX4(C, 11) \
  "v_mfma_f32_32x32x4_2b_bf16 " VR(N, 32) ", " VR(R_KR, 2) ", " VR(R_QR, 2) ", " VR(N, 32) " cbsz:1 abid:" STR(AB) "\n\t" \
  EX4(C, 15) EX4(C, 19) \
  PV(R_OA, VC + 4, C + 8) \
  "s_waitcnt lgkmcnt(0)\n\t" \
  KRLOAD \
  EX4(C, 23) \
  PV(R_OB, VC, C + 16) \
  EX4(C, 27) EX(C, 31)
#define KRL(i) "ds_read_b64 " VR(R_KR, 2) ", v" STR(R_KRAD) " offset:" STR((i / 2) * 3072) "\n\t"
#define STAGE_TOP \
  "s_barrier\n\t" \
  "v_add_u32 v" STR(R_KAD) ", %[dk], v" STR(R_KAD) "\n\tv_add_u32 v" STR(R_KRAD) ", %[dk], v" STR(R_KRAD) "\n\tv_add_u32 v" STR(R_VAD) ", %[dv], v" STR(R_VAD) "\n\t" \
  "s_sub_u32 %[dk], 0, %[dk]\n\ts_sub_u32 %[dv], 0, %[dv]\n\t"

DI f32x32 cat16(const f32x16& a, const f32x16& b) {
  return __builtin_shufflevector(a, b, 0, 1, 2, 3, 4, 5, 6, 7, 8, 9, 10, 11, 12, 13, 14, 15, 16, 17, 18, 19, 20, 21, 22, 23, 24, 25, 26, 27, 28, 29, 30, 31);
}
DI float max16(const f32x16& s) { float m = s[0];
#pragma unroll
  for (int i = 1; i < 16; ++i) m = fmaxf(m, s[i]);
  return m; }
DI f32x16 splat16(float v) { f32x16 s;
#pragma unroll
  for (int i = 0; i < 16; ++i) s[i] = v;
  return s; }
DI bool out_of_range(const f32x16& o) {
  float chk = 0.f;
#pragma unroll
  for (int i = 0; i < 12; ++i) chk += fabsf(o[i]);
  return !(chk < 1e37f) || !(upper_half(o[8]) > 0.f);
}

DI void p2_compute(const Ctx& c) {
  const int l = c.l, w = c.w, r = l & 31, hh = l >> 5, h = c.h;
  const int e = h & 1, cb = 5 * (h >> 1) + 2 * e, q0 = c.qb * 256 + w * 64;
  unsigned kad = c.lds0 + KRING + 12288 + (e + hh) * 1024 + r * 16, krad = c.lds0 + KRING + 12288 + (e ? 0 : 2048) + l * 16 + 8 * e;
  unsigned vad = c.lds0 + VRING + 11264 + hh * 352 + (r < 21 ? r : 21) * 16;
  LDS_BAR();
  s16x8 qa, qb; s16x4 qr;
#pragma unroll 1
  for (unsigned n = 0; n < SPIN_CAP; ++n) {
    FENCE();
    const u32x4 ta = ld128_d(c.rs, WS_QK + ((cb + e + hh) * 2048 + q0 + r) * 16), tb = ld128_d(c.rs, WS_QK + ((cb + e + hh) * 2048 + q0 + 32 + r) * 16);
    const u32x2 tr = ld64_d(c.rs, WS_QK + ((e ? cb : cb + 2) * 2048 + q0 + l) * 16 + 8 * e);
    qa = __builtin_bit_cast(s16x8, ta); qb = __builtin_bit_cast(s16x8, tb); qr = __builtin_bit_cast(s16x4, tr);
    if (!__any(umax(umax(umax4(ta), umax4(tb)), umax(tr[0], tr[1])) == POISON)) break;
  }
  publish<0>(c, L_CNT1, 4, WS_F1 + c.b * 8);
  STAMP(5);
  LDS_BAR();
  STAMP(6);
  s16x4 kr = *(const LAS s16x4*)((lptr)(size_t)krad + 3 * 3072);
  s16x8 kf = ld128((lptr)(size_t)kad + 3 * 3072);
  f32x32 sa = {}, sb = {};
  sa = __builtin_amdgcn_mfma_f32_32x32x4bf16_1k(kr, qr, sa, 1, 0, 0);
  f32x16 s0 = mfma32(kf, qa, lo16(sa)), s1 = mfma32(kf, qb, hi16(sa));
  kf = ld128((lptr)(size_t)kad + 3 * 3072 + 512);
  f32x8 va = {}, vb = {};
  {
    const u32x4 t0 = *(const LAS u32x4*)((lptr)(size_t)vad + 7 * 1408), t1 = *(const LAS u32x4*)((lptr)(size_t)vad + 7 * 1408 + 704);
#pragma unroll
    for (int i = 0; i < 4; ++i) { va[i] = __uint_as_float(t0[i]); va[4 + i] = __uint_as_float(t1[i]); }
  }
  f32x16 na = splat16(-swap32_max(max16(s0))), nb = splat16(-swap32_max(max16(s1))), oa = {}, ob = {};
  sa = cat16(s0 + na, s1 + nb);
  unsigned idx = 0xDDDDDDDDu, cnt = 8, dk = -12288, dv = -11264;
  STAMP(7);
  asm volatile(
      "s_waitcnt lgkmcnt(0)\n\ts_nop 4\n"
      "1:\n\t"
      STEP(R_SA, R_SB, R_VA, R_VB, 1, 0, STAGE_TOP, KRL(0))
      STEP(R_SB, R_SA, R_VB, R_VA, 0, 1, "", "")
      STEP(R_SA, R_SB, R_VA, R_VB, 1, 2, "", KRL(2))
      STEP(R_SB, R_SA, R_VB, R_VA, 0, 3, "", "")
      STEP(R_SA, R_SB, R_VA, R_VB, 1, 4, "", KRL(4))
      STEP(R_SB, R_SA, R_VB, R_VA, 0, 5, "", "")
      STEP(R_SA, R_SB, R_VA, R_VB, 1, 6, "", KRL(6))
      STEP(R_SB, R_SA, R_VB, R_VA, 0, 7, "", "")
      "s_sub_u32 %[cnt], %[cnt], 1\n\t"
      "s_cmp_lg_u32 %[cnt], 0\n\t"
      "s_cbranch_scc1 1b\n\t"
      "s_waitcnt lgkmcnt(0)\n\t"
      "s_barrier\n\t"
      PV(R_OB, R_VB + 4, R_SB + 24)
      "s_nop 15\n\t"
      : "+{v[32:63]}"(sa), "+{v[64:95]}"(sb), "+{v[96:111]}"(oa), "+{v[112:127]}"(ob), "+{v[170:171]}"(kr), "+{v[172:175]}"(kf), "+{v[176:183]}"(va), "+{v[184:191]}"(vb),
        "+{v193}"(kad), "+{v194}"(krad), "+{v195}"(vad), [cnt] "+s"(cnt), [dk] "+s"(dk), [dv] "+s"(dv)
      : "{v[128:143]}"(na), "{v[144:159]}"(nb), "{v[160:163]}"(qa), "{v[164:167]}"(qb), "{v[168:169]}"(qr), "{v192}"(idx)
      : "memory", "scc");
  STAMP(8);
  if (__any(out_of_range(oa) || out_of_range(ob))) {
    const unsigned ko = WS_QK + ((80 + cb + e + hh) * 2048 + r) * 16, kro = WS_QK + ((80 + cb + (e ? 0 : 2)) * 2048 + r) * 16 + 8 * e;
    const unsigned vg = WS_V + (h * 256 + hh) * 320 + (r < 20 ? r : 0) * 16, pad = r == 20 ? ONES2 : 0u;
    float ma = -INFINITY, mb = -INFINITY;
#pragma unroll 1
    for (int pass = 0; pass < 2; ++pass) {
      na = splat16(pass ? -ma : 0.f); nb = splat16(pass ? -mb : 0.f); oa = splat16(0.f); ob = splat16(0.f);
#pragma unroll 1
      for (int t = 0; t < 64; ++t) {
        const s16x8 k16 = __builtin_bit_cast(s16x8, ld128_d(c.rs, ko + t * 512));
        f32x32 s = cat16(mfma32(k16, qa, na), mfma32(k16, qb, nb));
        s = __builtin_amdgcn_mfma_f32_32x32x4bf16_1k(__builtin_bit_cast(s16x4, ld64_d(c.rs, kro + t * 512)), qr, s, 0, 0, 0);
        if (pass == 0) { ma = fmaxf(ma, max16(lo16(s))); mb = fmaxf(mb, max16(hi16(s))); }
        else {
#pragma unroll
          for (int g = 0; g < 2; ++g) {
            u32x4 vf = ld128_d(c.rs, vg + (4 * t + 2 * g) * 320);
            if (r >= 20) vf = u32x4{pad, pad, pad, pad};
            f32x8 pa, pb;
#pragma unroll
            for (int i = 0; i < 8; ++i) { pa[i] = __builtin_amdgcn_exp2f(s[8 * g + i]); pb[i] = __builtin_amdgcn_exp2f(s[16 + 8 * g + i]); }
            oa = __builtin_amdgcn_smfmac_f32_32x32x32_bf16(__builtin_bit_cast(bf16x8, vf), __builtin_bit_cast(bf16x16, pa), oa, 0xDDDD, 0, 0);
            ob = __builtin_amdgcn_smfmac_f32_32x32x32_bf16(__builtin_bit_cast(bf16x8, vf), __builtin_bit_cast(bf16x16, pb), ob, 0xDDDD, 0, 0);
          }
        }
      }
      if (pass == 0) { ma = swap32_max(ma); mb = swap32_max(mb); }
    }
  }
#pragma unroll
  for (int t = 0; t < 2; ++t) {
    const f32x16& o = t ? ob : oa;
    const float rl = __builtin_amdgcn_rcpf(upper_half(o[8]));
    unsigned pk[6];
#pragma unroll
    for (int i = 0; i < 6; ++i) pk[i] = cvtpk(o[2 * i] * rl, o[2 * i + 1] * rl);
    const unsigned og = WS_O + (q0 >> 6) * 81920 + (32 * t + r) * 16;
    st128_d(c.rs, og + (cb + e + hh) * 1024, np(swap32_pair(pk[0], pk[1], pk[2], pk[3])));
    if (hh == 0) st64_d(c.rs, og + (e ? cb : cb + 2) * 1024 + 8 * e, u32x2{np(pk[4]), np(pk[5])});
  }
}

#define G_ACC 32
#define G_B0 72
#define G_B1 92
#define G_A 112
#define G_VB 160
#define G_VBO 161
#define G_VA0 162
#define G_VA1 163
#define G_CHK 164
#define GMM(m, nt, B, j) "v_mfma_f32_16x16x32_bf16 " VR(G_ACC + m * 20 + nt * 4, 4) ", " VR(B + nt * 4, 4) ", " VR(G_A + j * 8 + m * 4, 4) ", " VR(G_ACC + m * 20 + nt * 4, 4) "\n\t"
#define GRB(B, nt, ks) "ds_read_b64_tr_b16 " VR(B + nt * 4, 2) ", v" STR(G_VB) " offset:" STR(ks * 5120 + nt * 32) "\n\t" \
                       "ds_read_b64_tr_b16 " VR(B + nt * 4 + 2, 2) ", v" STR(G_VB) " offset:" STR(ks * 5120 + nt * 32 + 1280) "\n\t"
#define GCK(j, i) "v_max3_u32 v" STR(G_CHK) ", v[" STR(G_A + j * 8 + i * 2) "], v[" STR(G_A + j * 8 + i * 2 + 1) "], v" STR(G_CHK) "\n\t"
#define GLD1(j, m, va) "buffer_load_dwordx4 " VR(G_A + (j) * 8 + m * 4, 4) ", v" STR(va) ", %[rs], %[so] offen offset:" STR(m * 256) " sc1\n\t"
#define GLD2(j) GLD1(j, 0, G_VA0) GLD1(j, 1, G_VA0) GLD1(j + 1, 0, G_VA1) GLD1(j + 1, 1, G_VA1) "s_add_u32 %[so], %[so], 16384\n\t"
#define GNEXT "s_add_u32 %[sl], %[sl], 20480\n\ts_cmp_eq_u32 %[sl], 143360\n\ts_cselect_b32 %[sl], 0, %[sl]\n\tv_add_u32 v" STR(G_VB) ", %[sl], v" STR(G_VBO) "\n\t"
#define GBODY(j, BC, BN, ks, VM, L0, L1) \
  "s_waitcnt vmcnt(" STR(VM) ")\n\t" \
  GMM(0, 0, BC, j) GRB(BN, 0, ks) GMM(0, 1, BC, j) GRB(BN, 1, ks) GMM(0, 2, BC, j) GRB(BN, 2, ks) GMM(0, 3, BC, j) GCK(j, 0) GCK(j, 1) GMM(0, 4, BC, j) L0 \
  GMM(1, 0, BC, j) GRB(BN, 3, ks) GMM(1, 1, BC, j) GRB(BN, 4, ks) GMM(1, 2, BC, j) GCK(j, 2) GMM(1, 3, BC, j) GCK(j, 3) GMM(1, 4, BC, j) L1
#define GEVEN(j) "s_waitcnt lgkmcnt(0)\n\t" GBODY(j, G_B0, G_B1, 1, 10, GLD1(j, 0, G_VA0), GLD1(j, 1, G_VA0))
#define GODD(j, BAR) "s_waitcnt lgkmcnt(0)\n\t" BAR GNEXT GBODY(j, G_B1, G_B0, 0, 10, GLD1(j, 0, G_VA1), GLD1(j, 1, G_VA1) "s_add_u32 %[so], %[so], 16384\n\t")
#define TEVEN(j) "s_waitcnt lgkmcnt(0)\n\t" GBODY(j, G_B0, G_B1, 1, 10 - 2 * j, "", "")
#define TODD(j) "s_waitcnt lgkmcnt(0)\n\t" GNEXT GBODY(j, G_B1, G_B0, 0, 10 - 2 * j, "", "")

DI void g80_compute(const Ctx& c, int last, u32x2 (&res)[5]) {
  int l = c.l; asm volatile("" : "+v"(l));
  const int w = c.w, wm = w & 1, wk = w >> 1, g = l >> 4, q = (l & 15) >> 2, pp = l & 3;
  const unsigned tile = last ? WS_F + c.rt * 196608 : WS_O + c.rt * 81920;
  {
    const int hd = l & 31, e = hd & 1, cb = 5 * (hd >> 1) + 2 * e;
    const unsigned po = tile + 63 * 16 + (last ? (hd >> 2) * 24 + (hd & 3) * 6 + 5 : (e ? cb : cb + 2)) * 1024 + (last ? 0 : 8 * e);
#pragma unroll 1
    for (unsigned n = 0; n < SPIN_CAP; ++n) { FENCE(); if (!__any(ld32_d(c.rs, po) == POISON)) break; }
  }
  if (last) STAMP(16); else STAMP(10);
  const unsigned va0 = tile + (8 * wk + g) * 1024 + (32 * wm + (l & 15)) * 16;
  f32x32 acc0 = {}; f32x8 acc1 = {};
  unsigned chk = 0, so = 0, sl = last ? 0 : L_WO, cnt = last ? 3 : 1;
  asm volatile(
      "s_cmp_eq_u32 %[cnt], 1\n\ts_cbranch_scc1 4f\n\t"
      GLD2(0)
      "4:\n\t"
      GLD2(2) GLD2(4)
      "s_cmp_eq_u32 %[cnt], 1\n\ts_cbranch_scc0 4f\n\t"
      GLD2(0)
      "4:\n\t"
      "s_barrier\n\t"
      "v_add_u32 v" STR(G_VB) ", %[sl], v" STR(G_VBO) "\n\t"
      GRB(G_B0, 0, 0) GRB(G_B0, 1, 0) GRB(G_B0, 2, 0) GRB(G_B0, 3, 0) GRB(G_B0, 4, 0)
      "s_cmp_eq_u32 %[cnt], 1\n\ts_cbranch_scc1 2f\n"
      "1:\n\t"
      GEVEN(0) GODD(1, "")
      "2:\n\t"
      GEVEN(2) GODD(3, "") GEVEN(4)
      GODD(5, "s_barrier\n\t")
      "s_sub_u32 %[cnt], %[cnt], 1\n\ts_cmp_lg_u32 %[cnt], 0\n\ts_cbranch_scc1 1b\n\t"
      TEVEN(0) TODD(1) TEVEN(2) TODD(3) TEVEN(4)
      "s_waitcnt vmcnt(0) lgkmcnt(0)\n\ts_barrier\n\t"
      GMM(0, 0, G_B1, 5) GMM(1, 0, G_B1, 5) GCK(5, 0) GMM(0, 1, G_B1, 5) GCK(5, 1) GMM(1, 1, G_B1, 5) GCK(5, 2) GMM(0, 2, G_B1, 5) GCK(5, 3)
      GMM(1, 2, G_B1, 5) GMM(0, 3, G_B1, 5) GMM(1, 3, G_B1, 5) GMM(0, 4, G_B1, 5) GMM(1, 4, G_B1, 5)
      "s_nop 15\n\t"
      : "+{v[32:63]}"(acc0), "+{v[64:71]}"(acc1), "+{v164}"(chk), [so] "+s"(so), [sl] "+s"(sl), [cnt] "+s"(cnt)
      : "{v161}"(c.lds0 + (64 * wk + 16 * (g >> 1) + 4 * (g & 1) + q) * 160 + pp * 8), "{v162}"(va0), "{v163}"(va0 + 4096), [rs] "s"(c.rs)
      : "memory", "scc", "v160",
        "v72", "v73", "v74", "v75", "v76", "v77", "v78", "v79", "v80", "v81", "v82", "v83", "v84", "v85", "v86", "v87", "v88", "v89", "v90", "v91", "v92", "v93", "v94", "v95",
        "v96", "v97", "v98", "v99", "v100", "v101", "v102", "v103", "v104", "v105", "v106", "v107", "v108", "v109", "v110", "v111", "v112", "v113", "v114", "v115", "v116", "v117", "v118", "v119",
        "v120", "v121", "v122", "v123", "v124", "v125", "v126", "v127", "v128", "v129", "v130", "v131", "v132", "v133", "v134", "v135", "v136", "v137", "v138", "v139", "v140", "v141", "v142", "v143",
        "v144", "v145", "v146", "v147", "v148", "v149", "v150", "v151", "v152", "v153", "v154", "v155", "v156", "v157", "v158", "v159");
  f32x4 acc[2][5];
#pragma unroll
  for (int i = 0; i < 40; ++i) acc[i / 20][(i % 20) / 4][i % 4] = i < 32 ? acc0[i] : acc1[i - 32];
  if (__any(chk == POISON)) {
    const char* wg = (last ? c.p.w2 : c.p.wo) + c.cs * 160 + (l & 15) * 2;
#pragma unroll
    for (int i = 0; i < 10; ++i) acc[i / 5][i % 5] = f32x4{};
#pragma unroll 1
    for (int st = 0; st < (last ? 24 : 10); ++st) {
      u32x4 a0, a1;
#pragma unroll 1
      for (unsigned n = 0; n < SPIN_CAP; ++n) {
        FENCE();
        a0 = ld128_d(c.rs, va0 + (st >> 1) * 16384 + (st & 1) * 4096); a1 = ld128_d(c.rs, va0 + (st >> 1) * 16384 + (st & 1) * 4096 + 256);
        if (!__any(umax(umax4(a0), umax4(a1)) == POISON)) break;
      }
      const char* wr = wg + (size_t)((st >> 1) * 128 + 64 * wk + (st & 1) * 32 + 8 * g) * 1280;
#pragma unroll
      for (int nt = 0; nt < 5; ++nt) {
        s16x8 bf;
#pragma unroll
        for (int j = 0; j < 8; ++j) bf[j] = *(const GAS short*)(wr + j * 1280 + nt * 32);
        acc[0][nt] = mfma16(bf, __builtin_bit_cast(s16x8, a0), acc[0][nt]); acc[1][nt] = mfma16(bf, __builtin_bit_cast(s16x8, a1), acc[1][nt]);
      }
    }
  }
  lptr ex = c.lds + L_EX + l * 16;
#pragma unroll
  for (int nt = 0; nt < 5; ++nt) *(LAS f32x4*)(ex + w * 5120 + nt * 1024) = wk ? acc[0][nt] : acc[1][nt];
  LDS_BAR();
  if (last) STAMP(17); else STAMP(11);
#pragma unroll
  for (int nt = 0; nt < 5; ++nt) {
    const f32x4 u = (wk ? acc[1][nt] : acc[0][nt]) + *(LAS f32x4*)(ex + (w ^ 2) * 5120 + nt * 1024);
    res[nt] = u32x2{np(cvtpk(u[0] + bflo(res[nt][0]), u[1] + bfhi(res[nt][0]))), np(cvtpk(u[2] + bflo(res[nt][1]), u[3] + bfhi(res[nt][1])))};
  }
  const int row = 32 * wm + 16 * wk + (l & 15);
  if (!last) {
    float s1 = 0.f, s2 = 0.f;
#pragma unroll
    for (int nt = 0; nt < 5; ++nt) { s1 = dot2(res[nt][0], ONES2, dot2(res[nt][1], ONES2, s1)); s2 = dot2(res[nt][0], res[nt][0], dot2(res[nt][1], res[nt][1], s2)); }
    s1 = swap16_add(s1, s1); s2 = swap16_add(s2, s2); s1 = swap32_add(s1, s1); s2 = swap32_add(s2, s2);
    if (l < 16) st64_d(c.rs, WS_RS2 + (c.rt * 8 + c.cs) * 512 + row * 8, u32x2{np(__float_as_uint(s1)), np(__float_as_uint(s2))});
  }
  const rsrc_t ro = __builtin_amdgcn_make_buffer_rsrc(last ? c.p.out : c.p.ws + WS_H, 0, S * D * 2, 0x00020000);
  const unsigned ob = c.rt * 81920 + (last ? c.cs * 160 + row * 1280 : c.cs * 10240 + row * 16), oc = last ? 16 : 1024;
#pragma unroll
  for (int i = 0; i < 3; ++i) {
    const int n1 = i < 2 ? 2 * i + 1 : 4, chunk = 4 * i + (i < 2 ? 2 * (g & 1) : 0) + (g >> 1);
    st128_d(ro, ob + chunk * oc, swap16_pair(res[2 * i][0], res[2 * i][1], res[n1][0], res[n1][1]));
  }
}

#define F_X 80
#define F_Y 108
#define FMM(S, nt, m) "v_mfma_f32_16x16x32_bf16 " VR(32 + nt * 16 + m * 4, 4) ", " VR(S + nt * 4, 4) ", " VR(S + 12 + m * 4, 4) ", " VR(32 + nt * 16 + m * 4, 4) "\n\t"
#define FRA(S, m, ks) "ds_read_b128 " VR(S + 12 + m * 4, 4) ", v140 offset:" STR(ks * 4096 + m * 256) "\n\t"
#define FRB(S, nt, h, ks) "ds_read_b64_tr_b16 " VR(S + nt * 4 + h * 2, 2) ", v[141+" STR(nt) "] offset:" STR(ks * 12288 + h * 3072) "\n\t"
DI void p4_compute(const Ctx& c) {
  int l = c.l; asm volatile("" : "+v"(l));
  const int w = c.w, g = l >> 4, q = (l & 15) >> 2, pp = l & 3;
  unsigned boff[3];
#pragma unroll
  for (int nt = 0; nt < 3; ++nt) boff[nt] = c.lds0 + (16 * (g >> 1) + 4 * (g & 1) + q) * 384 + (((3 * w + nt) ^ (2 * (g & 1) + (q >> 1))) * 32) + pp * 8;
  f32x32 ac0 = {}; f32x16 ac1 = {};
  unsigned ch = 0, sa, sb = 0;
  asm volatile(
      "v_mov_b32 v140, v136\n\tv_mov_b32 v141, v137\n\tv_mov_b32 v142, v138\n\tv_mov_b32 v143, v139\n\t"
      "s_barrier\n\t"
      FRB(F_Y, 0, 0, 0) FRB(F_Y, 0, 1, 0) FRA(F_Y, 0, 0) FRA(F_Y, 1, 0) FRA(F_Y, 2, 0) FRA(F_Y, 3, 0) FRB(F_Y, 1, 0, 0) FRB(F_Y, 1, 1, 0) FRB(F_Y, 2, 0, 0) FRB(F_Y, 2, 1, 0)
      "s_branch 2f\n"
      "1:\n\t"
      FMM(F_X, 0, 2) FRB(F_Y, 0, 0, 0) FMM(F_X, 0, 3) FRB(F_Y, 0, 1, 0) FMM(F_X, 1, 0) FRA(F_Y, 0, 0) FMM(F_X, 1, 1) FRA(F_Y, 1, 0) FMM(F_X, 1, 2) FRA(F_Y, 2, 0)
      FMM(F_X, 1, 3) FRA(F_Y, 3, 0) FMM(F_X, 2, 0) FRB(F_Y, 1, 0, 0) FMM(F_X, 2, 1) FRB(F_Y, 1, 1, 0) FMM(F_X, 2, 2) FRB(F_Y, 2, 0, 0) FMM(F_X, 2, 3) FRB(F_Y, 2, 1, 0)
      "2:\n\t"
      WL(7) FMM(F_Y, 0, 0) FRB(F_X, 0, 0, 1) WL(7) FMM(F_Y, 0, 1) FRB(F_X, 0, 1, 1) WL(7) FMM(F_Y, 0, 2) FRA(F_X, 0, 1) WL(7) FMM(F_Y, 0, 3) FRA(F_X, 1, 1)
      WL(6) FMM(F_Y, 1, 0) FRA(F_X, 2, 1) "s_add_u32 %[ch], %[ch], 1\n\t" FMM(F_Y, 1, 1) FRA(F_X, 3, 1) "s_add_u32 %[sb], %[sb], 24576\n\t"
      FMM(F_Y, 1, 2) FRB(F_X, 1, 0, 1) "s_cmp_eq_u32 %[ch], 5\n\t" FMM(F_Y, 1, 3) FRB(F_X, 1, 1, 1)
      WL(8) FMM(F_Y, 2, 0) FRB(F_X, 2, 0, 1) "s_cselect_b32 %[sb], 0, %[sb]\n\t" FMM(F_Y, 2, 1) FRB(F_X, 2, 1, 1) "s_and_b32 %[sa], %[ch], 3\n\t"
      FMM(F_Y, 2, 2) "s_lshl_b32 %[sa], %[sa], 13\n\tv_add_u32 v141, %[sb], v137\n\t" FMM(F_Y, 2, 3) "v_add_u32 v142, %[sb], v138\n\t"
      WL(7) FMM(F_X, 0, 0) "v_add_u32 v143, %[sb], v139\n\t" WL(6) FMM(F_X, 0, 1) "v_add_u32 v140, %[sa], v136\n\ts_cmp_eq_u32 %[ch], 10\n\t"
      WL(0) "s_barrier\n\t"
      "s_cbranch_scc0 1b\n\t"
      FMM(F_X, 0, 2) FMM(F_X, 0, 3) FMM(F_X, 1, 0) FMM(F_X, 1, 1) FMM(F_X, 1, 2) FMM(F_X, 1, 3) FMM(F_X, 2, 0) FMM(F_X, 2, 1) FMM(F_X, 2, 2) FMM(F_X, 2, 3)
      "s_nop 15"
      : "+{v[32:63]}"(ac0), "+{v[64:79]}"(ac1), [ch] "+s"(ch), [sa] "=&s"(sa), [sb] "+s"(sb)
      : "{v136}"(c.lds0 + L4_A + g * 1024 + (l & 15) * 16), "{v137}"(boff[0]), "{v138}"(boff[1]), "{v139}"(boff[2])
      : "memory", "scc", "v140", "v141", "v142", "v143", CL8(80, 81, 82, 83, 84, 85, 86, 87), CL8(88, 89, 90, 91, 92, 93, 94, 95), CL8(96, 97, 98, 99, 100, 101, 102, 103), CL8(104, 105, 106, 107, 108, 109, 110, 111),
        CL8(112, 113, 114, 115, 116, 117, 118, 119), CL8(120, 121, 122, 123, 124, 125, 126, 127), CL8(128, 129, 130, 131, 132, 133, 134, 135));
  f32x4 acc[4][3];
#pragma unroll
  for (int i = 0; i < 48; ++i) acc[(i >> 2) & 3][i >> 4][i & 3] = i < 32 ? ac0[i] : ac1[i - 32];
  LAS float* ra = (LAS float*)(c.lds + L_RA); LAS float* rb = ra + 64; LAS float* csum = (LAS float*)(c.lds + L_CSUM);
  float a[4], b[4]; f32x4 c4[3];
#pragma unroll
  for (int m = 0; m < 4; ++m) { a[m] = ra[16 * m + (l & 15)]; b[m] = rb[16 * m + (l & 15)]; }
#pragma unroll
  for (int nt = 0; nt < 3; ++nt) c4[nt] = *(LAS f32x4*)(csum + w * 48 + nt * 16 + 4 * g);
  STAMP(14);
#pragma unroll
  for (int nt = 0; nt < 3; ++nt)
#pragma unroll
    for (int mp = 0; mp < 2; ++mp) {
      unsigned pk[2][2];
#pragma unroll
      for (int j = 0; j < 2; ++j) {
        const int m = 2 * mp + j; float y[4];
#pragma unroll
        for (int i = 0; i < 4; ++i) y[i] = fmaxf(fmaf(acc[m][nt][i], a[m], b[m] * c4[nt][i]), 0.f);
        pk[j][0] = cvtpk(y[0], y[1]); pk[j][1] = cvtpk(y[2], y[3]);
      }
      st128_d(c.rs, WS_F + c.rt * 196608 + (c.cs * 24 + w * 6 + nt * 2 + (g >> 1)) * 1024 + (32 * mp + (g & 1) * 16 + (l & 15)) * 16, swap16_pair(pk[0][0], pk[0][1], pk[1][0], pk[1][1]));
    }
}

#define PPOS 1
#define XDELAY 6
DI void compute_waves(Ctx& c) {
  const Params& p = c.p;
  const int l = c.l, w = c.w, b = c.b, r = l & 31, hh = l >> 5;
  {
    const int xrow = 512 * ((b & 7) >> 1) + 8 * (32 * (b & 1) + (b >> 3)) + 2 * w + hh;
    const char* xs = p.x + (size_t)(xrow * 1280 + r * 16);
    const int wi = c.ct < 15 ? ((b >> 1) & 3) * 30 + (b >> 3) : 0, rg = wi & 7, cg = 15 * (b & 1) + (wi >> 3);
    u32x4 xv[3], wv[3];
    __builtin_amdgcn_s_sleep(XDELAY);
    xv[0] = *(const GAS u32x4*)xs; xv[1] = *(const GAS u32x4*)(xs + 512); xv[2] = *(const GAS u32x4*)(xs + (r < 16 ? 1024 : 0));
    colsum_load(p.wqkv + (size_t)(rg * 80 * 3840 + cg * 128), 3840, l, w, wv);
    row_stats(c, xv, xrow);
    colsum_step1(c.lds + L_PART, l, w, wv);
    LDS_BAR();
    const float v = colsum_step2(c.lds + L_PART, l, w);
    if (l < 16 && c.ct < 15) st64_d(c.rs, WS_CS1 + (rg * 1920 + cg * 64 + 16 * w + l) * 8, u32x2{__float_as_uint(v), (unsigned)c.tag});
    STAMP(1);
    if (c.ct < 10) p1_compute<false>(c);
    else if (c.ct < 15) p1_compute<true>(c);
  }
  STAMP(4);
  p2_compute(c);
  STAMP(9);
  u32x2 res[5];
  {
    const char* xg = p.x + (size_t)((c.rt * 64 + 32 * (w & 1) + 16 * (w >> 1) + (l & 15)) * 1280 + c.cs * 160 + (l >> 4) * 8);
#pragma unroll
    for (int nt = 0; nt < 5; ++nt) res[nt] = *(const GAS u32x2*)(xg + nt * 32);
  }
  int last = 0;
#pragma unroll 1
  for (;;) {
    asm volatile("" : "+s"(last));
    g80_compute(c, last, res);
    if (last) break;
    STAMP(12);
    p4_compute(c);
    STAMP(15);
    asm volatile(".Lblk_end1:");
    last = 1;
  }
  STAMP(18);
  asm volatile(".Lblk_end2:");
}

struct Park { f32x32 a, b, c, d, e; f32x16 f; f32x4 g; };
#define PARK_REGS(m, pk) m "{v[76:107]}"(pk.a), m "{v[108:139]}"(pk.b), m "{v[140:171]}"(pk.c), m "{v[172:203]}"(pk.d), m "{v[204:235]}"(pk.e), m "{v[236:251]}"(pk.f), m "{v[252:255]}"(pk.g)
#define PKL1(k, j, o) "buffer_load_dwordx4 " VR(76 + k * 20 + j * 4, 4) ", %[v" #j "], %[rs], %[so] offen offset:" STR(o) "\n\t"
#define PKL(k, ch) "s_mov_b32 %[so], " STR(ch * 163840) "\n\t" PKL1(k, 0, 0) PKL1(k, 1, 1024) PKL1(k, 2, 2048) PKL1(k, 3, 3072) PKL1(k, 4, 0)
#define W80_OPS(r) [rs] "s"(r), [v0] "v"(vo80[0]), [v1] "v"(vo80[1]), [v2] "v"(vo80[2]), [v3] "v"(vo80[3]), [v4] "v"(vo80[4])
#define W80_DMA(r, dst, off) asm volatile("s_mov_b32 m0, %[d]\n\ts_nop 0\n\t" \
    "buffer_load_dwordx4 %[v0], %[rs], %[so] offen lds\n\tbuffer_load_dwordx4 %[v1], %[rs], %[so] offen offset:1024 lds\n\t" \
    "buffer_load_dwordx4 %[v2], %[rs], %[so] offen offset:2048 lds\n\tbuffer_load_dwordx4 %[v3], %[rs], %[so] offen offset:3072 lds\n\t" \
    "s_add_u32 m0, %[d], 4096\n\ts_nop 0\n\tbuffer_load_dwordx4 %[v4], %[rs], %[so] offen lds" :: [d] "s"(dst), [so] "s"(off), W80_OPS(r) : "memory", "scc")
#define PKS1(k, j) "ds_write_b128 %[va], " VR(76 + k * 20 + j * 4, 4) " offset:" STR(j * 1024) "\n\t"
#define PUT(k, slot) asm volatile(PKS1(k, 0) PKS1(k, 1) PKS1(k, 2) PKS1(k, 3) PKS1(k, 4) :: [va] "v"(wa + slot * 20480), PARK_REGS("", pk) : "memory")
#define P1_STATE [sa] "+s"(sa), [sb] "+s"(sb), [md] "+s"(md)
#define P1_CONST [rx] "s"(rx), [rw] "s"(rw), [l0] "s"(lds0 + lw * 4096), [a0] "v"(va[0]), [a1] "v"(va[1]), [a2] "v"(va[2]), [a3] "v"(va[3]), [b0] "v"(vb[0]), [b1x] "v"(vb[1]), [b2] "v"(vb[2]), [b3] "v"(vb[3])
#define P1_LD(v, r, s, o) "buffer_load_dwordx4 %[" v "], %[" r "], %[" s "] offen offset:" #o " lds\n\t"
#define P1_STAGE \
  "s_mov_b32 m0, %[md]\n\ts_add_u32 %[md], %[md], 16384\n\t" \
  P1_LD("a0", "rx", "sa", 0) P1_LD("a1", "rx", "sa", 1024) P1_LD("a2", "rx", "sa", 2048) P1_LD("a3", "rx", "sa", 3072) \
  "s_mov_b32 m0, %[md]\n\ts_add_u32 %[sa], %[sa], 128\n\t" \
  P1_LD("b0", "rw", "sb", 0) P1_LD("b1x", "rw", "sb", 1024) P1_LD("b2", "rw", "sb", 2048) P1_LD("b3", "rw", "sb", 3072) \
  "s_add_u32 %[sb], %[sb], 245760\n\ts_cmp_eq_u32 %[sa], 1280\n\ts_cselect_b32 %[sa], 0, %[sa]\n\ts_cselect_b32 %[sb], 0, %[sb]\n\t" \
  "s_sub_u32 %[md], %[md], %[l0]\n\ts_add_u32 %[md], %[md], 16384\n\ts_and_b32 %[md], %[md], 0x18000\n\ts_add_u32 %[md], %[md], %[l0]\n\t"
#define W1_OPS [rw] "s"(rw1), [dw] "s"(dw), [v0] "v"(vo[0]), [v1] "v"(vo[1]), [v2] "v"(vo[2]), [v3] "v"(vo[3]), [v4] "v"(vo[4]), [v5] "v"(vo[5])
#define LW1(v, o) "buffer_load_dwordx4 %[" v "], %[rw], %[t0] offen offset:" STR(o) " lds\n\t"
#define LW3_0 LW1("v0", 0) LW1("v1", 1024) LW1("v2", 2048)
#define LW3_1 LW1("v3", 0) LW1("v4", 1024) LW1("v5", 2048)
#define LW3(ch, u) "s_add_u32 m0, %[dw], " STR((ch - 5 * (ch / 5)) * 24576 + u * 3072) "\n\ts_mov_b32 %[t0], " STR(ch * 196608) "\n\t" LW3_##u
#define LA(ch) "s_add_u32 m0, %[da], " STR((ch & 3) * 8192) "\n\ts_mov_b32 %[t0], " STR(ch * 8192) "\n\t" \
  "buffer_load_dwordx4 %[va], %[rs], %[t0] offen sc1 lds\n\tbuffer_load_dwordx4 %[va], %[rs], %[t0] offen offset:1024 sc1 lds\n\t"
#define CKR(ch) "ds_read_b128 v[60:63], %[la] offset:" STR((ch & 3) * 8192) "\n\tds_read_b128 v[64:67], %[la] offset:" STR((ch & 3) * 8192 + 1024) "\n\t"
#define CKT "s_waitcnt lgkmcnt(0)\n\tv_max3_u32 v60, v60, v61, v62\n\tv_max3_u32 v64, v63, v64, v65\n\tv_max3_u32 v60, v60, v66, v67\n\tv_max_u32 v60, v60, v64\n\tv_cmp_eq_u32 vcc, -1, v60\n\t"
#define FIX(ch) "s_cbranch_vccz 2f\n\ts_mov_b32 %[t1], 0\n1:\n\t" LA(ch) "s_waitcnt vmcnt(0)\n\t" CKR(ch) CKT \
  "s_cbranch_vccz 2f\n\ts_add_u32 %[t1], %[t1], 1\n\ts_cmp_lt_u32 %[t1], 0x40000\n\ts_cbranch_scc1 1b\n2:\n\t"
#define IVL(ch, VA, FETCH, VW) "s_waitcnt vmcnt(" STR(VA) ")\n\t" CKR(ch) FETCH CKT FIX(ch) "s_waitcnt vmcnt(" STR(VW) ")\n\t"
DI u64 poisoned(lptr p) { return __builtin_amdgcn_ballot_w64(umax4(*(const LAS u32x4*)p) == POISON); }

DI void loader_waves(Ctx& c) {
  const Params& p = c.p;
  const int l = c.l, lw = c.w - 4, b = c.b, t4 = lw * 64 + l, cs = c.cs, rt = c.rt;
  const unsigned lds0 = c.lds0, lin = l * 16;
  {
    const int rot = (5 * ((c.ct + 2 * c.panel) & 7)) >> 2;
    unsigned va[4], vb[4], sa = rot * 128, sb = rot * 245760, md = lds0 + 65536 + lw * 4096;
#pragma unroll
    for (int i = 0; i < 4; ++i) {
      va[i] = (32 * lw + 8 * i + (l >> 3)) * 1280 + (((l & 7) ^ ((4 * i + (l >> 4)) & 7)) << 4) - i * 1024;
      vb[i] = (16 * lw + 4 * i + (l >> 4)) * 3840 + (((l & 15) ^ (((l >> 4) & 3) << 2)) << 4) - i * 1024;
    }
    const rsrc_t rx = __builtin_amdgcn_make_buffer_rsrc((void*)(p.x + c.panel * 163840), 0, 163840, 0x00020000);
    const rsrc_t rw = __builtin_amdgcn_make_buffer_rsrc((void*)(p.wqkv + c.ct * 256), 0, 640 * 3840 - c.ct * 256, 0x00020000);
    STAMP(0);
    if (c.ct < 15) asm volatile(P1_STAGE P1_STAGE : P1_STATE : P1_CONST : "memory", "scc");
    STAMP(2);
    {
      const char* pc; unsigned len;
      asm volatile("s_getpc_b64 %0\n.Lblk_pc:\n\ts_mov_b32 %1, .Lblk_end1-.Lblk_pc\n\ts_max_i32 %1, %1, .Lblk_end2-.Lblk_pc" : "=s"(pc), "=s"(len) :: "scc");
      const unsigned off = (((b >> 3) * 4 + lw) * 4 + (l & 3)) * 128;
      touch(pc, off < len ? off : 0u, lds0 + L_TOUCH);
    }
    if (c.ct < 15) {
      const O1 o1 = p1_places(c, lw);
      unsigned t, n = 0;
#define P1_PSN(b, s) "buffer_store_dwordx4 %[ps], %[" b "], %[rs], " s " offen sc1\n\t"
#define P1_POISON "s_add_u32 %[t], %[sm], %[sg]\n\t" P1_PSN("p0", "0") P1_PSN("p0", "%[sg]") P1_PSN("p0", "%[sm]") P1_PSN("p0", "%[t]") P1_PSN("p1", "0") P1_PSN("p1", "%[sg]") P1_PSN("p1", "%[sm]") P1_PSN("p1", "%[t]")
      asm volatile(
                   "s_waitcnt vmcnt(9)\n\ts_barrier\n\t" P1_STAGE P1_STAGE P1_POISON "s_waitcnt vmcnt(24)\n\ts_barrier\n\t" P1_STAGE "s_waitcnt vmcnt(24)\n\ts_barrier\n\t" P1_STAGE "s_add_u32 %[n], %[n], 1\n\ts_waitcnt vmcnt(24)\n"
                   "1:\n\ts_barrier\n\t" P1_STAGE
                   "s_add_u32 %[n], %[n], 1\n\ts_cmp_lt_u32 %[n], 5\n\ts_waitcnt vmcnt(16)\n\ts_cbranch_scc1 1b"
                   : P1_STATE, [t] "=&s"(t), [n] "+s"(n)
                   : P1_CONST, [p0] "v"(o1.base[0]), [p1] "v"(o1.base[1]), [sm] "s"(o1.sm), [sg] "s"(o1.sg), [rs] "s"(c.rs), [ps] "v"(u32x4{POISON, POISON, POISON, POISON}) : "memory", "scc");
      STAMP(5);
      glds_d(p.ws + WS_CS1 + (2 * lw * 1920 + c.ct * 128) * 8, lin, lds0 + L1_CG + lw * 2048);
      glds_d(p.ws + WS_CS1 + ((2 * lw + 1) * 1920 + c.ct * 128) * 8, lin, lds0 + L1_CG + lw * 2048 + 1024);
      glds_m(p.ws + WS_RS1 + (c.panel * 128 + 32 * lw) * 16, lin, lds0 + L1_RG + lw * 512, 0xFFFFFFFFull);
    }
    if (lw == 0 && l < 4) *(LAS unsigned*)(c.lds + L_CNT0 + l * 4) = 0u;
    wait_bar<19>();
    publish<19>(c, L_CNT0, 4, WS_F0 + b * 8);
    if (c.ct < 15) wait_bar<11>();
    {
      const unsigned pv = (l & 1) ? 0u : ONES2;
      if (l < 32) *(LAS u32x4*)(c.lds + VRING + (16 * lw + (l >> 1)) * 352 + 320 + (l & 1) * 16) = u32x4{pv, pv, pv, pv};
    }
    if (c.ct < 15) wait_bar<0>();
    STAMP(6);
    if (c.ct < 15) {
      LAS float* ra = (LAS float*)(c.lds + L1_FIN);
      const unsigned tag = (unsigned)c.tag;
      if (lw < 2) {
        u32x4 g = *(LAS u32x4*)(c.lds + L1_RG + t4 * 16);
#pragma unroll 1
        for (unsigned n = 0; (g[1] != tag || g[3] != tag) && n < SPIN_CAP; ++n) { FENCE(); g = ld128_d(c.rs, WS_RS1 + (c.panel * 128 + t4) * 16); }
        const float sc = c.ct < 5 ? QSCALE : 1.f;
        ra[t4] = __uint_as_float(g[0]) * sc; ra[128 + t4] = __uint_as_float(g[2]) * sc;
      } else {
        const int n0 = t4 - 128; float sum = 0.f; bool ok = true;
#pragma unroll
        for (int i = 0; i < 8; ++i) { const u32x2 g = *(LAS u32x2*)(c.lds + L1_CG + i * 1024 + n0 * 8); sum += __uint_as_float(g[0]); ok = ok && g[1] == tag; }
        if (__any(!ok)) {
          sum = 0.f;
#pragma unroll 1
          for (int i = 0; i < 8; ++i) {
            u32x2 g = {0u, ~tag};
#pragma unroll 1
            for (unsigned n = 0; g[1] != tag && n < SPIN_CAP; ++n) { FENCE(); g = ld64_d(c.rs, WS_CS1 + (i * 1920 + c.ct * 128 + n0) * 8); }
            sum += __uint_as_float(g[0]);
          }
        }
        ra[256 + n0] = sum;
      }
    }
    if (c.ct < 15) LDS_BAR();
  }
  Park pk;
  const rsrc_t rw2 = __builtin_amdgcn_make_buffer_rsrc((void*)(p.w2 + cs * 160), 0, FF * D * 2 - cs * 160, 0x00020000);
  unsigned vo80[5];
  {
    const int h = c.h, e = h & 1, cb = 5 * (h >> 1) + 2 * e;
    unsigned vk[3], vv[3]; u64 mv[3], mt[2];
#pragma unroll
    for (int i = 0; i < 3; ++i) {
      const int n = 64 * (3 * lw + i) + l, kg = n / 22, dd = n - 22 * kg;
      vk[i] = WS_QK + ((80 + cb + i) * 2048 + 64 + 64 * lw - 256) * 16 + lin - i * 1024;
      vv[i] = WS_V + (h * 256 + 4 - 32 + kg) * 320 + dd * 16 - i * 1024;
      mv[i] = __builtin_amdgcn_ballot_w64(dd < 20 && kg < 32);
      if (i < 2) mt[i] = __builtin_amdgcn_ballot_w64(dd < 20 && kg >= 28);
    }
    auto issue = [&](int s, bool withk, u64 m0, u64 m1, u64 m2) {
      unsigned t;
      if (withk) asm volatile("s_mov_b32 m0, %[d]\n\ts_lshl_b32 %[t], %[s], 12\n\t"
                              "buffer_load_dwordx4 %[v0], %[rs], %[t] offen sc1 lds\n\tbuffer_load_dwordx4 %[v1], %[rs], %[t] offen offset:1024 sc1 lds\n\tbuffer_load_dwordx4 %[v2], %[rs], %[t] offen offset:2048 sc1 lds"
                              : [t] "=&s"(t) : [d] "s"(lds0 + KRING + (s & 1) * 12288 + lw * 3072), [s] "s"(s + 1), [rs] "s"(c.rs), [v0] "v"(vk[0]), [v1] "v"(vk[1]), [v2] "v"(vk[2]) : "memory", "scc");
      asm volatile("s_mov_b32 m0, %[d]\n\ts_mul_i32 %[t], %[s], 10240\n\t"
                   "s_mov_b64 exec, %[m0]\n\tbuffer_load_dwordx4 %[v0], %[rs], %[t] offen sc1 lds\n\t"
                   "s_mov_b64 exec, %[m1]\n\ts_cbranch_execz 1f\n\tbuffer_load_dwordx4 %[v1], %[rs], %[t] offen offset:1024 sc1 lds\n\t"
                   "s_mov_b64 exec, %[m2]\n\ts_cbranch_execz 1f\n\tbuffer_load_dwordx4 %[v2], %[rs], %[t] offen offset:2048 sc1 lds\n1:\n\ts_mov_b64 exec, -1"
                   : [t] "=&s"(t) : [d] "s"(lds0 + VRING + (s & 1) * 11264 + lw * 3072), [s] "s"(s + 1), [rs] "s"(c.rs), [v0] "v"(vv[0]), [v1] "v"(vv[1]), [v2] "v"(vv[2]),
                     [m0] "s"(m0), [m1] "s"(m1), [m2] "s"(m2) : "memory");
    };
    auto bad = [&](int s, u64 m0, u64 m1, u64 m2) {
      lptr k = c.lds + KRING + (s & 1) * 12288 + lw * 3072 + lin, v = c.lds + VRING + (s & 1) * 11264 + lw * 3072 + lin;
      return (poisoned(k) | poisoned(k + 1024) | poisoned(k + 2048) | (poisoned(v) & m0) | (poisoned(v + 1024) & m1) | (poisoned(v + 2048) & m2)) != 0;
    };
#pragma unroll
    for (int i = 0; i < 5; ++i) { const int n = 64 * i + l, rr = n / 10; vo80[i] = swap23(32 * lw + rr) * 1280 + (n - 10 * rr) * 16 - (i & 3) * 1024; }
    const rsrc_t rwo = __builtin_amdgcn_make_buffer_rsrc((void*)(p.wo + cs * 160), 0, D * D * 2 - cs * 160, 0x00020000);
    const unsigned xw = (b >> 3) * 4 + lw, vt50 = (l < 50 ? l : 0) * 128, vt60 = (l < 60 ? l : 0) * 256, vps = WS_RS2 + (b * 4 + lw) * 12288 + lin;
    u32x4 psn = {POISON, POISON, POISON, POISON};
    asm volatile("" : "+v"(psn));
    if (lw == 0) poll256(p.ws, WS_F0 + l * 8, c.tag);
    STAMP(7);
    LDS_BAR();
#pragma unroll 1
    for (unsigned n = 0; n < SPIN_CAP; ++n) {
      if (lw == 3) issue(-1, true, mt[0], mt[1], 0);
      issue(0, true, mv[0], mv[1], mv[2]);
      if (lw != 3) break;
      wait_vm<5>();
      if (!bad(-1, mt[0], mt[1], 0)) break;
      DRAIN();
    }
    STAMP(8);
    LDS_BAR();
    STAMP(20);
#pragma unroll 1
    for (unsigned n = 0; n < SPIN_CAP; ++n) {
      DRAIN();
      if (!bad(0, mv[0], mv[1], mv[2])) break;
      issue(0, true, mv[0], mv[1], mv[2]);
    }
    STAMP(9);
    __builtin_amdgcn_s_setprio(3);
    const int b1 = b < 192 ? b : 0, rg1 = b1 / 24, cg1 = b1 - 24 * rg1;
    unsigned w1o = (t4 >> 3) * 3072 + (l & 7) * 16, cs2o = WS_CS2 + (rg1 * 1536 + cg1 * 64 + 16 * lw + l) * 4;
    asm volatile("" : "+v"(w1o), "+v"(cs2o));
#pragma unroll 1
    for (int s = 1; s < 5; ++s) {
      LDS_BAR(); STAMPV(20 + s);
      issue(s, true, mv[0], mv[1], mv[2]);
      asm volatile("buffer_store_dwordx4 %0, %1, %2, %3 offen sc1\n\tbuffer_store_dwordx4 %0, %1, %2, %3 offen offset:1024 sc1\n\t"
                     "buffer_store_dwordx4 %0, %1, %2, %3 offen offset:2048 sc1" :: "v"(psn), "v"(vps), "s"(c.rs), "s"((s - 1) * 3072) : "memory");
      if (s == 1) {
        touch(p.wo + xw * 6400, vt50, lds0 + L_TOUCH);
        wait_vm<4>();
#pragma unroll 1
        for (unsigned n = 0; bad(1, mv[0], mv[1], mv[2]) && n < SPIN_CAP; ++n) { issue(1, true, mv[0], mv[1], mv[2]); DRAIN(); }
        if (lw == 0) {
          poll256(p.ws, WS_F1 + l * 8, c.tag);
          if (b == 0 && l == 0) stflag(p.ws, WS_EPOCH, c.tag);
        }
      } else {
#pragma unroll 1
        for (int k = 2 * s - 4; k < (s < 4 ? 2 * s - 2 : 5); ++k)
          W80_DMA(rwo, lds0 + L_WO + k * 20480 + lw * 5120, k * 163840);
        if (s < 4) touch(p.w2 + (xw * 15360 + (s & 1) * 128), vt60, lds0 + L_TOUCH);
        if (s == 4) wait_vm<8>();
        wait_vm<14>();
        if (s == 2) {
          const char* src = p.w1 + (rg1 * 80 * 3072 + cg1 * 128);
          glds(src, w1o, lds0 + L_W1B + lw * 1024); glds(src + 32 * 3072, w1o, lds0 + L_W1B + 4096 + lw * 1024);
          if (lw < 2) glds(src + 64 * 3072, w1o, lds0 + L_W1B + 8192 + lw * 1024);
        }
      }
      STAMPV(9 + s);
    }
    u32x4 w1m[3] = {*(LAS u32x4*)(c.lds + L_W1B + t4 * 16), *(LAS u32x4*)(c.lds + L_W1B + 4096 + t4 * 16), {}};
    if (lw < 2) w1m[2] = *(LAS u32x4*)(c.lds + L_W1B + 8192 + t4 * 16);
    colsum_step1(c.lds + L_PART + 1024, l, lw, w1m);
    unsigned so;
#define PARK2(m, k0, c0, k1, c1) asm volatile(PKL(k0, c0) PKL(k1, c1) : PARK_REGS(m, pk), [so] "=&s"(so) : W80_OPS(rw2) : "memory")
    LDS_BAR(); STAMP(25);
    {
      const float cs2 = colsum_step2(c.lds + L_PART + 1024, l, lw);
      if (l < 16 && b < 192) st32_d(c.rs, cs2o, __float_as_uint(cs2));
    }
    issue(5, true, mv[0], mv[1], mv[2]);
    PARK2("=&", 0, 0, 1, 1);
    wait_bar<10>(); STAMP(26);
    publish<10>(c, L_CNTP, 4, WS_FP + b * 8);
    issue(6, true, mv[0], mv[1], mv[2]);
    PARK2("+", 2, 2, 3, 3);
    wait_bar<10>(); STAMP(27);
    issue(7, lw != 3, lw == 3 ? mv[0] & ~mt[0] : mv[0], lw == 3 ? 0 : mv[1], mv[2]);
    PARK2("+", 4, 7, 5, 8);
    wait_bar<10>(); STAMP(28);
    if (lw == 0) poll256(p.ws, WS_FP + l * 8, c.tag);
    PARK2("+", 6, 9, 7, 10);
    touch(p.w1 + xw * 15360, vt60, lds0 + L_TOUCH);
    touch(p.w1 + (xw * 15360 + 128), vt60, lds0 + L_TOUCH);
  }
  {
    unsigned vo[6];
#pragma unroll
    for (int i = 0; i < 3; ++i) {
      const int n = 64 * i + l, rr = n / 24, pos = n - 24 * rr;
      vo[i] = ((rr & 3) + 8 * (rr >> 2)) * 3072 + ((((pos >> 1) ^ ((rr >> 1) & 3)) << 1) | (pos & 1)) * 16 - i * 1024;
      vo[3 + i] = vo[i] + 4 * 3072;
    }
    const rsrc_t rw1 = __builtin_amdgcn_make_buffer_rsrc((void*)(p.w1 + cs * 384 + lw * 16 * 3072), 0, D * FF * 2 - cs * 384 - lw * 16 * 3072, 0x00020000);
    const unsigned dw = lds0 + lw * 6144, da = lds0 + L4_A + lw * 2048, va = WS_H + rt * 81920 + lw * 2048 + lin;
    const unsigned so = WS_RS2 + (rt * 8 + (l >> 4)) * 512 + (16 * lw + (l & 15)) * 8;
    unsigned t0, t1r; u32x2 s0, s1;
    LDS_BAR();
    STAMP(17);
    asm volatile(LW3(0, 0) LW3(0, 1) LW3(1, 0) LW3(1, 1) : [t0] "=&s"(t0) : W1_OPS : "memory");
    if (lw < 3) {
      float t = 0.f;
#pragma unroll
      for (int i = 0; i < 8; ++i) t += __uint_as_float(ld32_d(c.rs, WS_CS2 + (i * 1536 + cs * 192 + t4) * 4));
      *(LAS float*)(c.lds + L_CSUM + t4 * 4) = t;
    }
    asm volatile(PKL(8, 11) : PARK_REGS("+", pk), [so] "=&s"(t0) : W80_OPS(rw2) : "memory");
    LDS_BAR(); LDS_BAR(); LDS_BAR(); LDS_BAR();
    STAMP(18);
    asm volatile(
        LW3(2, 0) LW3(2, 1) LW3(3, 0) LW3(3, 1)
        "s_mov_b32 %[t1], 0\n"
        "1:\n\t" LA(0) LA(1) LA(2) LA(3)
        "s_waitcnt vmcnt(6)\n\t" CKR(0) CKT
        "s_cbranch_vccz 2f\n\ts_waitcnt vmcnt(0)\n\ts_add_u32 %[t1], %[t1], 1\n\ts_cmp_lt_u32 %[t1], 0x40000\n\ts_cbranch_scc1 1b\n"
        "2:\n\ts_barrier\n\t"
        IVL(1, 4, LW3(4, 0) LW3(4, 1), 63) "s_barrier\n\t"
        IVL(2, 8, LA(4) LW3(5, 0), 63) "s_barrier\n\t"
        IVL(3, 11, LA(5) LW3(5, 1), 63) "s_barrier\n\t"
        IVL(4, 8, LA(6) LW3(6, 0) LW3(6, 1), 63) "s_barrier\n\t"
        IVL(5, 11, LA(7) LW3(7, 0), 13) "s_barrier\n\t"
        IVL(6, 11, LA(8) LW3(7, 1) LW3(8, 0), 13) "s_barrier\n\t"
        IVL(7, 11, LA(9) LW3(8, 1), 8) "s_barrier\n\t"
        IVL(8, 11, "buffer_load_dwordx2 %[s0], %[so], %[rs], 0 offen sc1\n\tbuffer_load_dwordx2 %[s1], %[so], %[rs], 0 offen offset:2048 sc1\n\t" LW3(9, 0) LW3(9, 1), 8) "s_barrier\n\t"
        IVL(9, 11, "", 6)
        : [t0] "=&s"(t0), [t1] "=&s"(t1r), [s0] "=&v"(s0), [s1] "=&v"(s1)
        : W1_OPS, [rs] "s"(c.rs), [da] "s"(da), [va] "v"(va), [la] "v"(da + lin), [so] "v"(so) : "memory", "scc", "vcc", "v60", "v61", "v62", "v63", "v64", "v65", "v66", "v67");
    STAMP(29);
#pragma unroll 1
    for (unsigned n = 0; __any(umax(umax(s0[0], s0[1]), umax(s1[0], s1[1])) == POISON) && n < SPIN_CAP; ++n) { FENCE(); s0 = ld64_d(c.rs, so); s1 = ld64_d(c.rs, so + 2048); }
    float t1 = __uint_as_float(s0[0]) + __uint_as_float(s1[0]), t2 = __uint_as_float(s0[1]) + __uint_as_float(s1[1]);
    t1 = swap16_add(t1, t1); t2 = swap16_add(t2, t2); t1 = swap32_add(t1, t1); t2 = swap32_add(t2, t2);
    const float mean = t1 * (1.f / D), var = fmaxf(t2 * (1.f / D) - mean * mean, 0.f), a = __builtin_amdgcn_rsqf(var + LN_EPS);
    if (l < 32) *(LAS float*)(c.lds + L_RA + (l >> 4) * 256 + (16 * lw + (l & 15)) * 4) = l < 16 ? a : -mean * a;
    wait_bar<0>();
  }
  {
    const unsigned wa = lds0 + lw * 5120 + lin;
    LDS_BAR();
    STAMP(30);
#pragma unroll 1
    for (int k = 4; k < 7; ++k) W80_DMA(rw2, lds0 + k * 20480 + lw * 5120, k * 163840);
    PUT(0, 0); PUT(1, 1); PUT(2, 2); PUT(3, 3);
    STAMP(31);
    LDS_BAR();
    wait_bar<0>(); PUT(4, 0); PUT(5, 1); PUT(6, 2);
    LDS_BAR(); PUT(7, 3); PUT(8, 4);
    LDS_BAR(); LDS_BAR(); LDS_BAR();
  }
}

__global__ void __launch_bounds__(512, 2) block_kernel(Params p) {
  extern __shared__ __attribute__((aligned(16))) char smem[];
  Ctx c;
  c.p = p; c.lds = (lptr)smem; c.lds0 = (unsigned)(size_t)c.lds; c.l = threadIdx.x & 63; c.w = rfl(threadIdx.x >> 6); c.b = blockIdx.x;
  const int xcd = c.b & 7, j = c.b >> 3;
  const int t1 = 30 * (xcd & 1) + j;
  c.panel = 4 * (xcd >> 1) + (t1 & 3); c.ct = j < 30 ? t1 >> 2 : 15;
  c.h = c.rt = 4 * xcd + (j >> 3); c.qb = c.cs = j & 7;
  c.st_lo = c.st_hi = 0;
  c.rs = __builtin_amdgcn_make_buffer_rsrc(p.ws, 0, WS_END, 0x00020000);
  c.tag = *(const __attribute__((address_space(4))) u64*)(p.ws + WS_EPOCH) + 1;
  if (__builtin_expect(c.w >= 4, 1)) loader_waves(c);
  else { STAMP(0); compute_waves(c); }
}

extern "C" void kernel_launch(void* const* d_in, const int* in_sizes, int n_in, void* d_out, int out_size, void* d_ws, size_t ws_size, hipStream_t stream) {
  Params p;
  p.x = (const char*)d_in[0]; p.wqkv = (const char*)d_in[1]; p.wo = (const char*)d_in[2]; p.w1 = (const char*)d_in[4]; p.w2 = (const char*)d_in[5];
  p.out = (char*)d_out; p.ws = (char*)d_ws;
  (void)hipFuncSetAttribute((const void*)block_kernel, hipFuncAttributeMaxDynamicSharedMemorySize, LDS_BYTES);
  block_kernel<<<256, 512, LDS_BYTES, stream>>>(p);
}
```

```cpp
#include <hip/hip_runtime.h>
#include <hip/hip_bf16.h>

#define DI __device__ __forceinline__
#define LAS __attribute__((address_space(3)))
#define GAS __attribute__((address_space(1)))
typedef unsigned long long u64;
typedef __attribute__((ext_vector_type(8))) short s16x8;
typedef __attribute__((ext_vector_type(4))) short s16x4;
typedef __attribute__((ext_vector_type(8))) __bf16 bf16x8;
typedef __attribute__((ext_vector_type(16))) __bf16 bf16x16;
typedef __attribute__((ext_vector_type(2))) __bf16 bf16x2;
typedef __attribute__((ext_vector_type(2))) float f32x2;
typedef __attribute__((ext_vector_type(4))) float f32x4;
typedef __attribute__((ext_vector_type(8))) float f32x8;
typedef __attribute__((ext_vector_type(16))) float f32x16;
typedef __attribute__((ext_vector_type(32))) float f32x32;
typedef __attribute__((ext_vector_type(4))) unsigned u32x4;
typedef __attribute__((ext_vector_type(2))) unsigned u32x2;
typedef LAS char* lptr;
typedef __amdgpu_buffer_rsrc_t rsrc_t;

#define V_BY_HEAD
#define LAG 1
#define SDELAY 10
#define PDELAY 8
#define ADELAY 6
constexpr int S = 2048, D = 640, FF = 1536;
constexpr float LN_EPS = 1e-5f;
constexpr float QSCALE = 0.22360679774997896f * 1.4426950408889634f;

constexpr unsigned WS_EPOCH = 0, WS_F0 = 4096, WS_F1 = 6144, WS_FP = 8192;
constexpr unsigned WS_RS1 = 65536;
constexpr unsigned WS_CS1 = WS_RS1 + 2048 * 16;
constexpr unsigned WS_CS2 = WS_CS1 + 8 * 1920 * 8;
constexpr unsigned WS_QK = WS_CS2 + 8 * 1536 * 4;
constexpr unsigned WS_V = WS_QK + 160 * 2048 * 16;
constexpr unsigned WS_RS2 = WS_V + 256 * 640 * 16;
constexpr unsigned WS_O = WS_RS2 + 32 * 8 * 512;
constexpr unsigned WS_H = WS_O + S * D * 2;
constexpr unsigned WS_F = WS_H + S * D * 2;
constexpr unsigned WS_END = WS_F + S * FF * 2;
constexpr int LDS_BYTES = 163840;
constexpr unsigned SPIN_CAP = 1u << 18;
constexpr unsigned POISON = 0xFFFFFFFFu;
constexpr int L1_CG = 131072, L1_RG = L1_CG + 8192, L1_FIN = L1_RG + 2048;
constexpr int L1_XS = 131072, L1_WS = L1_XS + 10240;
constexpr int KRING = 0, VRING = 24576, L_W1B = 47104, L_WO = 57344;
constexpr int L4_A = 122880, L_EX = 98304;
constexpr int L_CNT0 = 159744, L_CNT1 = L_CNT0 + 4, L_CNTP = L_CNT0 + 8, L_CSUM = L_CNT0 + 256, L_RA = L_CSUM + 768, L_TOUCH = L_RA + 512, L_PART = L_TOUCH + 256;

#define TS(i) ""
#define TS_CLOB
#define TS_PUT(i, k)
#define STAMP(k)
#define STAMPV(k)
struct Params { const char* x; const char* wqkv; const char* wo; const char* w1; const char* w2; char* out; char* ws; };
struct Ctx { Params p; lptr lds; unsigned lds0; rsrc_t rs; u64 tag; int l, w, b, panel, ct, h, qb, rt, cs, rot5, xr0, wi; mutable unsigned st_lo, st_hi; };

DI unsigned rfl(unsigned v) { return __builtin_amdgcn_readfirstlane(v); }
DI unsigned cvtpk(float lo, float hi) { f32x2 v = {lo, hi}; return __builtin_bit_cast(unsigned, __builtin_convertvector(v, bf16x2)); }
DI float bflo(unsigned v) { return __uint_as_float(v << 16); }
DI float bfhi(unsigned v) { return __uint_as_float(v & 0xffff0000u); }
DI void glds(const char* sbase, unsigned voff, unsigned lds) {
  asm volatile("s_mov_b32 m0, %2\n\ts_nop 0\n\tglobal_load_lds_dwordx4 %0, %1" :: "v"(voff), "s"(sbase), "s"(lds) : "memory");
}
DI void glds_d(const char* sbase, unsigned voff, unsigned lds) {
  asm volatile("s_mov_b32 m0, %2\n\ts_nop 0\n\tglobal_load_lds_dwordx4 %0, %1 sc1" :: "v"(voff), "s"(sbase), "s"(lds) : "memory");
}
DI void glds_m(const char* sbase, unsigned voff, unsigned lds, u64 mask) {
  asm volatile("s_mov_b32 m0, %2\n\ts_mov_b64 exec, %3\n\tglobal_load_lds_dwordx4 %0, %1 sc1\n\ts_mov_b64 exec, -1" :: "v"(voff), "s"(sbase), "s"(lds), "s"(mask) : "memory");
}
DI void touch(const char* sbase, unsigned voff, unsigned lds) {
  asm volatile("s_mov_b32 m0, %2\n\ts_nop 0\n\tglobal_load_lds_dword %0, %1" :: "v"(voff), "s"(sbase), "s"(lds) : "memory");
}
DI void touch64(const char* a, unsigned lds) { asm volatile("s_mov_b32 m0, %1\n\ts_nop 0\n\tglobal_load_lds_dword %0, off" :: "v"(a), "s"(lds) : "memory"); }
template <int N> DI void wait_vm() { asm volatile("s_waitcnt vmcnt(%0)" :: "i"(N) : "memory"); }
template <int N> DI void wait_bar() { asm volatile("s_waitcnt vmcnt(%0) lgkmcnt(0)\n\ts_barrier" :: "i"(N) : "memory"); }
#define LDS_BAR() asm volatile("s_waitcnt lgkmcnt(0)\n\ts_barrier" ::: "memory")
#define DRAIN() asm volatile("s_waitcnt vmcnt(0)" ::: "memory")
#define STR_(x) #x
#define STR(x) STR_(x)
#define VR(b, n) "v[" STR(b) ":" STR(b) "+" STR(n) "-1]"
#define WL(n) "s_waitcnt lgkmcnt(" #n ")\n\t"
#define CL8(a, b, c, d, e, f, g, h) "v" #a, "v" #b, "v" #c, "v" #d, "v" #e, "v" #f, "v" #g, "v" #h
DI s16x8 ld128(lptr p) { return *(const LAS s16x8*)p; }
DI s16x4 ldtr(lptr p) { return __builtin_amdgcn_ds_read_tr16_b64_v4i16((LAS s16x4*)p); }
DI s16x8 ldtr2(lptr p, int d) { return __builtin_shufflevector(ldtr(p), ldtr(p + d), 0, 1, 2, 3, 4, 5, 6, 7); }
DI f32x16 mfma32(s16x8 a, s16x8 b, f32x16 c) { return __builtin_amdgcn_mfma_f32_32x32x16_bf16(__builtin_bit_cast(bf16x8, a), __builtin_bit_cast(bf16x8, b), c, 0, 0, 0); }
DI f32x4 mfma16(s16x8 a, s16x8 b, f32x4 c) { return __builtin_amdgcn_mfma_f32_16x16x32_bf16(__builtin_bit_cast(bf16x8, a), __builtin_bit_cast(bf16x8, b), c, 0, 0, 0); }
DI float dot2(unsigned a, unsigned b, float c) { return __builtin_amdgcn_fdot2_f32_bf16(__builtin_bit_cast(bf16x2, a), __builtin_bit_cast(bf16x2, b), c, false); }
constexpr unsigned ONES2 = 0x3f803f80u;
DI void st128_d(rsrc_t rs, unsigned off, u32x4 v) { __builtin_amdgcn_raw_buffer_store_b128(v, rs, off, 0, 16); }
DI void st_poison(rsrc_t rs, unsigned off, unsigned soff) {
  asm volatile("buffer_store_dwordx4 %0, %1, %2, %3 offen sc1\n\ts_nop 1" :: "v"(u32x4{POISON, POISON, POISON, POISON}), "v"(off), "s"(rs), "s"(soff) : "memory");
}
DI void st64_d(rsrc_t rs, unsigned off, u32x2 v) { __builtin_amdgcn_raw_buffer_store_b64(v, rs, off, 0, 16); }
DI void st32_d(rsrc_t rs, unsigned off, unsigned v) { __builtin_amdgcn_raw_buffer_store_b32(v, rs, off, 0, 16); }
DI u32x4 ld128_d(rsrc_t rs, unsigned off) { return __builtin_amdgcn_raw_buffer_load_b128(rs, off, 0, 16); }
DI u32x2 ld64_d(rsrc_t rs, unsigned off) { return __builtin_amdgcn_raw_buffer_load_b64(rs, off, 0, 16); }
DI unsigned ld32_d(rsrc_t rs, unsigned off) { return __builtin_amdgcn_raw_buffer_load_b32(rs, off, 0, 16); }
DI u64 ldflag(const char* ws, unsigned off) { return __hip_atomic_load((GAS u64*)(ws + off), __ATOMIC_RELAXED, __HIP_MEMORY_SCOPE_AGENT); }
DI void stflag(char* ws, unsigned off, u64 v) { __hip_atomic_store((GAS u64*)(ws + off), v, __ATOMIC_RELAXED, __HIP_MEMORY_SCOPE_AGENT); }
DI void poll256(const char* ws, unsigned off, u64 tag) {
#pragma unroll 1
  for (unsigned n = 0; n < SPIN_CAP; ++n) {
    const u64 a = ldflag(ws, off), b = ldflag(ws, off + 512), c = ldflag(ws, off + 1024), d = ldflag(ws, off + 1536);
    if (__all(a == tag && b == tag && c == tag && d == tag)) break;
    __builtin_amdgcn_s_sleep(4);
  }
}
template <int YOUNGER> DI void publish(const Ctx& c, int cnt_off, unsigned n, unsigned flag_off) {
  wait_vm<YOUNGER>();
  if (c.l == 0) {
    const unsigned old = __hip_atomic_fetch_add((LAS unsigned*)(c.lds + cnt_off), 1u, __ATOMIC_RELAXED, __HIP_MEMORY_SCOPE_WORKGROUP);
    if (old == n - 1) stflag(c.p.ws, flag_off, c.tag);
  }
}
template <int CTRL> DI float dpp_add(float v) { return v + __int_as_float(__builtin_amdgcn_update_dpp(0, __float_as_int(v), CTRL, 0xf, 0xf, true)); }
DI float swap16_add(float a, float b) { const auto t = __builtin_amdgcn_permlane16_swap(__float_as_uint(a), __float_as_uint(b), false, false); return __uint_as_float(t[0]) + __uint_as_float(t[1]); }
DI float swap32_add(float a, float b) { const auto t = __builtin_amdgcn_permlane32_swap(__float_as_uint(a), __float_as_uint(b), false, false); return __uint_as_float(t[0]) + __uint_as_float(t[1]); }
DI float swap32_max(float a) { const auto t = __builtin_amdgcn_permlane32_swap(__float_as_uint(a), __float_as_uint(a), false, false); return fmaxf(__uint_as_float(t[0]), __uint_as_float(t[1])); }
DI float upper_half(float a) { const auto t = __builtin_amdgcn_permlane32_swap(__float_as_uint(a), __float_as_uint(a), false, false); return __uint_as_float(t[1]); }
DI u32x4 swap32_pair(unsigned a0, unsigned a1, unsigned b0, unsigned b1) {
  const auto s0 = __builtin_amdgcn_permlane32_swap(a0, b0, false, false), s1 = __builtin_amdgcn_permlane32_swap(a1, b1, false, false);
  return u32x4{s0[0], s1[0], s0[1], s1[1]};
}
DI u32x4 swap16_pair(unsigned a0, unsigned a1, unsigned b0, unsigned b1) {
  const auto s0 = __builtin_amdgcn_permlane16_swap(a0, b0, false, false), s1 = __builtin_amdgcn_permlane16_swap(a1, b1, false, false);
  return u32x4{s0[0], s1[0], s0[1], s1[1]};
}
DI void colsum_step1(lptr part, int l, int w4, const u32x4 (&m)[3]) {
  float v[8], u[4], t[2];
#pragma unroll
  for (int i = 0; i < 4; ++i) { v[2 * i] = bflo(m[0][i]) + bflo(m[1][i]) + bflo(m[2][i]); v[2 * i + 1] = bfhi(m[0][i]) + bfhi(m[1][i]) + bfhi(m[2][i]); }
#pragma unroll
  for (int i = 0; i < 4; ++i) u[i] = swap32_add(v[i], v[i + 4]);
#pragma unroll
  for (int i = 0; i < 2; ++i) t[i] = dpp_add<0x128>(swap16_add(u[i], u[i + 2]));
  *(LAS f32x2*)(part + (w4 * 64 + (l & 7) * 8 + 4 * (l >> 5) + 2 * ((l >> 4) & 1)) * 4) = f32x2{t[0], t[1]};
}
DI float colsum_step2(lptr part, int l, int w4) {
  float v = *(LAS float*)(part + ((l >> 4) * 64 + 16 * w4 + (l & 15)) * 4);
  v = swap16_add(v, v); return swap32_add(v, v);
}
DI unsigned np(unsigned v) { return v < POISON - 1 ? v : POISON - 1; }
DI u32x4 np(u32x4 v) { return u32x4{np(v[0]), np(v[1]), np(v[2]), np(v[3])}; }
DI unsigned umax(unsigned a, unsigned b) { return a > b ? a : b; }
DI unsigned umin(unsigned a, unsigned b) { return a < b ? a : b; }
DI unsigned umax4(u32x4 v) { return umax(umax(v[0], v[1]), umax(v[2], v[3])); }
#define FENCE() asm volatile("" ::: "memory")
DI f32x16 lo16(const f32x32& s) { return __builtin_shufflevector(s, s, 0, 1, 2, 3, 4, 5, 6, 7, 8, 9, 10, 11, 12, 13, 14, 15); }
DI f32x16 hi16(const f32x32& s) { return __builtin_shufflevector(s, s, 16, 17, 18, 19, 20, 21, 22, 23, 24, 25, 26, 27, 28, 29, 30, 31); }
DI int swap23(int k) { return (k & ~12) | ((k & 4) << 1) | ((k & 8) >> 1); }

DI void row_stats(const Ctx& c, const u32x4 (&xv)[3], int xrow) {
  const int r = c.l & 31;
  float s1 = 0.f, s2 = 0.f;
#pragma unroll
  for (int i = 0; i < 3; ++i)
#pragma unroll
    for (int k = 0; k < 4; ++k) { const unsigned v = (i < 2 || r < 16) ? xv[i][k] : 0u; s1 = dot2(v, ONES2, s1); s2 = dot2(v, v, s2); }
  s1 = dpp_add<0x128>(dpp_add<0x141>(dpp_add<0x4E>(dpp_add<0xB1>(s1)))); s2 = dpp_add<0x128>(dpp_add<0x141>(dpp_add<0x4E>(dpp_add<0xB1>(s2))));
  s1 = swap16_add(s1, s1); s2 = swap16_add(s2, s2);
  const float mean = s1 * (1.f / D), var = fmaxf(s2 * (1.f / D) - mean * mean, 0.f), a = __builtin_amdgcn_rsqf(var + LN_EPS);
  const unsigned tag = (unsigned)c.tag;
  if (r == 0) st128_d(c.rs, WS_RS1 + xrow * 16, u32x4{__float_as_uint(a), tag, __float_as_uint(-mean * a), tag});
}
struct O1 { unsigned base[2], sm, sg; };
DI O1 p1_places(const Ctx& c, int w) {
  const int wm = w >> 1, wn = w & 1, r = c.l & 31, hh = c.l >> 5; const bool vt = c.ct >= 10;
  O1 o;
#pragma unroll
  for (int n = 0; n < 2; ++n) {
    const int f = (c.ct - 10) * 128 + wn * 64 + (n ^ wm) * 32 + r, h = f / 20, d = f - 20 * h, dd = (h & 1) ? (d < 4 ? d + 16 : d - 4) : d;
    o.base[n] = vt ? WS_V + (h * 256 + c.panel * 16 + wm * 8 + hh) * 320 + dd * 16 : WS_QK + ((c.ct * 16 + wn * 8 + (n ^ wm) * 4 + hh) * 2048 + c.panel * 128 + wm * 64 + r) * 16;
  }
  o.sm = vt ? 4 * 320 : 512; o.sg = vt ? 2 * 320 : 2 * 32768;
  return o;
}
#define Q_X 96
#define Q_Y 128
#define QMK(S, k, m, n) "v_mfma_f32_32x32x16_bf16 " VR(32 + m * 32 + n * 16, 16) ", " VR(S + 16 + k * 8 + n * 4, 4) ", " VR(S + k * 8 + m * 4, 4) ", " VR(32 + m * 32 + n * 16, 16) "\n\t"
#define QMV(S, k, m, n) "v_mfma_f32_32x32x16_bf16 " VR(32 + m * 32 + n * 16, 16) ", " VR(S + k * 8 + m * 4, 4) ", " VR(S + 16 + k * 8 + n * 4, 4) ", " VR(32 + m * 32 + n * 16, 16) "\n\t"
#define QRA(S, k, m, ks) "ds_read_b128 " VR(S + k * 8 + m * 4, 4) ", v[160+" STR(ks) "] offset:" STR(m * 4096) "\n\t"
#define QRB(S, k, n, h, ks) "ds_read_b64_tr_b16 " VR(S + 16 + k * 8 + n * 4 + h * 2, 2) ", v[164+" STR(n) "] offset:" STR((ks) * 4096 + h * 1024) "\n\t"
#define QR0(S, k, ks) QRA(S, k, 0, ks) QRB(S, k, 0, 0, ks)
#define QR1(S, k, ks) QRB(S, k, 0, 1, ks) QRB(S, k, 1, 0, ks)
#define QR2(S, k, ks) QRB(S, k, 1, 1, ks) QRA(S, k, 1, ks)
#define QAD(i) "v_add_u32 v[160+" #i "], %[dl], v[160+" #i "]\n\t"
#define P1_LOOP(MM) \
  QR0(Q_Y, 0, 0) QR1(Q_Y, 0, 0) QR2(Q_Y, 0, 0) QR0(Q_Y, 1, 1) QR1(Q_Y, 1, 1) QR2(Q_Y, 1, 1) \
  "s_branch 2f\n" \
  "1:\n\t" \
  MM(Q_X, 0, 0, 1) QR0(Q_Y, 0, 0) MM(Q_X, 0, 1, 0) QR1(Q_Y, 0, 0) MM(Q_X, 0, 1, 1) QR2(Q_Y, 0, 0) \
  MM(Q_X, 1, 0, 0) QR0(Q_Y, 1, 1) MM(Q_X, 1, 0, 1) QR1(Q_Y, 1, 1) MM(Q_X, 1, 1, 0) QR2(Q_Y, 1, 1) MM(Q_X, 1, 1, 1) \
  "2:\n\t" \
  WL(9) MM(Q_Y, 0, 0, 0) QR0(Q_X, 0, 2) WL(9) MM(Q_Y, 0, 0, 1) QR1(Q_X, 0, 2) WL(10) MM(Q_Y, 0, 1, 0) QR2(Q_X, 0, 2) \
  MM(Q_Y, 0, 1, 1) QR0(Q_X, 1, 3) WL(11) MM(Q_Y, 1, 0, 0) QR1(Q_X, 1, 3) WL(11) MM(Q_Y, 1, 0, 1) QR2(Q_X, 1, 3) \
  WL(12) MM(Q_Y, 1, 1, 0) "s_add_u32 %[st], %[st], 1\n\ts_and_b32 %[dl], %[st], 3\n\ts_cselect_b32 %[dl], 32768, %[wr]\n\t" \
  MM(Q_Y, 1, 1, 1) QAD(0) QAD(1) \
  WL(9) MM(Q_X, 0, 0, 0) QAD(2) QAD(3) QAD(4) QAD(5) \
  "s_cmp_eq_u32 %[st], 12\n\ts_cbranch_scc1 3f\n\t" \
  WL(0) "s_barrier\n\ts_branch 1b\n" \
  "3:\n\t" WL(0) \
  MM(Q_X, 0, 0, 1) MM(Q_X, 0, 1, 0) MM(Q_X, 0, 1, 1) \
  MM(Q_X, 1, 0, 0) MM(Q_X, 1, 0, 1) MM(Q_X, 1, 1, 0) MM(Q_X, 1, 1, 1) \
  "s_nop 15\n\ts_nop 3"
#define P1_OPERANDS \
  : "+{v[32:63]}"(ac0), "+{v[64:95]}"(ac1), "+{v160}"(aoff[0]), "+{v161}"(aoff[1]), "+{v162}"(aoff[2]), "+{v163}"(aoff[3]), "+{v164}"(boff[0]), "+{v165}"(boff[1]), \
    [st] "+s"(st), [dl] "=&s"(dl) \
  : [wr] "s"(-98304) \
  : "memory", "scc", CL8(96, 97, 98, 99, 100, 101, 102, 103), CL8(104, 105, 106, 107, 108, 109, 110, 111), CL8(112, 113, 114, 115, 116, 117, 118, 119), CL8(120, 121, 122, 123, 124, 125, 126, 127), \
    CL8(128, 129, 130, 131, 132, 133, 134, 135), CL8(136, 137, 138, 139, 140, 141, 142, 143), CL8(144, 145, 146, 147, 148, 149, 150, 151), CL8(152, 153, 154, 155, 156, 157, 158, 159)
template <bool VT>
DI void p1_compute(const Ctx& c) {
  const int l = c.l, w = c.w, wm = w >> 1, wn = w & 1, r = l & 31, hh = l >> 5, q = (l & 15) >> 2, pp = l & 3;
  unsigned aoff[4], boff[2];
#pragma unroll
  for (int i = 0; i < 4; ++i) aoff[i] = c.lds0 + 65536 + (wm * 64 + r) * 128 + (((2 * i + hh) ^ ((r >> 1) & 7)) << 4);
#pragma unroll
  for (int n = 0; n < 2; ++n) boff[n] = c.lds0 + 65536 + 16384 + (8 * hh + q) * 256 + (((wn * 8 + (n ^ wm) * 4 + 2 * ((l >> 4) & 1) + (pp >> 1)) ^ (q << 2)) << 4) + 8 * (pp & 1);
  f32x32 ac0 = {}, ac1 = {};
  unsigned st = 2, dl;
  if (VT) asm volatile(P1_LOOP(QMV) P1_OPERANDS); else asm volatile(P1_LOOP(QMK) P1_OPERANDS);
  const f32x16 acc[2][2] = {{lo16(ac0), hi16(ac0)}, {lo16(ac1), hi16(ac1)}};
  LAS float* ra = (LAS float*)(c.lds + L1_FIN); LAS float* rb = ra + 128; LAS float* cs = ra + 256;
  const O1 o1 = p1_places(c, w);
  LDS_BAR();
  STAMP(3);
#pragma unroll
  for (int m = 0; m < 2; ++m)
#pragma unroll
    for (int n = 0; n < 2; ++n) {
      const int row0 = wm * 64 + m * 32, col0 = wn * 64 + (n ^ wm) * 32;
      unsigned pk[8];
      const float s0 = VT ? cs[col0 + r] : ra[row0 + r], s1 = VT ? 0.f : rb[row0 + r];
#pragma unroll
      for (int g = 0; g < 4; ++g) {
        float y[4];
        if (VT) {
          const f32x4 a4 = *(LAS f32x4*)(ra + row0 + 8 * g + 4 * hh), b4 = *(LAS f32x4*)(rb + row0 + 8 * g + 4 * hh);
#pragma unroll
          for (int i = 0; i < 4; ++i) y[i] = fmaf(acc[m][n][4 * g + i], a4[i], b4[i] * s0);
        } else {
          const f32x4 c4 = *(LAS f32x4*)(cs + col0 + 8 * g + 4 * hh);
#pragma unroll
          for (int i = 0; i < 4; ++i) y[i] = fmaf(acc[m][n][4 * g + i], s0, s1 * c4[i]);
        }
        pk[2 * g] = cvtpk(y[0], y[1]); pk[2 * g + 1] = cvtpk(y[2], y[3]);
      }
#pragma unroll
      for (int gp = 0; gp < 2; ++gp) {
        const u32x4 v = swap32_pair(pk[4 * gp], pk[4 * gp + 1], pk[4 * gp + 2], pk[4 * gp + 3]);
        st128_d(c.rs, o1.base[n] + (m * o1.sm + gp * o1.sg), np(v));
      }
    }
}

#define EX(b, i) "v_exp_f32 v[" STR(b) "+" STR(i) "], v[" STR(b) "+" STR(i) "]\n\t"
#define EX3(b, i) EX(b, i) EX(b, i + 1) EX(b, i + 2)
#define EX4(b, i) EX3(b, i) EX(b, i + 3)
#define R_SA 32
#define R_SB 64
#define R_OA 96
#define R_OB 112
#define R_NA 128
#define R_NB 144
#define R_QA 160
#define R_QB 164
#define R_QR 168
#define R_KR 170
#define R_K 172
#define R_VA 176
#define R_VB 184
#define R_IDX 192
#define R_KAD 193
#define R_KRAD 194
#define R_VAD 195
#define PV(o, vreg, sreg) "v_smfmac_f32_32x32x32_bf16 " VR(o, 16) ", " VR(vreg, 4) ", " VR(sreg, 8) ", v" STR(R_IDX) "\n\t"
#define QK16(dst, q, nm) "v_mfma_f32_32x32x16_bf16 " VR(dst, 16) ", " VR(R_K, 4) ", " VR(q, 4) ", " VR(nm, 16) "\n\t"
#define STEP(C, N, VC, VN, AB, i, MID, KRLOAD) \
  QK16(N, R_QA, R_NA) \
  MID \
  EX4(C, 0) \
  PV(R_OB, VN + 4, N + 24) \
  "ds_read_b128 " VR(VN, 4) ", v" STR(R_VAD) " offset:" STR(i * 1408) "\n\t" \
  "ds_read_b128 " VR(VN + 4, 4) ", v" STR(R_VAD) " offset:" STR(i * 1408 + 704) "\n\t" \
  EX3(C, 4) \
  QK16(N + 16, R_QB, R_NB) \
  "ds_read_b128 " VR(R_K, 4) ", v" STR(R_KAD) " offset:" STR((i / 2) * 3072 + (i & 1) * 512) "\n\t" \
  EX4(C, 7) \
  PV(R_OA, VC, C) \
  "s_waitcnt lgkmcnt(3)\n\t" \
  EX4(C, 11) \
  "v_mfma_f32_32x32x4_2b_bf16 " VR(N, 32) ", " VR(R_KR, 2) ", " VR(R_QR, 2) ", " VR(N, 32) " cbsz:1 abid:" STR(AB) "\n\t" \
  EX4(C, 15) EX4(C, 19) \
  PV(R_OA, VC + 4, C + 8) \
  "s_waitcnt lgkmcnt(0)\n\t" \
  KRLOAD \
  EX4(C, 23) \
  PV(R_OB, VC, C + 16) \
  EX4(C, 27) EX(C, 31)
#define KRL(i) "ds_read_b64 " VR(R_KR, 2) ", v" STR(R_KRAD) " offset:" STR((i / 2) * 3072) "\n\t"
#define STAGE_TOP \
  "s_barrier\n\t" \
  "v_add_u32 v" STR(R_KAD) ", %[dk], v" STR(R_KAD) "\n\tv_add_u32 v" STR(R_KRAD) ", %[dk], v" STR(R_KRAD) "\n\tv_add_u32 v" STR(R_VAD) ", %[dv], v" STR(R_VAD) "\n\t" \
  "s_sub_u32 %[dk], 0, %[dk]\n\ts_sub_u32 %[dv], 0, %[dv]\n\t"

DI f32x32 cat16(const f32x16& a, const f32x16& b) {
  return __builtin_shufflevector(a, b, 0, 1, 2, 3, 4, 5, 6, 7, 8, 9, 10, 11, 12, 13, 14, 15, 16, 17, 18, 19, 20, 21, 22, 23, 24, 25, 26, 27, 28, 29, 30, 31);
}
DI float max16(const f32x16& s) { float m = s[0];
#pragma unroll
  for (int i = 1; i < 16; ++i) m = fmaxf(m, s[i]);
  return m; }
DI f32x16 splat16(float v) { f32x16 s;
#pragma unroll
  for (int i = 0; i < 16; ++i) s[i] = v;
  return s; }
DI bool out_of_range(const f32x16& o) {
  float chk = 0.f;
#pragma unroll
  for (int i = 0; i < 12; ++i) chk += fabsf(o[i]);
  return !(chk < 1e37f) || !(upper_half(o[8]) > 0.f);
}

DI void p2_compute(const Ctx& c) {
  const int l = c.l, w = c.w, r = l & 31, hh = l >> 5, h = c.h;
  const int e = h & 1, cb = 5 * (h >> 1) + 2 * e, q0 = c.qb * 256 + w * 64;
  unsigned kad = c.lds0 + KRING + 12288 + (e + hh) * 1024 + r * 16, krad = c.lds0 + KRING + 12288 + (e ? 0 : 2048) + l * 16 + 8 * e;
  unsigned vad = c.lds0 + VRING + 11264 + hh * 352 + (r < 21 ? r : 21) * 16;
  LDS_BAR();
  s16x8 qa, qb; s16x4 qr;
#pragma unroll 1
  for (unsigned n = 0; n < SPIN_CAP; ++n) {
    FENCE();
    const u32x4 ta = ld128_d(c.rs, WS_QK + ((cb + e + hh) * 2048 + q0 + r) * 16), tb = ld128_d(c.rs, WS_QK + ((cb + e + hh) * 2048 + q0 + 32 + r) * 16);
    const u32x2 tr = ld64_d(c.rs, WS_QK + ((e ? cb : cb + 2) * 2048 + q0 + l) * 16 + 8 * e);
    qa = __builtin_bit_cast(s16x8, ta); qb = __builtin_bit_cast(s16x8, tb); qr = __builtin_bit_cast(s16x4, tr);
    if (!__any(umax(umax(umax4(ta), umax4(tb)), umax(tr[0], tr[1])) == POISON)) break;
  }
  publish<0>(c, L_CNT1, 4, WS_F1 + c.b * 8);
  STAMP(5);
  LDS_BAR();
  STAMP(6);
  s16x4 kr = *(const LAS s16x4*)((lptr)(size_t)krad + 3 * 3072);
  s16x8 kf = ld128((lptr)(size_t)kad + 3 * 3072);
  f32x32 sa = {}, sb = {};
  sa = __builtin_amdgcn_mfma_f32_32x32x4bf16_1k(kr, qr, sa, 1, 0, 0);
  f32x16 s0 = mfma32(kf, qa, lo16(sa)), s1 = mfma32(kf, qb, hi16(sa));
  kf = ld128((lptr)(size_t)kad + 3 * 3072 + 512);
  f32x8 va = {}, vb = {};
  {
    const u32x4 t0 = *(const LAS u32x4*)((lptr)(size_t)vad + 7 * 1408), t1 = *(const LAS u32x4*)((lptr)(size_t)vad + 7 * 1408 + 704);
#pragma unroll
    for (int i = 0; i < 4; ++i) { va[i] = __uint_as_float(t0[i]); va[4 + i] = __uint_as_float(t1[i]); }
  }
  f32x16 na = splat16(-swap32_max(max16(s0))), nb = splat16(-swap32_max(max16(s1))), oa = {}, ob = {};
  sa = cat16(s0 + na, s1 + nb);
  unsigned idx = 0xDDDDDDDDu, cnt = 8, dk = -12288, dv = -11264;
  STAMP(7);
  asm volatile(
      "s_waitcnt lgkmcnt(0)\n\ts_nop 4\n"
      "1:\n\t"
      STEP(R_SA, R_SB, R_VA, R_VB, 1, 0, STAGE_TOP, KRL(0))
      STEP(R_SB, R_SA, R_VB, R_VA, 0, 1, "", "")
      STEP(R_SA, R_SB, R_VA, R_VB, 1, 2, "", KRL(2))
      STEP(R_SB, R_SA, R_VB, R_VA, 0, 3, "", "")
      STEP(R_SA, R_SB, R_VA, R_VB, 1, 4, "", KRL(4))
      STEP(R_SB, R_SA, R_VB, R_VA, 0, 5, "", "")
      STEP(R_SA, R_SB, R_VA, R_VB, 1, 6, "", KRL(6))
      STEP(R_SB, R_SA, R_VB, R_VA, 0, 7, "", "")
      "s_sub_u32 %[cnt], %[cnt], 1\n\t"
      "s_cmp_lg_u32 %[cnt], 0\n\t"
      "s_cbranch_scc1 1b\n\t"
      "s_waitcnt lgkmcnt(0)\n\t"
      "s_barrier\n\t"
      PV(R_OB, R_VB + 4, R_SB + 24)
      "s_nop 15\n\t"
      : "+{v[32:63]}"(sa), "+{v[64:95]}"(sb), "+{v[96:111]}"(oa), "+{v[112:127]}"(ob), "+{v[170:171]}"(kr), "+{v[172:175]}"(kf), "+{v[176:183]}"(va), "+{v[184:191]}"(vb),
        "+{v193}"(kad), "+{v194}"(krad), "+{v195}"(vad), [cnt] "+s"(cnt), [dk] "+s"(dk), [dv] "+s"(dv)
      : "{v[128:143]}"(na), "{v[144:159]}"(nb), "{v[160:163]}"(qa), "{v[164:167]}"(qb), "{v[168:169]}"(qr), "{v192}"(idx)
      : "memory", "scc");
  STAMP(8);
  if (__any(out_of_range(oa) || out_of_range(ob))) {
    const unsigned ko = WS_QK + ((80 + cb + e + hh) * 2048 + r) * 16, kro = WS_QK + ((80 + cb + (e ? 0 : 2)) * 2048 + r) * 16 + 8 * e;
    const unsigned vg = WS_V + (h * 256 + hh) * 320 + (r < 20 ? r : 0) * 16, pad = r == 20 ? ONES2 : 0u;
    float ma = -INFINITY, mb = -INFINITY;
#pragma unroll 1
    for (int pass = 0; pass < 2; ++pass) {
      na = splat16(pass ? -ma : 0.f); nb = splat16(pass ? -mb : 0.f); oa = splat16(0.f); ob = splat16(0.f);
#pragma unroll 1
      for (int t = 0; t < 64; ++t) {
        const s16x8 k16 = __builtin_bit_cast(s16x8, ld128_d(c.rs, ko + t * 512));
        f32x32 s = cat16(mfma32(k16, qa, na), mfma32(k16, qb, nb));
        s = __builtin_amdgcn_mfma_f32_32x32x4bf16_1k(__builtin_bit_cast(s16x4, ld64_d(c.rs, kro + t * 512)), qr, s, 0, 0, 0);
        if (pass == 0) { ma = fmaxf(ma, max16(lo16(s))); mb = fmaxf(mb, max16(hi16(s))); }
        else {
#pragma unroll
          for (int g = 0; g < 2; ++g) {
            u32x4 vf = ld128_d(c.rs, vg + (4 * t + 2 * g) * 320);
            if (r >= 20) vf = u32x4{pad, pad, pad, pad};
            f32x8 pa, pb;
#pragma unroll
            for (int i = 0; i < 8; ++i) { pa[i] = __builtin_amdgcn_exp2f(s[8 * g + i]); pb[i] = __builtin_amdgcn_exp2f(s[16 + 8 * g + i]); }
            oa = __builtin_amdgcn_smfmac_f32_32x32x32_bf16(__builtin_bit_cast(bf16x8, vf), __builtin_bit_cast(bf16x16, pa), oa, 0xDDDD, 0, 0);
            ob = __builtin_amdgcn_smfmac_f32_32x32x32_bf16(__builtin_bit_cast(bf16x8, vf), __builtin_bit_cast(bf16x16, pb), ob, 0xDDDD, 0, 0);
          }
        }
      }
      if (pass == 0) { ma = swap32_max(ma); mb = swap32_max(mb); }
    }
  }
#pragma unroll
  for (int t = 0; t < 2; ++t) {
    const f32x16& o = t ? ob : oa;
    const float rl = __builtin_amdgcn_rcpf(upper_half(o[8]));
    unsigned pk[6];
#pragma unroll
    for (int i = 0; i < 6; ++i) pk[i] = cvtpk(o[2 * i] * rl, o[2 * i + 1] * rl);
    const unsigned og = WS_O + (q0 >> 6) * 81920 + (32 * t + r) * 16;
    st128_d(c.rs, og + (cb + e + hh) * 1024, np(swap32_pair(pk[0], pk[1], pk[2], pk[3])));
    if (hh == 0) st64_d(c.rs, og + (e ? cb : cb + 2) * 1024 + 8 * e, u32x2{np(pk[4]), np(pk[5])});
  }
}

#define G_ACC 32
#define G_B0 72
#define G_B1 92
#define G_A 112
#define G_VB 160
#define G_VBO 161
#define G_VA0 162
#define G_VA1 163
#define G_CHK 164
#define GMM(m, nt, B, j) "v_mfma_f32_16x16x32_bf16 " VR(G_ACC + m * 20 + nt * 4, 4) ", " VR(B + nt * 4, 4) ", " VR(G_A + j * 8 + m * 4, 4) ", " VR(G_ACC + m * 20 + nt * 4, 4) "\n\t"
#define GRB(B, nt, ks) "ds_read_b64_tr_b16 " VR(B + nt * 4, 2) ", v" STR(G_VB) " offset:" STR(ks * 5120 + nt * 32) "\n\t" \
                       "ds_read_b64_tr_b16 " VR(B + nt * 4 + 2, 2) ", v" STR(G_VB) " offset:" STR(ks * 5120 + nt * 32 + 1280) "\n\t"
#define GCK(j, i) "v_max3_u32 v" STR(G_CHK) ", v[" STR(G_A + j * 8 + i * 2) "], v[" STR(G_A + j * 8 + i * 2 + 1) "], v" STR(G_CHK) "\n\t"
#define GLD1(j, m, va) "buffer_load_dwordx4 " VR(G_A + (j) * 8 + m * 4, 4) ", v" STR(va) ", %[rs], %[so] offen offset:" STR(m * 256) " sc1\n\t"
#define GADV "s_add_u32 %[so], %[so], 16384\n\ts_cmp_eq_u32 %[so], %[sw]\n\ts_cselect_b32 %[so], 0, %[so]\n\t"
#define GLD2(j) GLD1(j, 0, G_VA0) GLD1(j, 1, G_VA0) GLD1(j + 1, 0, G_VA1) GLD1(j + 1, 1, G_VA1) GADV
#define GNEXT "s_add_u32 %[sl], %[sl], 20480\n\ts_cmp_eq_u32 %[sl], %[lw]\n\ts_cselect_b32 %[sl], %[lr], %[sl]\n\tv_add_u32 v" STR(G_VB) ", %[sl], v" STR(G_VBO) "\n\t"
#define GBODY(j, BC, BN, ks, VM, L0, L1) \
  "s_waitcnt vmcnt(" STR(VM) ")\n\t" \
  GMM(0, 0, BC, j) GRB(BN, 0, ks) GMM(0, 1, BC, j) GRB(BN, 1, ks) GMM(0, 2, BC, j) GRB(BN, 2, ks) GMM(0, 3, BC, j) GCK(j, 0) GCK(j, 1) GMM(0, 4, BC, j) L0 \
  GMM(1, 0, BC, j) GRB(BN, 3, ks) GMM(1, 1, BC, j) GRB(BN, 4, ks) GMM(1, 2, BC, j) GCK(j, 2) GMM(1, 3, BC, j) GCK(j, 3) GMM(1, 4, BC, j) L1
#define GEVEN(j) "s_waitcnt lgkmcnt(0)\n\t" GBODY(j, G_B0, G_B1, 1, 10, GLD1(j, 0, G_VA0), GLD1(j, 1, G_VA0))
#define GODD(j, BAR) "s_waitcnt lgkmcnt(0)\n\t" BAR GNEXT GBODY(j, G_B1, G_B0, 0, 10, GLD1(j, 0, G_VA1), GLD1(j, 1, G_VA1) GADV)
#define TEVEN(j) "s_waitcnt lgkmcnt(0)\n\t" GBODY(j, G_B0, G_B1, 1, 10 - 2 * j, "", "")
#define TODD(j) "s_waitcnt lgkmcnt(0)\n\t" GNEXT GBODY(j, G_B1, G_B0, 0, 10 - 2 * j, "", "")

DI void g80_compute(const Ctx& c, int last, u32x2 (&res)[5]) {
  int l = c.l; asm volatile("" : "+v"(l));
  const int w = c.w, wm = w & 1, wk = w >> 1, g = l >> 4, q = (l & 15) >> 2, pp = l & 3;
  const unsigned tile = last ? WS_F + c.rt * 196608 : WS_O + c.rt * 81920;
  __builtin_amdgcn_s_barrier();
  __builtin_amdgcn_s_sleep(PDELAY);
  {
    const int hd = l & 31, e = hd & 1, cb = 5 * (hd >> 1) + 2 * e;
    const unsigned po = tile + 63 * 16 + (last ? (hd >> 2) * 24 + (hd & 3) * 6 + 5 : (e ? cb : cb + 2)) * 1024 + (last ? 0 : 8 * e);
#pragma unroll 1
    for (unsigned n = 0; n < SPIN_CAP; ++n) { FENCE(); if (!__any(ld32_d(c.rs, po) == POISON)) break; __builtin_amdgcn_s_sleep(2); }
  }
  if (last) STAMP(16); else STAMP(10);
  const unsigned va0 = tile + (8 * wk + g) * 1024 + (32 * wm + (l & 15)) * 16;
  f32x32 acc0 = {}; f32x8 acc1 = {};
  const unsigned r0 = last ? c.rot5 : (5 * c.cs) >> 3;
  unsigned chk = 0, so = r0 * 16384, sl = last ? 0 : L_WO + r0 * 20480, cnt = last ? 3 : 1;
  asm volatile(
      "s_cmp_eq_u32 %[cnt], 1\n\ts_cbranch_scc1 4f\n\t"
      GLD2(0)
      "4:\n\t"
      GLD2(2) GLD2(4)
      "s_cmp_eq_u32 %[cnt], 1\n\ts_cbranch_scc0 4f\n\t"
      GLD2(0)
      "4:\n\t" TS(0)
      "s_barrier\n\t" TS(1)
      "v_add_u32 v" STR(G_VB) ", %[sl], v" STR(G_VBO) "\n\t"
      GRB(G_B0, 0, 0) GRB(G_B0, 1, 0) GRB(G_B0, 2, 0) GRB(G_B0, 3, 0) GRB(G_B0, 4, 0)
      "s_cmp_eq_u32 %[cnt], 1\n\ts_cbranch_scc1 2f\n"
      "1:\n\t"
      GEVEN(0) GODD(1, "")
      "2:\n\t"
      GEVEN(2) GODD(3, "") GEVEN(4)
      GODD(5, "s_barrier\n\t")
      "s_sub_u32 %[cnt], %[cnt], 1\n\ts_cmp_lg_u32 %[cnt], 0\n\ts_cbranch_scc1 1b\n\t"
      TEVEN(0) TODD(1) TEVEN(2) TODD(3) TEVEN(4)
      "s_waitcnt vmcnt(0) lgkmcnt(0)\n\ts_barrier\n\t"
      GMM(0, 0, G_B1, 5) GMM(1, 0, G_B1, 5) GCK(5, 0) GMM(0, 1, G_B1, 5) GCK(5, 1) GMM(1, 1, G_B1, 5) GCK(5, 2) GMM(0, 2, G_B1, 5) GCK(5, 3)
      GMM(1, 2, G_B1, 5) GMM(0, 3, G_B1, 5) GMM(1, 3, G_B1, 5) GMM(0, 4, G_B1, 5) GMM(1, 4, G_B1, 5)
      "s_nop 15\n\t" TS(5)
      : "+{v[32:63]}"(acc0), "+{v[64:71]}"(acc1), "+{v164}"(chk), [so] "+s"(so), [sl] "+s"(sl), [cnt] "+s"(cnt)
      : "{v161}"(c.lds0 + (64 * wk + 16 * (g >> 1) + 4 * (g & 1) + q) * 160 + pp * 8), "{v162}"(va0), "{v163}"(va0 + 4096), [rs] "s"(c.rs),
        [sw] "s"(last ? 196608 : 81920), [lw] "s"(last ? 143360 : L_WO + 102400), [lr] "s"(last ? 0 : L_WO)
      : "memory", "scc", "v160",
        "v72", "v73", "v74", "v75", "v76", "v77", "v78", "v79", "v80", "v81", "v82", "v83", "v84", "v85", "v86", "v87", "v88", "v89", "v90", "v91", "v92", "v93", "v94", "v95",
        "v96", "v97", "v98", "v99", "v100", "v101", "v102", "v103", "v104", "v105", "v106", "v107", "v108", "v109", "v110", "v111", "v112", "v113", "v114", "v115", "v116", "v117", "v118", "v119",
        "v120", "v121", "v122", "v123", "v124", "v125", "v126", "v127", "v128", "v129", "v130", "v131", "v132", "v133", "v134", "v135", "v136", "v137", "v138", "v139", "v140", "v141", "v142", "v143",
        "v144", "v145", "v146", "v147", "v148", "v149", "v150", "v151", "v152", "v153", "v154", "v155", "v156", "v157", "v158", "v159" TS_CLOB);
  if (last) { TS_PUT(0, 25); TS_PUT(1, 26); TS_PUT(2, 27); TS_PUT(3, 28); TS_PUT(4, 29); TS_PUT(5, 30); } else { TS_PUT(0, 19); TS_PUT(1, 20); TS_PUT(4, 21); TS_PUT(5, 22); }
  f32x4 acc[2][5];
#pragma unroll
  for (int i = 0; i < 40; ++i) acc[i / 20][(i % 20) / 4][i % 4] = i < 32 ? acc0[i] : acc1[i - 32];
  if (__any(chk == POISON)) {
    const char* wg = (last ? c.p.w2 : c.p.wo) + c.cs * 160 + (l & 15) * 2;
#pragma unroll
    for (int i = 0; i < 10; ++i) acc[i / 5][i % 5] = f32x4{};
#pragma unroll 1
    for (int st = 0; st < (last ? 24 : 10); ++st) {
      u32x4 a0, a1;
#pragma unroll 1
      for (unsigned n = 0; n < SPIN_CAP; ++n) {
        FENCE();
        a0 = ld128_d(c.rs, va0 + (st >> 1) * 16384 + (st & 1) * 4096); a1 = ld128_d(c.rs, va0 + (st >> 1) * 16384 + (st & 1) * 4096 + 256);
        if (!__any(umax(umax4(a0), umax4(a1)) == POISON)) break;
      }
      const char* wr = wg + (size_t)((st >> 1) * 128 + 64 * wk + (st & 1) * 32 + 8 * g) * 1280;
#pragma unroll
      for (int nt = 0; nt < 5; ++nt) {
        s16x8 bf;
#pragma unroll
        for (int j = 0; j < 8; ++j) bf[j] = *(const GAS short*)(wr + j * 1280 + nt * 32);
        acc[0][nt] = mfma16(bf, __builtin_bit_cast(s16x8, a0), acc[0][nt]); acc[1][nt] = mfma16(bf, __builtin_bit_cast(s16x8, a1), acc[1][nt]);
      }
    }
  }
  lptr ex = c.lds + L_EX + l * 16;
#pragma unroll
  for (int nt = 0; nt < 5; ++nt) *(LAS f32x4*)(ex + w * 5120 + nt * 1024) = wk ? acc[0][nt] : acc[1][nt];
  LDS_BAR();
  if (last) STAMP(17); else STAMP(11);
#pragma unroll
  for (int nt = 0; nt < 5; ++nt) {
    const f32x4 u = (wk ? acc[1][nt] : acc[0][nt]) + *(LAS f32x4*)(ex + (w ^ 2) * 5120 + nt * 1024);
    res[nt] = u32x2{np(cvtpk(u[0] + bflo(res[nt][0]), u[1] + bfhi(res[nt][0]))), np(cvtpk(u[2] + bflo(res[nt][1]), u[3] + bfhi(res[nt][1])))};
  }
  const int row = 32 * wm + 16 * wk + (l & 15);
  if (!last) {
    float s1 = 0.f, s2 = 0.f;
#pragma unroll
    for (int nt = 0; nt < 5; ++nt) { s1 = dot2(res[nt][0], ONES2, dot2(res[nt][1], ONES2, s1)); s2 = dot2(res[nt][0], res[nt][0], dot2(res[nt][1], res[nt][1], s2)); }
    s1 = swap16_add(s1, s1); s2 = swap16_add(s2, s2); s1 = swap32_add(s1, s1); s2 = swap32_add(s2, s2);
    if (l < 16) st64_d(c.rs, WS_RS2 + (c.rt * 8 + c.cs) * 512 + row * 8, u32x2{np(__float_as_uint(s1)), np(__float_as_uint(s2))});
  }
  const rsrc_t ro = __builtin_amdgcn_make_buffer_rsrc(last ? c.p.out : c.p.ws + WS_H, 0, S * D * 2, 0x00020000);
  const unsigned ob = c.rt * 81920 + (last ? c.cs * 160 + row * 1280 : c.cs * 10240 + row * 16), oc = last ? 16 : 1024;
#pragma unroll
  for (int i = 0; i < 3; ++i) {
    const int n1 = i < 2 ? 2 * i + 1 : 4, chunk = 4 * i + (i < 2 ? 2 * (g & 1) : 0) + (g >> 1);
    st128_d(ro, ob + chunk * oc, swap16_pair(res[2 * i][0], res[2 * i][1], res[n1][0], res[n1][1]));
  }
}

#define F_X 80
#define F_Y 108
#define FMM(S, nt, m) "v_mfma_f32_16x16x32_bf16 " VR(32 + nt * 16 + m * 4, 4) ", " VR(S + nt * 4, 4) ", " VR(S + 12 + m * 4, 4) ", " VR(32 + nt * 16 + m * 4, 4) "\n\t"
#define FRA(S, m, ks) "ds_read_b128 " VR(S + 12 + m * 4, 4) ", v140 offset:" STR(ks * 4096 + m * 256) "\n\t"
#define FRB(S, nt, h, ks) "ds_read_b64_tr_b16 " VR(S + nt * 4 + h * 2, 2) ", v[141+" STR(nt) "] offset:" STR(ks * 12288 + h * 3072) "\n\t"
DI void p4_compute(const Ctx& c) {
  int l = c.l; asm volatile("" : "+v"(l));
  const int w = c.w, g = l >> 4, q = (l & 15) >> 2, pp = l & 3;
  unsigned boff[3];
#pragma unroll
  for (int nt = 0; nt < 3; ++nt) boff[nt] = c.lds0 + (16 * (g >> 1) + 4 * (g & 1) + q) * 384 + (((3 * w + nt) ^ (2 * (g & 1) + (q >> 1))) * 32) + pp * 8;
  f32x32 ac0 = {}; f32x16 ac1 = {};
  unsigned ch = 0, sa, sb = 0;
  asm volatile(
      "v_mov_b32 v140, v136\n\tv_mov_b32 v141, v137\n\tv_mov_b32 v142, v138\n\tv_mov_b32 v143, v139\n\t"
      "s_barrier\n\t" TS(0)
      FRB(F_Y, 0, 0, 0) FRB(F_Y, 0, 1, 0) FRA(F_Y, 0, 0) FRA(F_Y, 1, 0) FRA(F_Y, 2, 0) FRA(F_Y, 3, 0) FRB(F_Y, 1, 0, 0) FRB(F_Y, 1, 1, 0) FRB(F_Y, 2, 0, 0) FRB(F_Y, 2, 1, 0)
      "s_branch 2f\n"
      "1:\n\t"
      FMM(F_X, 0, 2) FRB(F_Y, 0, 0, 0) FMM(F_X, 0, 3) FRB(F_Y, 0, 1, 0) FMM(F_X, 1, 0) FRA(F_Y, 0, 0) FMM(F_X, 1, 1) FRA(F_Y, 1, 0) FMM(F_X, 1, 2) FRA(F_Y, 2, 0)
      FMM(F_X, 1, 3) FRA(F_Y, 3, 0) FMM(F_X, 2, 0) FRB(F_Y, 1, 0, 0) FMM(F_X, 2, 1) FRB(F_Y, 1, 1, 0) FMM(F_X, 2, 2) FRB(F_Y, 2, 0, 0) FMM(F_X, 2, 3) FRB(F_Y, 2, 1, 0)
      "2:\n\t"
      WL(7) FMM(F_Y, 0, 0) FRB(F_X, 0, 0, 1) WL(7) FMM(F_Y, 0, 1) FRB(F_X, 0, 1, 1) WL(7) FMM(F_Y, 0, 2) FRA(F_X, 0, 1) WL(7) FMM(F_Y, 0, 3) FRA(F_X, 1, 1)
      WL(6) FMM(F_Y, 1, 0) FRA(F_X, 2, 1) "s_add_u32 %[ch], %[ch], 1\n\t" FMM(F_Y, 1, 1) FRA(F_X, 3, 1) "s_add_u32 %[sb], %[sb], 24576\n\t"
      FMM(F_Y, 1, 2) FRB(F_X, 1, 0, 1) "s_cmp_eq_u32 %[ch], 5\n\t" FMM(F_Y, 1, 3) FRB(F_X, 1, 1, 1)
      WL(8) FMM(F_Y, 2, 0) FRB(F_X, 2, 0, 1) "s_cselect_b32 %[sb], 0, %[sb]\n\t" FMM(F_Y, 2, 1) FRB(F_X, 2, 1, 1) "s_and_b32 %[sa], %[ch], 3\n\t"
      FMM(F_Y, 2, 2) "s_lshl_b32 %[sa], %[sa], 13\n\tv_add_u32 v141, %[sb], v137\n\t" FMM(F_Y, 2, 3) "v_add_u32 v142, %[sb], v138\n\t"
      WL(7) FMM(F_X, 0, 0) "v_add_u32 v143, %[sb], v139\n\t" WL(6) FMM(F_X, 0, 1) "v_add_u32 v140, %[sa], v136\n\ts_cmp_eq_u32 %[ch], 10\n\t"
      WL(0) "s_barrier\n\t"
      "s_cbranch_scc0 1b\n\t"
      FMM(F_X, 0, 2) FMM(F_X, 0, 3) FMM(F_X, 1, 0) FMM(F_X, 1, 1) FMM(F_X, 1, 2) FMM(F_X, 1, 3) FMM(F_X, 2, 0) FMM(F_X, 2, 1) FMM(F_X, 2, 2) FMM(F_X, 2, 3)
      "s_nop 15"
      : "+{v[32:63]}"(ac0), "+{v[64:79]}"(ac1), [ch] "+s"(ch), [sa] "=&s"(sa), [sb] "+s"(sb)
      : "{v136}"(c.lds0 + L4_A + g * 1024 + (l & 15) * 16), "{v137}"(boff[0]), "{v138}"(boff[1]), "{v139}"(boff[2])
      : "memory", "scc", "v140", "v141", "v142", "v143", CL8(80, 81, 82, 83, 84, 85, 86, 87), CL8(88, 89, 90, 91, 92, 93, 94, 95), CL8(96, 97, 98, 99, 100, 101, 102, 103), CL8(104, 105, 106, 107, 108, 109, 110, 111),
        CL8(112, 113, 114, 115, 116, 117, 118, 119), CL8(120, 121, 122, 123, 124, 125, 126, 127), CL8(128, 129, 130, 131, 132, 133, 134, 135) TS_CLOB);
  TS_PUT(0, 13);
  f32x4 acc[4][3];
#pragma unroll
  for (int i = 0; i < 48; ++i) acc[(i >> 2) & 3][i >> 4][i & 3] = i < 32 ? ac0[i] : ac1[i - 32];
  LAS float* ra = (LAS float*)(c.lds + L_RA); LAS float* rb = ra + 64; LAS float* csum = (LAS float*)(c.lds + L_CSUM);
  float a[4], b[4]; f32x4 c4[3];
#pragma unroll
  for (int m = 0; m < 4; ++m) { a[m] = ra[16 * m + (l & 15)]; b[m] = rb[16 * m + (l & 15)]; }
#pragma unroll
  for (int nt = 0; nt < 3; ++nt) c4[nt] = *(LAS f32x4*)(csum + w * 48 + nt * 16 + 4 * g);
  STAMP(14);
#pragma unroll
  for (int nt = 0; nt < 3; ++nt)
#pragma unroll
    for (int mp = 0; mp < 2; ++mp) {
      unsigned pk[2][2];
#pragma unroll
      for (int j = 0; j < 2; ++j) {
        const int m = 2 * mp + j; float y[4];
#pragma unroll
        for (int i = 0; i < 4; ++i) y[i] = fmaxf(fmaf(acc[m][nt][i], a[m], b[m] * c4[nt][i]), 0.f);
        pk[j][0] = cvtpk(y[0], y[1]); pk[j][1] = cvtpk(y[2], y[3]);
      }
      st128_d(c.rs, WS_F + c.rt * 196608 + (c.cs * 24 + w * 6 + nt * 2 + (g >> 1)) * 1024 + (32 * mp + (g & 1) * 16 + (l & 15)) * 16, swap16_pair(pk[0][0], pk[0][1], pk[1][0], pk[1][1]));
    }
}

DI void compute_waves(Ctx& c) {
  const Params& p = c.p;
  const int l = c.l, w = c.w, b = c.b, r = l & 31, hh = l >> 5;
  {
    const int xrow = c.xr0 + 2 * w + hh, rg = c.wi & 7, cg = 15 * (b & 1) + (c.wi >> 3);
    LDS_BAR();
    lptr xs = c.lds + L1_XS + (2 * w + hh) * 1280 + r * 16, ws = c.lds + L1_WS + (w * 64 + l) * 16;
    const u32x4 xv[3] = {*(LAS u32x4*)xs, *(LAS u32x4*)(xs + 512), *(LAS u32x4*)(xs + 1024)};
    u32x4 wv[3] = {*(LAS u32x4*)ws, *(LAS u32x4*)(ws + 4096), {}};
    if (w < 2) wv[2] = *(LAS u32x4*)(ws + 8192);
    row_stats(c, xv, xrow);
    colsum_step1(c.lds + L_PART, l, w, wv);
    LDS_BAR();
    const float v = colsum_step2(c.lds + L_PART, l, w);
    if (l < 16 && c.ct < 15) st64_d(c.rs, WS_CS1 + (rg * 1920 + cg * 64 + 16 * w + l) * 8, u32x2{__float_as_uint(v), (unsigned)c.tag});
    STAMP(1);
    if (c.ct < 10) p1_compute<false>(c);
    else if (c.ct < 15) p1_compute<true>(c);
  }
  STAMP(4);
  p2_compute(c);
  STAMP(9);
  u32x2 res[5];
  {
    const char* xg = p.x + (size_t)((c.rt * 64 + 32 * (w & 1) + 16 * (w >> 1) + (l & 15)) * 1280 + c.cs * 160 + (l >> 4) * 8);
#pragma unroll
    for (int nt = 0; nt < 5; ++nt) res[nt] = *(const GAS u32x2*)(xg + nt * 32);
  }
  int last = 0;
#pragma unroll 1
  for (;;) {
    asm volatile("" : "+s"(last));
    g80_compute(c, last, res);
    if (last) break;
    STAMP(12);
    __builtin_amdgcn_s_barrier();
    p4_compute(c);
    STAMP(15);
    asm volatile(".Lblk_end1:");
    last = 1;
  }
  STAMP(18);
  asm volatile(".Lblk_end2:");
}

struct Park { f32x32 a, b, c, d, e; f32x16 f; f32x4 g; };
#define PARK_REGS(m, pk) m "{v[76:107]}"(pk.a), m "{v[108:139]}"(pk.b), m "{v[140:171]}"(pk.c), m "{v[172:203]}"(pk.d), m "{v[204:235]}"(pk.e), m "{v[236:251]}"(pk.f), m "{v[252:255]}"(pk.g)
#define PKL1(k, j, o) "buffer_load_dwordx4 " VR(76 + k * 20 + j * 4, 4) ", %[v" #j "], %[rs], %[so] offen offset:" STR(o) "\n\t"
#define PKL(k, ch) "s_add_u32 %[so], %[r5], " STR(ch * 163840 - 1966080) "\n\ts_cselect_b32 %[so], %[so], " STR(ch * 163840) "\n\ts_cselect_b32 %[t], 0, %[r5]\n\ts_add_u32 %[so], %[so], %[t]\n\t" PKL1(k, 0, 0) PKL1(k, 1, 1024) PKL1(k, 2, 2048) PKL1(k, 3, 3072) PKL1(k, 4, 0)
#define W80_OPS(r) [rs] "s"(r), [v0] "v"(vo80[0]), [v1] "v"(vo80[1]), [v2] "v"(vo80[2]), [v3] "v"(vo80[3]), [v4] "v"(vo80[4])
#define W80_DMA(r, dst, off) asm volatile("s_mov_b32 m0, %[d]\n\ts_nop 0\n\t" \
    "buffer_load_dwordx4 %[v0], %[rs], %[so] offen lds\n\tbuffer_load_dwordx4 %[v1], %[rs], %[so] offen offset:1024 lds\n\t" \
    "buffer_load_dwordx4 %[v2], %[rs], %[so] offen offset:2048 lds\n\tbuffer_load_dwordx4 %[v3], %[rs], %[so] offen offset:3072 lds\n\t" \
    "s_add_u32 m0, %[d], 4096\n\ts_nop 0\n\tbuffer_load_dwordx4 %[v4], %[rs], %[so] offen lds" :: [d] "s"(dst), [so] "s"(off), W80_OPS(r) : "memory", "scc")
#define PKS1(k, j) "ds_write_b128 %[va], " VR(76 + k * 20 + j * 4, 4) " offset:" STR(j * 1024) "\n\t"
#define PUT(k, slot) asm volatile(PKS1(k, 0) PKS1(k, 1) PKS1(k, 2) PKS1(k, 3) PKS1(k, 4) :: [va] "v"(wa + slot * 20480), PARK_REGS("", pk) : "memory")
#define P1_STATE [sa] "+s"(sa), [sb] "+s"(sb), [md] "+s"(md)
#define P1_CONST [rx] "s"(rx), [rw] "s"(rw), [l0] "s"(lds0 + lw * 4096), [a0] "v"(va[0]), [a1] "v"(va[1]), [a2] "v"(va[2]), [a3] "v"(va[3]), [b0] "v"(vb[0]), [b1x] "v"(vb[1]), [b2] "v"(vb[2]), [b3] "v"(vb[3])
#define P1_LD(v, r, s, o) "buffer_load_dwordx4 %[" v "], %[" r "], %[" s "] offen offset:" #o " lds\n\t"
#define P1_STAGE \
  "s_mov_b32 m0, %[md]\n\ts_add_u32 %[md], %[md], 16384\n\t" \
  P1_LD("a0", "rx", "sa", 0) P1_LD("a1", "rx", "sa", 1024) P1_LD("a2", "rx", "sa", 2048) P1_LD("a3", "rx", "sa", 3072) \
  "s_mov_b32 m0, %[md]\n\ts_add_u32 %[sa], %[sa], 128\n\t" \
  P1_LD("b0", "rw", "sb", 0) P1_LD("b1x", "rw", "sb", 1024) P1_LD("b2", "rw", "sb", 2048) P1_LD("b3", "rw", "sb", 3072) \
  "s_add_u32 %[sb], %[sb], 245760\n\ts_cmp_eq_u32 %[sa], 1280\n\ts_cselect_b32 %[sa], 0, %[sa]\n\ts_cselect_b32 %[sb], 0, %[sb]\n\t" \
  "s_sub_u32 %[md], %[md], %[l0]\n\ts_add_u32 %[md], %[md], 16384\n\ts_and_b32 %[md], %[md], 0x18000\n\ts_add_u32 %[md], %[md], %[l0]\n\t"
#define W1_OPS [rw] "s"(rw1), [dw] "s"(dw), [v0] "v"(vo[0]), [v1] "v"(vo[1]), [v2] "v"(vo[2]), [v3] "v"(vo[3]), [v4] "v"(vo[4]), [v5] "v"(vo[5])
#define LW1(v, o) "buffer_load_dwordx4 %[" v "], %[rw], %[t0] offen offset:" STR(o) " lds\n\t"
#define LW3_0 LW1("v0", 0) LW1("v1", 1024) LW1("v2", 2048)
#define LW3_1 LW1("v3", 0) LW1("v4", 1024) LW1("v5", 2048)
#define LW3(ch, u) "s_add_u32 m0, %[dw], " STR((ch - 5 * (ch / 5)) * 24576 + u * 3072) "\n\ts_mov_b32 %[t0], " STR(ch * 196608) "\n\t" LW3_##u
#define LA(ch) "s_add_u32 m0, %[da], " STR((ch & 3) * 8192) "\n\ts_mov_b32 %[t0], " STR(ch * 8192) "\n\t" \
  "buffer_load_dwordx4 %[va], %[rs], %[t0] offen sc1 lds\n\tbuffer_load_dwordx4 %[va], %[rs], %[t0] offen offset:1024 sc1 lds\n\t"
#define CKR(ch) "ds_read_b128 v[60:63], %[la] offset:" STR((ch & 3) * 8192) "\n\tds_read_b128 v[64:67], %[la] offset:" STR((ch & 3) * 8192 + 1024) "\n\t"
#define CKT "s_waitcnt lgkmcnt(0)\n\tv_max3_u32 v60, v60, v61, v62\n\tv_max3_u32 v64, v63, v64, v65\n\tv_max3_u32 v60, v60, v66, v67\n\tv_max_u32 v60, v60, v64\n\tv_cmp_eq_u32 vcc, -1, v60\n\t"
#define FIX(ch) "s_cbranch_vccz 2f\n\ts_mov_b32 %[t1], 0\n1:\n\ts_sleep 2\n\t" LA(ch) "s_waitcnt vmcnt(0)\n\t" CKR(ch) CKT \
  "s_cbranch_vccz 2f\n\ts_add_u32 %[t1], %[t1], 1\n\ts_cmp_lt_u32 %[t1], 0x40000\n\ts_cbranch_scc1 1b\n2:\n\t"
#define IVL(ch, VA, FETCH, VW) "s_waitcnt vmcnt(" STR(VA) ")\n\t" CKR(ch) FETCH CKT FIX(ch) "s_waitcnt vmcnt(" STR(VW) ")\n\t"
DI u64 poisoned(lptr p) { return __builtin_amdgcn_ballot_w64(umax4(*(const LAS u32x4*)p) == POISON); }

DI void loader_waves(Ctx& c) {
  const Params& p = c.p;
  const int l = c.l, lw = c.w - 4, b = c.b, t4 = lw * 64 + l, cs = c.cs, rt = c.rt;
  const unsigned lds0 = c.lds0, lin = l * 16;
  {
    const int rot = (5 * ((c.ct + 2 * c.panel) & 7)) >> 2;
    unsigned va[4], vb[4], sa = rot * 128, sb = rot * 245760, md = lds0 + 65536 + lw * 4096;
#pragma unroll
    for (int i = 0; i < 4; ++i) {
      va[i] = (32 * lw + 8 * i + (l >> 3)) * 1280 + (((l & 7) ^ ((4 * i + (l >> 4)) & 7)) << 4) - i * 1024;
      vb[i] = (16 * lw + 4 * i + (l >> 4)) * 3840 + (((l & 15) ^ (((l >> 4) & 3) << 2)) << 4) - i * 1024;
    }
    const rsrc_t rx = __builtin_amdgcn_make_buffer_rsrc((void*)(p.x + c.panel * 163840), 0, 163840, 0x00020000);
    const rsrc_t rw = __builtin_amdgcn_make_buffer_rsrc((void*)(p.wqkv + c.ct * 256), 0, 640 * 3840 - c.ct * 256, 0x00020000);
    STAMP(0);
    {
      const char* src = lw < 2 ? p.x + (c.xr0 * 1280 + lw * 5120) : p.wqkv + (((c.wi & 7) * 80 + (lw - 2) * 40) * 3840 + (15 * (b & 1) + (c.wi >> 3)) * 128);
      const unsigned vo = lw < 2 ? lin : (l >> 3) * 3840 + (l & 7) * 16, st = lw < 2 ? 1024 : 8 * 3840;
#pragma unroll
      for (int i = 0; i < 5; ++i) glds(src + i * st, vo, lds0 + L1_XS + lw * 5120 + i * 1024);
    }
    if (c.ct < 15) asm volatile("s_sleep " STR(SDELAY) "\n\t" P1_STAGE "s_waitcnt vmcnt(8)\n\ts_barrier\n\t" P1_STAGE : P1_STATE : P1_CONST : "memory", "scc");
    else wait_bar<0>();
    STAMP(2);
    {
      const char* pc; unsigned len;
      asm volatile("s_getpc_b64 %0\n.Lblk_pc:\n\ts_mov_b32 %1, .Lblk_end1-.Lblk_pc\n\ts_max_i32 %1, %1, .Lblk_end2-.Lblk_pc" : "=s"(pc), "=s"(len) :: "scc");
      const unsigned off = (((b >> 3) * 4 + lw) * 4 + (l & 3)) * 128;
      touch(pc, off < len ? off : 0u, lds0 + L_TOUCH);
    }
    if (c.ct < 15) {
      const O1 o1 = p1_places(c, lw);
      unsigned t, n = 0;
#define P1_PSN(b, s) "buffer_store_dwordx4 %[ps], %[" b "], %[rs], " s " offen sc1\n\t"
#define P1_POISON "s_add_u32 %[t], %[sm], %[sg]\n\t" P1_PSN("p0", "0") P1_PSN("p0", "%[sg]") P1_PSN("p0", "%[sm]") P1_PSN("p0", "%[t]") P1_PSN("p1", "0") P1_PSN("p1", "%[sg]") P1_PSN("p1", "%[sm]") P1_PSN("p1", "%[t]")
      asm volatile(
                   "s_waitcnt vmcnt(9)\n\ts_barrier\n\t" P1_STAGE P1_POISON "s_waitcnt vmcnt(16)\n\ts_barrier\n\t" P1_STAGE "s_waitcnt vmcnt(16)\n"
                   "1:\n\ts_barrier\n\t" P1_STAGE
                   "s_add_u32 %[n], %[n], 1\n\ts_cmp_lt_u32 %[n], 6\n\ts_waitcnt vmcnt(8)\n\ts_cbranch_scc1 1b"
                   : P1_STATE, [t] "=&s"(t), [n] "+s"(n)
                   : P1_CONST, [p0] "v"(o1.base[0]), [p1] "v"(o1.base[1]), [sm] "s"(o1.sm), [sg] "s"(o1.sg), [rs] "s"(c.rs), [ps] "v"(u32x4{POISON, POISON, POISON, POISON}) : "memory", "scc");
      STAMP(5);
      glds_d(p.ws + WS_CS1 + (2 * lw * 1920 + c.ct * 128) * 8, lin, lds0 + L1_CG + lw * 2048);
      glds_d(p.ws + WS_CS1 + ((2 * lw + 1) * 1920 + c.ct * 128) * 8, lin, lds0 + L1_CG + lw * 2048 + 1024);
      glds_m(p.ws + WS_RS1 + (c.panel * 128 + 32 * lw) * 16, lin, lds0 + L1_RG + lw * 512, 0xFFFFFFFFull);
    }
    if (lw == 0 && l < 4) *(LAS unsigned*)(c.lds + L_CNT0 + l * 4) = 0u;
    wait_bar<11>();
    publish<11>(c, L_CNT0, 4, WS_F0 + b * 8);
    {
      const unsigned pv = (l & 1) ? 0u : ONES2;
      if (l < 32) *(LAS u32x4*)(c.lds + VRING + (16 * lw + (l >> 1)) * 352 + 320 + (l & 1) * 16) = u32x4{pv, pv, pv, pv};
    }
    if (c.ct < 15) wait_bar<0>();
    STAMP(6);
    if (c.ct < 15) {
      LAS float* ra = (LAS float*)(c.lds + L1_FIN);
      const unsigned tag = (unsigned)c.tag;
      if (lw < 2) {
        u32x4 g = *(LAS u32x4*)(c.lds + L1_RG + t4 * 16);
#pragma unroll 1
        for (unsigned n = 0; (g[1] != tag || g[3] != tag) && n < SPIN_CAP; ++n) { FENCE(); g = ld128_d(c.rs, WS_RS1 + (c.panel * 128 + t4) * 16); }
        const float sc = c.ct < 5 ? QSCALE : 1.f;
        ra[t4] = __uint_as_float(g[0]) * sc; ra[128 + t4] = __uint_as_float(g[2]) * sc;
      } else {
        const int n0 = t4 - 128; float sum = 0.f; bool ok = true;
#pragma unroll
        for (int i = 0; i < 8; ++i) { const u32x2 g = *(LAS u32x2*)(c.lds + L1_CG + i * 1024 + n0 * 8); sum += __uint_as_float(g[0]); ok = ok && g[1] == tag; }
        if (__any(!ok)) {
          sum = 0.f;
#pragma unroll 1
          for (int i = 0; i < 8; ++i) {
            u32x2 g = {0u, ~tag};
#pragma unroll 1
            for (unsigned n = 0; g[1] != tag && n < SPIN_CAP; ++n) { FENCE(); g = ld64_d(c.rs, WS_CS1 + (i * 1920 + c.ct * 128 + n0) * 8); }
            sum += __uint_as_float(g[0]);
          }
        }
        ra[256 + n0] = sum;
      }
    }
    if (c.ct < 15) LDS_BAR();
  }
  Park pk;
  const rsrc_t rw2 = __builtin_amdgcn_make_buffer_rsrc((void*)(p.w2 + cs * 160), 0, FF * D * 2 - cs * 160, 0x00020000);
  unsigned vo80[5];
  {
    const int h = c.h, e = h & 1, cb = 5 * (h >> 1) + 2 * e;
    unsigned vk[3], vv[3]; u64 mv[3], mt[2];
#pragma unroll
    for (int i = 0; i < 3; ++i) {
      const int n = 64 * (3 * lw + i) + l, kg = n / 22, dd = n - 22 * kg;
      vk[i] = WS_QK + ((80 + cb + i) * 2048 + 64 + 64 * lw - 256) * 16 + lin - i * 1024;
      vv[i] = WS_V + (h * 256 + 4 - 32 + kg) * 320 + dd * 16 - i * 1024;
      mv[i] = __builtin_amdgcn_ballot_w64(dd < 20 && kg < 32);
      if (i < 2) mt[i] = __builtin_amdgcn_ballot_w64(dd < 20 && kg >= 28);
    }
    auto issue = [&](int s, bool withk, u64 m0, u64 m1, u64 m2) {
      unsigned t;
      if (withk) asm volatile("s_mov_b32 m0, %[d]\n\ts_lshl_b32 %[t], %[s], 12\n\t"
                              "buffer_load_dwordx4 %[v0], %[rs], %[t] offen sc1 lds\n\tbuffer_load_dwordx4 %[v1], %[rs], %[t] offen offset:1024 sc1 lds\n\tbuffer_load_dwordx4 %[v2], %[rs], %[t] offen offset:2048 sc1 lds"
                              : [t] "=&s"(t) : [d] "s"(lds0 + KRING + (s & 1) * 12288 + lw * 3072), [s] "s"(s + 1), [rs] "s"(c.rs), [v0] "v"(vk[0]), [v1] "v"(vk[1]), [v2] "v"(vk[2]) : "memory", "scc");
      asm volatile("s_mov_b32 m0, %[d]\n\ts_mul_i32 %[t], %[s], 10240\n\t"
                   "s_mov_b64 exec, %[m0]\n\tbuffer_load_dwordx4 %[v0], %[rs], %[t] offen sc1 lds\n\t"
                   "s_mov_b64 exec, %[m1]\n\ts_cbranch_execz 1f\n\tbuffer_load_dwordx4 %[v1], %[rs], %[t] offen offset:1024 sc1 lds\n\t"
                   "s_mov_b64 exec, %[m2]\n\ts_cbranch_execz 1f\n\tbuffer_load_dwordx4 %[v2], %[rs], %[t] offen offset:2048 sc1 lds\n1:\n\ts_mov_b64 exec, -1"
                   : [t] "=&s"(t) : [d] "s"(lds0 + VRING + (s & 1) * 11264 + lw * 3072), [s] "s"(s + 1), [rs] "s"(c.rs), [v0] "v"(vv[0]), [v1] "v"(vv[1]), [v2] "v"(vv[2]),
                     [m0] "s"(m0), [m1] "s"(m1), [m2] "s"(m2) : "memory");
    };
    auto bad = [&](int s, u64 m0, u64 m1, u64 m2) {
      lptr k = c.lds + KRING + (s & 1) * 12288 + lw * 3072 + lin, v = c.lds + VRING + (s & 1) * 11264 + lw * 3072 + lin;
      return (poisoned(k) | poisoned(k + 1024) | poisoned(k + 2048) | (poisoned(v) & m0) | (poisoned(v + 1024) & m1) | (poisoned(v + 2048) & m2)) != 0;
    };
#pragma unroll
    for (int i = 0; i < 5; ++i) { const int n = 64 * i + l, rr = n / 10; vo80[i] = swap23(32 * lw + rr) * 1280 + (n - 10 * rr) * 16 - (i & 3) * 1024; }
    const rsrc_t rwo = __builtin_amdgcn_make_buffer_rsrc((void*)(p.wo + cs * 160), 0, D * D * 2 - cs * 160, 0x00020000);
    const unsigned xw = (b >> 3) * 4 + lw, vt60 = (l < 60 ? l : 0) * 256;
    u32x4 psn = {POISON, POISON, POISON, POISON};
    asm volatile("" : "+v"(psn));
    const int b1 = b < 192 ? b : 0, rg1 = b1 / 24, cg1 = b1 - 24 * rg1;
    unsigned w1o = (t4 >> 3) * 3072 + (l & 7) * 16, cs2o = WS_CS2 + (rg1 * 1536 + cg1 * 64 + 16 * lw + l) * 4;
    const char* bw = l < 32 ? ((l & 16) ? p.w2 : p.w1) + ((b * 4 + lw) * 15 + umin(l & 15, 14)) * 128 : p.wo + umin((b * 4 + lw) * 7 + umin(l - 32, 6), 6399) * 128;
    asm volatile("" : "+v"(w1o), "+v"(cs2o), "+v"(bw));
    if (lw == 0) poll256(p.ws, WS_F0 + l * 8, c.tag);
    STAMP(7);
    LDS_BAR();
#pragma unroll 1
    for (unsigned n = 0; n < SPIN_CAP; ++n) {
      if (lw == 3) issue(-1, true, mt[0], mt[1], 0);
      issue(0, true, mv[0], mv[1], mv[2]);
      if (lw != 3) break;
      wait_vm<5>();
      if (!bad(-1, mt[0], mt[1], 0)) break;
      DRAIN();
    }
    STAMP(8);
    LDS_BAR();
    STAMP(20);
#pragma unroll 1
    for (unsigned n = 0; n < SPIN_CAP; ++n) {
      DRAIN();
      if (!bad(0, mv[0], mv[1], mv[2])) break;
      issue(0, true, mv[0], mv[1], mv[2]);
    }
    STAMP(9);
    __builtin_amdgcn_s_setprio(3);
#pragma unroll 1
    for (int s = 1; s < 5; ++s) {
      LDS_BAR(); STAMPV(20 + s);
      issue(s, true, mv[0], mv[1], mv[2]);
#pragma unroll
      for (int k = 0; k < 3; ++k) {
        const int q = 12 * lw + 3 * (s - 1) + k;
        asm volatile("buffer_store_dwordx4 %0, %1, %2, %3 offen sc1" :: "v"(psn), "v"(lin), "s"(c.rs),
                     "s"(q < 10 ? WS_O + rt * 81920 + cs * 10240 + q * 1024 : q < 20 ? WS_H + rt * 81920 + cs * 10240 + (q - 10) * 1024 : q < 44 ? WS_F + rt * 196608 + cs * 24576 + (q - 20) * 1024 : WS_RS2 + (rt * 8 + cs) * 512) : "memory");
      }
      if (s == 1) {
        wait_vm<3>();
#pragma unroll 1
        for (unsigned n = 0; bad(1, mv[0], mv[1], mv[2]) && n < SPIN_CAP; ++n) { issue(1, true, mv[0], mv[1], mv[2]); DRAIN(); }
        if (lw == 0) {
          poll256(p.ws, WS_F1 + l * 8, c.tag);
          if (b == 0 && l == 0) stflag(p.ws, WS_EPOCH, c.tag);
        }
        touch64(bw, lds0 + L_TOUCH);
      } else {
#pragma unroll 1
        for (int k = 2 * s - 4; k < (s < 4 ? 2 * s - 2 : 5); ++k)
        { const int kr = k + (rt & 3) < 5 ? k + (rt & 3) : k + (rt & 3) - 5; W80_DMA(rwo, lds0 + L_WO + kr * 20480 + lw * 5120, kr * 163840); }
        if (s == 4) wait_vm<8>();
        wait_vm<13>();
        if (s == 3) {
          const char* src = p.w1 + (rg1 * 80 * 3072 + cg1 * 128);
          glds(src, w1o, lds0 + L_W1B + lw * 1024); glds(src + 32 * 3072, w1o, lds0 + L_W1B + 4096 + lw * 1024);
          if (lw < 2) glds(src + 64 * 3072, w1o, lds0 + L_W1B + 8192 + lw * 1024);
        }
      }
      STAMPV(9 + s);
    }
    u32x4 w1m[3] = {*(LAS u32x4*)(c.lds + L_W1B + t4 * 16), *(LAS u32x4*)(c.lds + L_W1B + 4096 + t4 * 16), {}};
    if (lw < 2) w1m[2] = *(LAS u32x4*)(c.lds + L_W1B + 8192 + t4 * 16);
    colsum_step1(c.lds + L_PART + 1024, l, lw, w1m);
    unsigned so, st;
#define PARK2(m, k0, c0, k1, c1) asm volatile(PKL(k0, c0) PKL(k1, c1) : PARK_REGS(m, pk), [so] "=&s"(so), [t] "=&s"(st) : W80_OPS(rw2), [r5] "s"(c.rot5 * 163840) : "memory", "scc")
    LDS_BAR(); STAMP(25);
    {
      const float cs2 = colsum_step2(c.lds + L_PART + 1024, l, lw);
      if (l < 16 && b < 192) st32_d(c.rs, cs2o, __float_as_uint(cs2));
    }
    issue(5, true, mv[0], mv[1], mv[2]);
    PARK2("=&", 0, 0, 1, 1);
    wait_bar<10>(); STAMP(26);
    publish<10>(c, L_CNTP, 4, WS_FP + b * 8);
    issue(6, true, mv[0], mv[1], mv[2]);
    PARK2("+", 2, 2, 3, 3);
    wait_bar<10>(); STAMP(27);
    issue(7, lw != 3, lw == 3 ? mv[0] & ~mt[0] : mv[0], lw == 3 ? 0 : mv[1], mv[2]);
    PARK2("+", 4, 7, 5, 8);
    wait_bar<10>(); STAMP(28);
    if (lw == 0) poll256(p.ws, WS_FP + l * 8, c.tag);
    PARK2("+", 6, 9, 7, 10);
    if (lw == 1) {
      const int i = l & 7, k = l >> 3;
      touch64(k == 0 ? p.ws + (WS_O + c.qb * 327680 + i * 40960) : k == 1 ? p.ws + (WS_O + rt * 81920 + i * 10240) : k == 2 ? p.ws + (WS_H + rt * 81920 + i * 10240) : k == 3 ? p.ws + (WS_F + rt * 196608 + i * 24576)
              : k == 4 ? p.out + (rt * 81920 + i * 10240) : k == 5 ? p.x + (rt * 81920 + i * 10240) : p.ws + (WS_RS2 + rt * 4096 + i * 512), lds0 + L_TOUCH);
    }
    touch(p.w1 + xw * 15360, vt60, lds0 + L_TOUCH);
    touch(p.w1 + (xw * 15360 + 128), vt60, lds0 + L_TOUCH);
  }
  {
    unsigned vo[6];
#pragma unroll
    for (int i = 0; i < 3; ++i) {
      const int n = 64 * i + l, rr = n / 24, pos = n - 24 * rr;
      vo[i] = ((rr & 3) + 8 * (rr >> 2)) * 3072 + ((((pos >> 1) ^ ((rr >> 1) & 3)) << 1) | (pos & 1)) * 16 - i * 1024;
      vo[3 + i] = vo[i] + 4 * 3072;
    }
    const rsrc_t rw1 = __builtin_amdgcn_make_buffer_rsrc((void*)(p.w1 + cs * 384 + lw * 16 * 3072), 0, D * FF * 2 - cs * 384 - lw * 16 * 3072, 0x00020000);
    const unsigned dw = lds0 + lw * 6144, da = lds0 + L4_A + lw * 2048, va = WS_H + rt * 81920 + lw * 2048 + lin;
    const unsigned so = WS_RS2 + (rt * 8 + (l >> 4)) * 512 + (16 * lw + (l & 15)) * 8;
    unsigned t0, t1r; u32x2 s0, s1;
    LDS_BAR();
    STAMP(17);
    LDS_BAR();
    asm volatile(LW3(0, 0) LW3(0, 1) LW3(1, 0) LW3(1, 1) : [t0] "=&s"(t0) : W1_OPS : "memory");
    if (lw < 3) {
      float t = 0.f;
#pragma unroll
      for (int i = 0; i < 8; ++i) t += __uint_as_float(ld32_d(c.rs, WS_CS2 + (i * 1536 + cs * 192 + t4) * 4));
      *(LAS float*)(c.lds + L_CSUM + t4 * 4) = t;
    }
    asm volatile(PKL(8, 11) : PARK_REGS("+", pk), [so] "=&s"(t0), [t] "=&s"(t1r) : W80_OPS(rw2), [r5] "s"(c.rot5 * 163840) : "memory", "scc");
    LDS_BAR(); LDS_BAR(); LDS_BAR(); LDS_BAR();
    STAMP(18);
    LDS_BAR();
    asm volatile(
        LW3(2, 0) LW3(2, 1) LW3(3, 0) LW3(3, 1)
        "s_mov_b32 %[t1], 0\n"
        "1:\n\ts_sleep " STR(ADELAY) "\n\t" LA(0) LA(1) LA(2) LA(3)
        "s_waitcnt vmcnt(6)\n\t" CKR(0) CKT
        "s_cbranch_vccz 2f\n\ts_waitcnt vmcnt(0)\n\ts_add_u32 %[t1], %[t1], 1\n\ts_cmp_lt_u32 %[t1], 0x40000\n\ts_cbranch_scc1 1b\n"
        "2:\n\ts_barrier\n\t"
        IVL(1, 4, LW3(4, 0) LW3(4, 1), 63) "s_barrier\n\t"
        IVL(2, 8, LA(4) LW3(5, 0), 63) "s_barrier\n\t"
        IVL(3, 11, LA(5) LW3(5, 1), 63) "s_barrier\n\t"
        IVL(4, 8, LA(6) LW3(6, 0) LW3(6, 1), 63) "s_barrier\n\t"
        IVL(5, 11, LA(7) LW3(7, 0), 13) "s_barrier\n\t"
        IVL(6, 11, LA(8) LW3(7, 1) LW3(8, 0), 13) "s_barrier\n\t"
        IVL(7, 11, LA(9) LW3(8, 1), 8) "s_barrier\n\t"
        IVL(8, 11, "buffer_load_dwordx2 %[s0], %[so], %[rs], 0 offen sc1\n\tbuffer_load_dwordx2 %[s1], %[so], %[rs], 0 offen offset:2048 sc1\n\t" LW3(9, 0) LW3(9, 1), 8) "s_barrier\n\t"
        IVL(9, 11, "", 6)
        : [t0] "=&s"(t0), [t1] "=&s"(t1r), [s0] "=&v"(s0), [s1] "=&v"(s1)
        : W1_OPS, [rs] "s"(c.rs), [da] "s"(da), [va] "v"(va), [la] "v"(da + lin), [so] "v"(so) : "memory", "scc", "vcc", "v60", "v61", "v62", "v63", "v64", "v65", "v66", "v67");
    STAMP(29);
#pragma unroll 1
    for (unsigned n = 0; __any(umax(umax(s0[0], s0[1]), umax(s1[0], s1[1])) == POISON) && n < SPIN_CAP; ++n) { FENCE(); s0 = ld64_d(c.rs, so); s1 = ld64_d(c.rs, so + 2048); }
    float t1 = __uint_as_float(s0[0]) + __uint_as_float(s1[0]), t2 = __uint_as_float(s0[1]) + __uint_as_float(s1[1]);
    t1 = swap16_add(t1, t1); t2 = swap16_add(t2, t2); t1 = swap32_add(t1, t1); t2 = swap32_add(t2, t2);
    const float mean = t1 * (1.f / D), var = fmaxf(t2 * (1.f / D) - mean * mean, 0.f), a = __builtin_amdgcn_rsqf(var + LN_EPS);
    if (l < 32) *(LAS float*)(c.lds + L_RA + (l >> 4) * 256 + (16 * lw + (l & 15)) * 4) = l < 16 ? a : -mean * a;
    wait_bar<0>();
  }
  {
    const unsigned wa = lds0 + lw * 5120 + lin;
    LDS_BAR();
    STAMP(30);
    PUT(0, 0); PUT(1, 1); PUT(2, 2);
    LDS_BAR();
#pragma unroll 1
    for (int k = 4; k < 7; ++k) W80_DMA(rw2, lds0 + k * 20480 + lw * 5120, (k + c.rot5 < 12 ? k + c.rot5 : k + c.rot5 - 12) * 163840);
    PUT(3, 3);
    STAMP(31);
    LDS_BAR();
    wait_bar<0>(); PUT(4, 0); PUT(5, 1); PUT(6, 2);
    LDS_BAR(); PUT(7, 3); PUT(8, 4);
    LDS_BAR(); LDS_BAR(); LDS_BAR();
  }
}

__global__ void __launch_bounds__(512, 2) block_kernel(Params p) {
  extern __shared__ __attribute__((aligned(16))) char smem[];
  Ctx c;
  c.p = p; c.lds = (lptr)smem; c.lds0 = (unsigned)(size_t)c.lds; c.l = threadIdx.x & 63; c.w = rfl(threadIdx.x >> 6); c.b = blockIdx.x;
  const int xcd = c.b & 7, j = c.b >> 3;
  const int t1 = 30 * (xcd & 1) + j;
  c.panel = 4 * (xcd >> 1) + (t1 & 3); c.ct = j < 30 ? t1 >> 2 : 15;
  c.h = c.rt = 4 * xcd + (j >> 3); c.qb = c.cs = j & 7;
  c.xr0 = 512 * (xcd >> 1) + 8 * (32 * (xcd & 1) + j); c.wi = j < 30 ? (xcd >> 1) * 30 + j : 0;
  c.rot5 = (3 * (j >> 3) + ((3 * c.cs) >> 1)) % 12;
  c.st_lo = c.st_hi = 0;
  c.rs = __builtin_amdgcn_make_buffer_rsrc(p.ws, 0, WS_END, 0x00020000);
  c.tag = *(const __attribute__((address_space(4))) u64*)(p.ws + WS_EPOCH) + 1;
  if (__builtin_expect(c.w >= 4, 1)) loader_waves(c);
  else { STAMP(0); compute_waves(c); }
}

extern "C" void kernel_launch(void* const* d_in, const int* in_sizes, int n_in, void* d_out, int out_size, void* d_ws, size_t ws_size, hipStream_t stream) {
  Params p;
  p.x = (const char*)d_in[0]; p.wqkv = (const char*)d_in[1]; p.wo = (const char*)d_in[2]; p.w1 = (const char*)d_in[4]; p.w2 = (const char*)d_in[5];
  p.out = (char*)d_out; p.ws = (char*)d_ws;
  (void)hipFuncSetAttribute((const void*)block_kernel, hipFuncAttributeMaxDynamicSharedMemorySize, LDS_BYTES);
  block_kernel<<<256, 512, LDS_BYTES, stream>>>(p);
}
```

```cpp
#include <hip/hip_runtime.h>
#include <hip/hip_bf16.h>

#define DI __device__ __forceinline__
#define LAS __attribute__((address_space(3)))
#define GAS __attribute__((address_space(1)))
typedef unsigned long long u64;
typedef __attribute__((ext_vector_type(8))) short s16x8;
typedef __attribute__((ext_vector_type(4))) short s16x4;
typedef __attribute__((ext_vector_type(8))) __bf16 bf16x8;
typedef __attribute__((ext_vector_type(16))) __bf16 bf16x16;
typedef __attribute__((ext_vector_type(2))) __bf16 bf16x2;
typedef __attribute__((ext_vector_type(2))) float f32x2;
typedef __attribute__((ext_vector_type(4))) float f32x4;
typedef __attribute__((ext_vector_type(8))) float f32x8;
typedef __attribute__((ext_vector_type(16))) float f32x16;
typedef __attribute__((ext_vector_type(32))) float f32x32;
typedef __attribute__((ext_vector_type(4))) unsigned u32x4;
typedef __attribute__((ext_vector_type(2))) unsigned u32x2;
typedef LAS char* lptr;
typedef __amdgpu_buffer_rsrc_t rsrc_t;

#define V_BY_HEAD
#define LAG 1
#define SDELAY 10
#define TDELAY 20
#define TSTAGE 4
#define WOT 20
#define KDELAY 24
#define QDELAY 20
#define RDELAY 4
#define LSTW 4
#define PSLEEP 2
#define PDELAY3 8
#define PDELAY 8
#define ADELAY 6
constexpr int S = 2048, D = 640, FF = 1536;
constexpr float LN_EPS = 1e-5f;
constexpr float QSCALE = 0.22360679774997896f * 1.4426950408889634f;

constexpr unsigned WS_EPOCH = 0, WS_F0 = 4096, WS_F1 = 6144, WS_FP = 8192;
constexpr unsigned WS_RS1 = 65536;
constexpr unsigned WS_CS1 = WS_RS1 + 2048 * 16;
constexpr unsigned WS_CS2 = WS_CS1 + 8 * 1920 * 8;
constexpr unsigned WS_QK = WS_CS2 + 8 * 1536 * 4;
constexpr unsigned WS_V = WS_QK + 160 * 2048 * 16;
constexpr unsigned WS_RS2 = WS_V + 256 * 640 * 16;
constexpr unsigned WS_O = WS_RS2 + 32 * 8 * 512;
constexpr unsigned WS_H = WS_O + S * D * 2;
constexpr unsigned WS_F = WS_H + S * D * 2;
constexpr unsigned WS_END = WS_F + S * FF * 2;
constexpr int LDS_BYTES = 163840;
constexpr unsigned SPIN_CAP = 1u << 18;
constexpr unsigned POISON = 0xFFFFFFFFu;
constexpr int L1_CG = 131072, L1_RG = L1_CG + 8192, L1_FIN = L1_RG + 2048;
constexpr int L1_XS = 131072, L1_WS = L1_XS + 10240;
constexpr int KRING = 0, VRING = 24576, L_W1B = 47104, L_WO = 57344;
constexpr int L4_A = 122880, L_EX = 98304;
constexpr int L_CNT0 = 159744, L_CNT1 = L_CNT0 + 4, L_CNTP = L_CNT0 + 8, L_CSUM = L_CNT0 + 256, L_RA = L_CSUM + 768, L_TOUCH = L_RA + 512, L_PART = L_TOUCH + 256;

#define TS(i) ""
#define TS_CLOB
#define TS_PUT(i, k)
#define STAMP(k)
#define STAMPV(k)
struct Params { const char* x; const char* wqkv; const char* wo; const char* w1; const char* w2; char* out; char* ws; };
struct Ctx { Params p; lptr lds; unsigned lds0; rsrc_t rs; u64 tag; int l, w, b, panel, ct, h, qb, rt, cs, rot5, xr0, wi; mutable unsigned st_lo, st_hi; };

DI unsigned rfl(unsigned v) { return __builtin_amdgcn_readfirstlane(v); }
DI unsigned cvtpk(float lo, float hi) { f32x2 v = {lo, hi}; return __builtin_bit_cast(unsigned, __builtin_convertvector(v, bf16x2)); }
DI float bflo(unsigned v) { return __uint_as_float(v << 16); }
DI float bfhi(unsigned v) { return __uint_as_float(v & 0xffff0000u); }
DI void glds(const char* sbase, unsigned voff, unsigned lds) {
  asm volatile("s_mov_b32 m0, %2\n\ts_nop 0\n\tglobal_load_lds_dwordx4 %0, %1" :: "v"(voff), "s"(sbase), "s"(lds) : "memory");
}
DI void glds_d(const char* sbase, unsigned voff, unsigned lds) {
  asm volatile("s_mov_b32 m0, %2\n\ts_nop 0\n\tglobal_load_lds_dwordx4 %0, %1 sc1" :: "v"(voff), "s"(sbase), "s"(lds) : "memory");
}
DI void glds_m(const char* sbase, unsigned voff, unsigned lds, u64 mask) {
  asm volatile("s_mov_b32 m0, %2\n\ts_mov_b64 exec, %3\n\tglobal_load_lds_dwordx4 %0, %1 sc1\n\ts_mov_b64 exec, -1" :: "v"(voff), "s"(sbase), "s"(lds), "s"(mask) : "memory");
}
DI void touch(const char* sbase, unsigned voff, unsigned lds) {
  asm volatile("s_mov_b32 m0, %2\n\ts_nop 0\n\tglobal_load_lds_dword %0, %1" :: "v"(voff), "s"(sbase), "s"(lds) : "memory");
}
#define TSC ""
DI void touch64(const char* a, unsigned lds) { asm volatile("s_mov_b32 m0, %1\n\ts_nop 0\n\tglobal_load_lds_dword %0, off" TSC :: "v"(a), "s"(lds) : "memory"); }
template <int N> DI void wait_vm() { asm volatile("s_waitcnt vmcnt(%0)" :: "i"(N) : "memory"); }
template <int N> DI void wait_bar() { asm volatile("s_waitcnt vmcnt(%0) lgkmcnt(0)\n\ts_barrier" :: "i"(N) : "memory"); }
#define LDS_BAR() asm volatile("s_waitcnt lgkmcnt(0)\n\ts_barrier" ::: "memory")
#define DRAIN() asm volatile("s_waitcnt vmcnt(0)" ::: "memory")
#define STR_(x) #x
#define STR(x) STR_(x)
#define VR(b, n) "v[" STR(b) ":" STR(b) "+" STR(n) "-1]"
#define WL(n) "s_waitcnt lgkmcnt(" #n ")\n\t"
#define CL8(a, b, c, d, e, f, g, h) "v" #a, "v" #b, "v" #c, "v" #d, "v" #e, "v" #f, "v" #g, "v" #h
DI s16x8 ld128(lptr p) { return *(const LAS s16x8*)p; }
DI s16x4 ldtr(lptr p) { return __builtin_amdgcn_ds_read_tr16_b64_v4i16((LAS s16x4*)p); }
DI s16x8 ldtr2(lptr p, int d) { return __builtin_shufflevector(ldtr(p), ldtr(p + d), 0, 1, 2, 3, 4, 5, 6, 7); }
DI f32x16 mfma32(s16x8 a, s16x8 b, f32x16 c) { return __builtin_amdgcn_mfma_f32_32x32x16_bf16(__builtin_bit_cast(bf16x8, a), __builtin_bit_cast(bf16x8, b), c, 0, 0, 0); }
DI f32x4 mfma16(s16x8 a, s16x8 b, f32x4 c) { return __builtin_amdgcn_mfma_f32_16x16x32_bf16(__builtin_bit_cast(bf16x8, a), __builtin_bit_cast(bf16x8, b), c, 0, 0, 0); }
DI float dot2(unsigned a, unsigned b, float c) { return __builtin_amdgcn_fdot2_f32_bf16(__builtin_bit_cast(bf16x2, a), __builtin_bit_cast(bf16x2, b), c, false); }
constexpr unsigned ONES2 = 0x3f803f80u;
DI void st128_d(rsrc_t rs, unsigned off, u32x4 v) { __builtin_amdgcn_raw_buffer_store_b128(v, rs, off, 0, 16); }
DI void st_poison(rsrc_t rs, unsigned off, unsigned soff) {
  asm volatile("buffer_store_dwordx4 %0, %1, %2, %3 offen sc1\n\ts_nop 1" :: "v"(u32x4{POISON, POISON, POISON, POISON}), "v"(off), "s"(rs), "s"(soff) : "memory");
}
DI void st64_d(rsrc_t rs, unsigned off, u32x2 v) { __builtin_amdgcn_raw_buffer_store_b64(v, rs, off, 0, 16); }
DI void st32_d(rsrc_t rs, unsigned off, unsigned v) { __builtin_amdgcn_raw_buffer_store_b32(v, rs, off, 0, 16); }
DI u32x4 ld128_d(rsrc_t rs, unsigned off) { return __builtin_amdgcn_raw_buffer_load_b128(rs, off, 0, 16); }
DI u32x2 ld64_d(rsrc_t rs, unsigned off) { return __builtin_amdgcn_raw_buffer_load_b64(rs, off, 0, 16); }
DI unsigned ld32_d(rsrc_t rs, unsigned off) { return __builtin_amdgcn_raw_buffer_load_b32(rs, off, 0, 16); }
DI u64 ldflag(const char* ws, unsigned off) { return __hip_atomic_load((GAS u64*)(ws + off), __ATOMIC_RELAXED, __HIP_MEMORY_SCOPE_AGENT); }
DI void stflag(char* ws, unsigned off, u64 v) { __hip_atomic_store((GAS u64*)(ws + off), v, __ATOMIC_RELAXED, __HIP_MEMORY_SCOPE_AGENT); }
DI void read_tag(Ctx& c) {
  const char* ws = c.p.ws; asm volatile("" : "+s"(ws));
  c.rs = __builtin_amdgcn_make_buffer_rsrc(c.p.ws, 0, WS_END, 0x00020000);
  c.tag = *(const __attribute__((address_space(4))) u64*)(ws + WS_EPOCH) + 1;
}
DI void poll256(const char* ws, unsigned off, u64 tag) {
#pragma unroll 1
  for (unsigned n = 0; n < SPIN_CAP; ++n) {
    const u64 a = ldflag(ws, off), b = ldflag(ws, off + 512), c = ldflag(ws, off + 1024), d = ldflag(ws, off + 1536);
    if (__all(a == tag && b == tag && c == tag && d == tag)) break;
    __builtin_amdgcn_s_sleep(4);
  }
}
template <int YOUNGER> DI void publish(const Ctx& c, int cnt_off, unsigned n, unsigned flag_off) {
  wait_vm<YOUNGER>();
  if (c.l == 0) {
    const unsigned old = __hip_atomic_fetch_add((LAS unsigned*)(c.lds + cnt_off), 1u, __ATOMIC_RELAXED, __HIP_MEMORY_SCOPE_WORKGROUP);
    if (old == n - 1) stflag(c.p.ws, flag_off, c.tag);
  }
}
template <int CTRL> DI float dpp_add(float v) { return v + __int_as_float(__builtin_amdgcn_update_dpp(0, __float_as_int(v), CTRL, 0xf, 0xf, true)); }
DI float swap16_add(float a, float b) { const auto t = __builtin_amdgcn_permlane16_swap(__float_as_uint(a), __float_as_uint(b), false, false); return __uint_as_float(t[0]) + __uint_as_float(t[1]); }
DI float swap32_add(float a, float b) { const auto t = __builtin_amdgcn_permlane32_swap(__float_as_uint(a), __float_as_uint(b), false, false); return __uint_as_float(t[0]) + __uint_as_float(t[1]); }
DI float swap32_max(float a) { const auto t = __builtin_amdgcn_permlane32_swap(__float_as_uint(a), __float_as_uint(a), false, false); return fmaxf(__uint_as_float(t[0]), __uint_as_float(t[1])); }
DI float upper_half(float a) { const auto t = __builtin_amdgcn_permlane32_swap(__float_as_uint(a), __float_as_uint(a), false, false); return __uint_as_float(t[1]); }
DI u32x4 swap32_pair(unsigned a0, unsigned a1, unsigned b0, unsigned b1) {
  const auto s0 = __builtin_amdgcn_permlane32_swap(a0, b0, false, false), s1 = __builtin_amdgcn_permlane32_swap(a1, b1, false, false);
  return u32x4{s0[0], s1[0], s0[1], s1[1]};
}
DI u32x4 swap16_pair(unsigned a0, unsigned a1, unsigned b0, unsigned b1) {
  const auto s0 = __builtin_amdgcn_permlane16_swap(a0, b0, false, false), s1 = __builtin_amdgcn_permlane16_swap(a1, b1, false, false);
  return u32x4{s0[0], s1[0], s0[1], s1[1]};
}
DI void colsum_step1(lptr part, int l, int w4, const u32x4 (&m)[3]) {
  float v[8], u[4], t[2];
#pragma unroll
  for (int i = 0; i < 4; ++i) { v[2 * i] = bflo(m[0][i]) + bflo(m[1][i]) + bflo(m[2][i]); v[2 * i + 1] = bfhi(m[0][i]) + bfhi(m[1][i]) + bfhi(m[2][i]); }
#pragma unroll
  for (int i = 0; i < 4; ++i) u[i] = swap32_add(v[i], v[i + 4]);
#pragma unroll
  for (int i = 0; i < 2; ++i) t[i] = dpp_add<0x128>(swap16_add(u[i], u[i + 2]));
  *(LAS f32x2*)(part + (w4 * 64 + (l & 7) * 8 + 4 * (l >> 5) + 2 * ((l >> 4) & 1)) * 4) = f32x2{t[0], t[1]};
}
DI float colsum_step2(lptr part, int l, int w4) {
  float v = *(LAS float*)(part + ((l >> 4) * 64 + 16 * w4 + (l & 15)) * 4);
  v = swap16_add(v, v); return swap32_add(v, v);
}
DI unsigned np(unsigned v) { return v < POISON - 1 ? v : POISON - 1; }
DI u32x4 np(u32x4 v) { return u32x4{np(v[0]), np(v[1]), np(v[2]), np(v[3])}; }
DI unsigned umax(unsigned a, unsigned b) { return a > b ? a : b; }
DI unsigned umin(unsigned a, unsigned b) { return a < b ? a : b; }
DI unsigned umax4(u32x4 v) { return umax(umax(v[0], v[1]), umax(v[2], v[3])); }
#define FENCE() asm volatile("" ::: "memory")
DI f32x16 lo16(const f32x32& s) { return __builtin_shufflevector(s, s, 0, 1, 2, 3, 4, 5, 6, 7, 8, 9, 10, 11, 12, 13, 14, 15); }
DI f32x16 hi16(const f32x32& s) { return __builtin_shufflevector(s, s, 16, 17, 18, 19, 20, 21, 22, 23, 24, 25, 26, 27, 28, 29, 30, 31); }
DI int swap23(int k) { return (k & ~12) | ((k & 4) << 1) | ((k & 8) >> 1); }

DI void row_stats(const Ctx& c, const u32x4 (&xv)[3], int xrow) {
  const int r = c.l & 31;
  float s1 = 0.f, s2 = 0.f;
#pragma unroll
  for (int i = 0; i < 3; ++i)
#pragma unroll
    for (int k = 0; k < 4; ++k) { const unsigned v = (i < 2 || r < 16) ? xv[i][k] : 0u; s1 = dot2(v, ONES2, s1); s2 = dot2(v, v, s2); }
  s1 = dpp_add<0x128>(dpp_add<0x141>(dpp_add<0x4E>(dpp_add<0xB1>(s1)))); s2 = dpp_add<0x128>(dpp_add<0x141>(dpp_add<0x4E>(dpp_add<0xB1>(s2))));
  s1 = swap16_add(s1, s1); s2 = swap16_add(s2, s2);
  const float mean = s1 * (1.f / D), var = fmaxf(s2 * (1.f / D) - mean * mean, 0.f), a = __builtin_amdgcn_rsqf(var + LN_EPS);
  const unsigned tag = (unsigned)c.tag;
  if (r == 0) st128_d(c.rs, WS_RS1 + xrow * 16, u32x4{__float_as_uint(a), tag, __float_as_uint(-mean * a), tag});
}
struct O1 { unsigned base[2], sm, sg; };
DI O1 p1_places(const Ctx& c, int w) {
  const int wm = w >> 1, wn = w & 1, r = c.l & 31, hh = c.l >> 5; const bool vt = c.ct >= 10;
  O1 o;
#pragma unroll
  for (int n = 0; n < 2; ++n) {
    const int f = (c.ct - 10) * 128 + wn * 64 + (n ^ wm) * 32 + r, h = f / 20, d = f - 20 * h, dd = (h & 1) ? (d < 4 ? d + 16 : d - 4) : d;
    o.base[n] = vt ? WS_V + (h * 256 + c.panel * 16 + wm * 8 + hh) * 320 + dd * 16 : WS_QK + ((c.ct * 16 + wn * 8 + (n ^ wm) * 4 + hh) * 2048 + c.panel * 128 + wm * 64 + r) * 16;
  }
  o.sm = vt ? 4 * 320 : 512; o.sg = vt ? 2 * 320 : 2 * 32768;
  return o;
}
#define Q_X 96
#define Q_Y 128
#define QMK(S, k, m, n) "v_mfma_f32_32x32x16_bf16 " VR(32 + m * 32 + n * 16, 16) ", " VR(S + 16 + k * 8 + n * 4, 4) ", " VR(S + k * 8 + m * 4, 4) ", " VR(32 + m * 32 + n * 16, 16) "\n\t"
#define QMV(S, k, m, n) "v_mfma_f32_32x32x16_bf16 " VR(32 + m * 32 + n * 16, 16) ", " VR(S + k * 8 + m * 4, 4) ", " VR(S + 16 + k * 8 + n * 4, 4) ", " VR(32 + m * 32 + n * 16, 16) "\n\t"
#define QRA(S, k, m, ks) "ds_read_b128 " VR(S + k * 8 + m * 4, 4) ", v[160+" STR(ks) "] offset:" STR(m * 4096) "\n\t"
#define QRB(S, k, n, h, ks) "ds_read_b64_tr_b16 " VR(S + 16 + k * 8 + n * 4 + h * 2, 2) ", v[164+" STR(n) "] offset:" STR((ks) * 4096 + h * 1024) "\n\t"
#define QR0(S, k, ks) QRA(S, k, 0, ks) QRB(S, k, 0, 0, ks)
#define QR1(S, k, ks) QRB(S, k, 0, 1, ks) QRB(S, k, 1, 0, ks)
#define QR2(S, k, ks) QRB(S, k, 1, 1, ks) QRA(S, k, 1, ks)
#define QAD(i) "v_add_u32 v[160+" #i "], %[dl], v[160+" #i "]\n\t"
#define P1_LOOP(MM) \
  QR0(Q_Y, 0, 0) QR1(Q_Y, 0, 0) QR2(Q_Y, 0, 0) QR0(Q_Y, 1, 1) QR1(Q_Y, 1, 1) QR2(Q_Y, 1, 1) \
  "s_branch 2f\n" \
  "1:\n\t" \
  MM(Q_X, 0, 0, 1) QR0(Q_Y, 0, 0) MM(Q_X, 0, 1, 0) QR1(Q_Y, 0, 0) MM(Q_X, 0, 1, 1) QR2(Q_Y, 0, 0) \
  MM(Q_X, 1, 0, 0) QR0(Q_Y, 1, 1) MM(Q_X, 1, 0, 1) QR1(Q_Y, 1, 1) MM(Q_X, 1, 1, 0) QR2(Q_Y, 1, 1) MM(Q_X, 1, 1, 1) \
  "2:\n\t" \
  WL(9) MM(Q_Y, 0, 0, 0) QR0(Q_X, 0, 2) WL(9) MM(Q_Y, 0, 0, 1) QR1(Q_X, 0, 2) WL(10) MM(Q_Y, 0, 1, 0) QR2(Q_X, 0, 2) \
  MM(Q_Y, 0, 1, 1) QR0(Q_X, 1, 3) WL(11) MM(Q_Y, 1, 0, 0) QR1(Q_X, 1, 3) WL(11) MM(Q_Y, 1, 0, 1) QR2(Q_X, 1, 3) \
  WL(12) MM(Q_Y, 1, 1, 0) "s_add_u32 %[st], %[st], 1\n\ts_and_b32 %[dl], %[st], 3\n\ts_cselect_b32 %[dl], 32768, %[wr]\n\t" \
  MM(Q_Y, 1, 1, 1) QAD(0) QAD(1) \
  WL(9) MM(Q_X, 0, 0, 0) QAD(2) QAD(3) QAD(4) QAD(5) \
  "s_cmp_eq_u32 %[st], 12\n\ts_cbranch_scc1 3f\n\t" \
  WL(0) "s_barrier\n\ts_cmp_lg_u32 %[st], %[fk]\n\ts_cbranch_scc1 1b\n\t" \
  "s_mov_b64 exec, 1\n\tbuffer_store_dwordx2 v[168:169], v166, %[rs], 0 offen sc1\n\ts_mov_b64 exec, -1\n\ts_branch 1b\n" \
  "3:\n\t" WL(0) \
  MM(Q_X, 0, 0, 1) MM(Q_X, 0, 1, 0) MM(Q_X, 0, 1, 1) \
  MM(Q_X, 1, 0, 0) MM(Q_X, 1, 0, 1) MM(Q_X, 1, 1, 0) MM(Q_X, 1, 1, 1) \
  "s_nop 15\n\ts_nop 3"
#define P1_OPERANDS \
  : "+{v[32:63]}"(ac0), "+{v[64:95]}"(ac1), "+{v160}"(aoff[0]), "+{v161}"(aoff[1]), "+{v162}"(aoff[2]), "+{v163}"(aoff[3]), "+{v164}"(boff[0]), "+{v165}"(boff[1]), \
    [st] "+s"(st), [dl] "=&s"(dl) \
  : [wr] "s"(-98304), [fk] "s"(w == 0 ? 5 : 0), [rs] "s"(c.rs), "{v166}"(WS_F0 + c.b * 8), "{v[168:169]}"(c.tag) \
  : "memory", "scc", CL8(96, 97, 98, 99, 100, 101, 102, 103), CL8(104, 105, 106, 107, 108, 109, 110, 111), CL8(112, 113, 114, 115, 116, 117, 118, 119), CL8(120, 121, 122, 123, 124, 125, 126, 127), \
    CL8(128, 129, 130, 131, 132, 133, 134, 135), CL8(136, 137, 138, 139, 140, 141, 142, 143), CL8(144, 145, 146, 147, 148, 149, 150, 151), CL8(152, 153, 154, 155, 156, 157, 158, 159)
template <bool VT>
DI void p1_compute(const Ctx& c) {
  const int l = c.l, w = c.w, wm = w >> 1, wn = w & 1, r = l & 31, hh = l >> 5, q = (l & 15) >> 2, pp = l & 3;
  unsigned aoff[4], boff[2];
#pragma unroll
  for (int i = 0; i < 4; ++i) aoff[i] = c.lds0 + 65536 + (wm * 64 + r) * 128 + (((2 * i + hh) ^ ((r >> 1) & 7)) << 4);
#pragma unroll
  for (int n = 0; n < 2; ++n) boff[n] = c.lds0 + 65536 + 16384 + (8 * hh + q) * 256 + (((wn * 8 + (n ^ wm) * 4 + 2 * ((l >> 4) & 1) + (pp >> 1)) ^ (q << 2)) << 4) + 8 * (pp & 1);
  f32x32 ac0 = {}, ac1 = {};
  unsigned st = 2, dl;
  if (VT) asm volatile(P1_LOOP(QMV) P1_OPERANDS); else asm volatile(P1_LOOP(QMK) P1_OPERANDS);
  const f32x16 acc[2][2] = {{lo16(ac0), hi16(ac0)}, {lo16(ac1), hi16(ac1)}};
  LAS float* ra = (LAS float*)(c.lds + L1_FIN); LAS float* rb = ra + 128; LAS float* cs = ra + 256;
  const O1 o1 = p1_places(c, w);
  LDS_BAR();
  STAMP(3);
#pragma unroll
  for (int m = 0; m < 2; ++m)
#pragma unroll
    for (int n = 0; n < 2; ++n) {
      const int row0 = wm * 64 + m * 32, col0 = wn * 64 + (n ^ wm) * 32;
      unsigned pk[8];
      const float s0 = VT ? cs[col0 + r] : ra[row0 + r], s1 = VT ? 0.f : rb[row0 + r];
#pragma unroll
      for (int g = 0; g < 4; ++g) {
        float y[4];
        if (VT) {
          const f32x4 a4 = *(LAS f32x4*)(ra + row0 + 8 * g + 4 * hh), b4 = *(LAS f32x4*)(rb + row0 + 8 * g + 4 * hh);
#pragma unroll
          for (int i = 0; i < 4; ++i) y[i] = fmaf(acc[m][n][4 * g + i], a4[i], b4[i] * s0);
        } else {
          const f32x4 c4 = *(LAS f32x4*)(cs + col0 + 8 * g + 4 * hh);
#pragma unroll
          for (int i = 0; i < 4; ++i) y[i] = fmaf(acc[m][n][4 * g + i], s0, s1 * c4[i]);
        }
        pk[2 * g] = cvtpk(y[0], y[1]); pk[2 * g + 1] = cvtpk(y[2], y[3]);
      }
#pragma unroll
      for (int gp = 0; gp < 2; ++gp) {
        const u32x4 v = swap32_pair(pk[4 * gp], pk[4 * gp + 1], pk[4 * gp + 2], pk[4 * gp + 3]);
        st128_d(c.rs, o1.base[n] + (m * o1.sm + gp * o1.sg), np(v));
      }
    }
}

#define EX(b, i) "v_exp_f32 v[" STR(b) "+" STR(i) "], v[" STR(b) "+" STR(i) "]\n\t"
#define EX3(b, i) EX(b, i) EX(b, i + 1) EX(b, i + 2)
#define EX4(b, i) EX3(b, i) EX(b, i + 3)
#define R_SA 32
#define R_SB 64
#define R_OA 96
#define R_OB 112
#define R_NA 128
#define R_NB 144
#define R_QA 160
#define R_QB 164
#define R_QR 168
#define R_KR 170
#define R_K 172
#define R_VA 176
#define R_VB 184
#define R_IDX 192
#define R_KAD 193
#define R_KRAD 194
#define R_VAD 195
#define PV(o, vreg, sreg) "v_smfmac_f32_32x32x32_bf16 " VR(o, 16) ", " VR(vreg, 4) ", " VR(sreg, 8) ", v" STR(R_IDX) "\n\t"
#define QK16(dst, q, nm) "v_mfma_f32_32x32x16_bf16 " VR(dst, 16) ", " VR(R_K, 4) ", " VR(q, 4) ", " VR(nm, 16) "\n\t"
#define STEP(C, N, VC, VN, AB, i, MID, KRLOAD) \
  QK16(N, R_QA, R_NA) \
  MID \
  EX4(C, 0) \
  PV(R_OB, VN + 4, N + 24) \
  "ds_read_b128 " VR(VN, 4) ", v" STR(R_VAD) " offset:" STR(i * 1408) "\n\t" \
  "ds_read_b128 " VR(VN + 4, 4) ", v" STR(R_VAD) " offset:" STR(i * 1408 + 704) "\n\t" \
  EX3(C, 4) \
  QK16(N + 16, R_QB, R_NB) \
  "ds_read_b128 " VR(R_K, 4) ", v" STR(R_KAD) " offset:" STR((i / 2) * 3072 + (i & 1) * 512) "\n\t" \
  EX4(C, 7) \
  PV(R_OA, VC, C) \
  "s_waitcnt lgkmcnt(3)\n\t" \
  EX4(C, 11) \
  "v_mfma_f32_32x32x4_2b_bf16 " VR(N, 32) ", " VR(R_KR, 2) ", " VR(R_QR, 2) ", " VR(N, 32) " cbsz:1 abid:" STR(AB) "\n\t" \
  EX4(C, 15) EX4(C, 19) \
  PV(R_OA, VC + 4, C + 8) \
  "s_waitcnt lgkmcnt(0)\n\t" \
  KRLOAD \
  EX4(C, 23) \
  PV(R_OB, VC, C + 16) \
  EX4(C, 27) EX(C, 31)
#define KRL(i) "ds_read_b64 " VR(R_KR, 2) ", v" STR(R_KRAD) " offset:" STR((i / 2) * 3072) "\n\t"
#define STAGE_TOP \
  "s_barrier\n\t" \
  "v_add_u32 v" STR(R_KAD) ", %[dk], v" STR(R_KAD) "\n\tv_add_u32 v" STR(R_KRAD) ", %[dk], v" STR(R_KRAD) "\n\tv_add_u32 v" STR(R_VAD) ", %[dv], v" STR(R_VAD) "\n\t" \
  "s_sub_u32 %[dk], 0, %[dk]\n\ts_sub_u32 %[dv], 0, %[dv]\n\t"

DI f32x32 cat16(const f32x16& a, const f32x16& b) {
  return __builtin_shufflevector(a, b, 0, 1, 2, 3, 4, 5, 6, 7, 8, 9, 10, 11, 12, 13, 14, 15, 16, 17, 18, 19, 20, 21, 22, 23, 24, 25, 26, 27, 28, 29, 30, 31);
}
DI float max16(const f32x16& s) { float m = s[0];
#pragma unroll
  for (int i = 1; i < 16; ++i) m = fmaxf(m, s[i]);
  return m; }
DI f32x16 splat16(float v) { f32x16 s;
#pragma unroll
  for (int i = 0; i < 16; ++i) s[i] = v;
  return s; }
DI bool out_of_range(const f32x16& o) {
  float chk = 0.f;
#pragma unroll
  for (int i = 0; i < 12; ++i) chk += fabsf(o[i]);
  return !(chk < 1e37f) || !(upper_half(o[8]) > 0.f);
}

DI void p2_compute(const Ctx& c) {
  const int l = c.l, w = c.w, r = l & 31, hh = l >> 5, h = c.h;
  const int e = h & 1, cb = 5 * (h >> 1) + 2 * e, q0 = c.qb * 256 + w * 64;
  unsigned kad = c.lds0 + KRING + 12288 + (e + hh) * 1024 + r * 16, krad = c.lds0 + KRING + 12288 + (e ? 0 : 2048) + l * 16 + 8 * e;
  unsigned vad = c.lds0 + VRING + 11264 + hh * 352 + (r < 21 ? r : 21) * 16;
  LDS_BAR();
  __builtin_amdgcn_s_sleep(QDELAY);
  s16x8 qa, qb; s16x4 qr;
#pragma unroll 1
  for (unsigned n = 0; n < SPIN_CAP; ++n) {
    FENCE();
    const u32x4 ta = ld128_d(c.rs, WS_QK + ((cb + e + hh) * 2048 + q0 + r) * 16), tb = ld128_d(c.rs, WS_QK + ((cb + e + hh) * 2048 + q0 + 32 + r) * 16);
    const u32x2 tr = ld64_d(c.rs, WS_QK + ((e ? cb : cb + 2) * 2048 + q0 + l) * 16 + 8 * e);
    qa = __builtin_bit_cast(s16x8, ta); qb = __builtin_bit_cast(s16x8, tb); qr = __builtin_bit_cast(s16x4, tr);
    if (!__any(umax(umax(umax4(ta), umax4(tb)), umax(tr[0], tr[1])) == POISON)) break;
    __builtin_amdgcn_s_sleep(RDELAY);
  }
  publish<0>(c, L_CNT1, 4, WS_F1 + c.b * 8);
  STAMP(5);
#pragma unroll 1
  for (unsigned n = 0; n < SPIN_CAP && !*(volatile LAS unsigned*)(c.lds + L_CNT0); ++n) __builtin_amdgcn_s_sleep(1);
  FENCE();
  STAMP(6);
  s16x4 kr = *(const LAS s16x4*)((lptr)(size_t)krad + 3 * 3072);
  s16x8 kf = ld128((lptr)(size_t)kad + 3 * 3072);
  f32x32 sa = {}, sb = {};
  sa = __builtin_amdgcn_mfma_f32_32x32x4bf16_1k(kr, qr, sa, 1, 0, 0);
  f32x16 s0 = mfma32(kf, qa, lo16(sa)), s1 = mfma32(kf, qb, hi16(sa));
  kf = ld128((lptr)(size_t)kad + 3 * 3072 + 512);
  f32x8 va = {}, vb = {};
  {
    const u32x4 t0 = *(const LAS u32x4*)((lptr)(size_t)vad + 7 * 1408), t1 = *(const LAS u32x4*)((lptr)(size_t)vad + 7 * 1408 + 704);
#pragma unroll
    for (int i = 0; i < 4; ++i) { va[i] = __uint_as_float(t0[i]); va[4 + i] = __uint_as_float(t1[i]); }
  }
  f32x16 na = splat16(-swap32_max(max16(s0))), nb = splat16(-swap32_max(max16(s1))), oa = {}, ob = {};
  sa = cat16(s0 + na, s1 + nb);
  unsigned idx = 0xDDDDDDDDu, cnt = 8, dk = -12288, dv = -11264;
  STAMP(7);
  asm volatile(
      "s_waitcnt lgkmcnt(0)\n\ts_nop 4\n"
      "1:\n\t"
      STEP(R_SA, R_SB, R_VA, R_VB, 1, 0, STAGE_TOP, KRL(0))
      STEP(R_SB, R_SA, R_VB, R_VA, 0, 1, "", "")
      STEP(R_SA, R_SB, R_VA, R_VB, 1, 2, "", KRL(2))
      STEP(R_SB, R_SA, R_VB, R_VA, 0, 3, "", "")
      STEP(R_SA, R_SB, R_VA, R_VB, 1, 4, "", KRL(4))
      STEP(R_SB, R_SA, R_VB, R_VA, 0, 5, "", "")
      STEP(R_SA, R_SB, R_VA, R_VB, 1, 6, "", KRL(6))
      STEP(R_SB, R_SA, R_VB, R_VA, 0, 7, "", "")
      "s_sub_u32 %[cnt], %[cnt], 1\n\t"
      "s_cmp_lg_u32 %[cnt], 0\n\t"
      "s_cbranch_scc1 1b\n\t"
      "s_waitcnt lgkmcnt(0)\n\t"
      "s_barrier\n\t"
      PV(R_OB, R_VB + 4, R_SB + 24)
      "s_nop 15\n\t"
      : "+{v[32:63]}"(sa), "+{v[64:95]}"(sb), "+{v[96:111]}"(oa), "+{v[112:127]}"(ob), "+{v[170:171]}"(kr), "+{v[172:175]}"(kf), "+{v[176:183]}"(va), "+{v[184:191]}"(vb),
        "+{v193}"(kad), "+{v194}"(krad), "+{v195}"(vad), [cnt] "+s"(cnt), [dk] "+s"(dk), [dv] "+s"(dv)
      : "{v[128:143]}"(na), "{v[144:159]}"(nb), "{v[160:163]}"(qa), "{v[164:167]}"(qb), "{v[168:169]}"(qr), "{v192}"(idx)
      : "memory", "scc");
  STAMP(8);
  if (__any(out_of_range(oa) || out_of_range(ob))) {
    const unsigned ko = WS_QK + ((80 + cb + e + hh) * 2048 + r) * 16, kro = WS_QK + ((80 + cb + (e ? 0 : 2)) * 2048 + r) * 16 + 8 * e;
    const unsigned vg = WS_V + (h * 256 + hh) * 320 + (r < 20 ? r : 0) * 16, pad = r == 20 ? ONES2 : 0u;
    float ma = -INFINITY, mb = -INFINITY;
#pragma unroll 1
    for (int pass = 0; pass < 2; ++pass) {
      na = splat16(pass ? -ma : 0.f); nb = splat16(pass ? -mb : 0.f); oa = splat16(0.f); ob = splat16(0.f);
#pragma unroll 1
      for (int t = 0; t < 64; ++t) {
        const s16x8 k16 = __builtin_bit_cast(s16x8, ld128_d(c.rs, ko + t * 512));
        f32x32 s = cat16(mfma32(k16, qa, na), mfma32(k16, qb, nb));
        s = __builtin_amdgcn_mfma_f32_32x32x4bf16_1k(__builtin_bit_cast(s16x4, ld64_d(c.rs, kro + t * 512)), qr, s, 0, 0, 0);
        if (pass == 0) { ma = fmaxf(ma, max16(lo16(s))); mb = fmaxf(mb, max16(hi16(s))); }
        else {
#pragma unroll
          for (int g = 0; g < 2; ++g) {
            u32x4 vf = ld128_d(c.rs, vg + (4 * t + 2 * g) * 320);
            if (r >= 20) vf = u32x4{pad, pad, pad, pad};
            f32x8 pa, pb;
#pragma unroll
            for (int i = 0; i < 8; ++i) { pa[i] = __builtin_amdgcn_exp2f(s[8 * g + i]); pb[i] = __builtin_amdgcn_exp2f(s[16 + 8 * g + i]); }
            oa = __builtin_amdgcn_smfmac_f32_32x32x32_bf16(__builtin_bit_cast(bf16x8, vf), __builtin_bit_cast(bf16x16, pa), oa, 0xDDDD, 0, 0);
            ob = __builtin_amdgcn_smfmac_f32_32x32x32_bf16(__builtin_bit_cast(bf16x8, vf), __builtin_bit_cast(bf16x16, pb), ob, 0xDDDD, 0, 0);
          }
        }
      }
      if (pass == 0) { ma = swap32_max(ma); mb = swap32_max(mb); }
    }
  }
#pragma unroll
  for (int t = 0; t < 2; ++t) {
    const f32x16& o = t ? ob : oa;
    const float rl = __builtin_amdgcn_rcpf(upper_half(o[8]));
    unsigned pk[6];
#pragma unroll
    for (int i = 0; i < 6; ++i) pk[i] = cvtpk(o[2 * i] * rl, o[2 * i + 1] * rl);
    const unsigned og = WS_O + (q0 >> 6) * 81920 + (32 * t + r) * 16;
    st128_d(c.rs, og + (cb + e + hh) * 1024, np(swap32_pair(pk[0], pk[1], pk[2], pk[3])));
    if (hh == 0) st64_d(c.rs, og + (e ? cb : cb + 2) * 1024 + 8 * e, u32x2{np(pk[4]), np(pk[5])});
  }
}

#define G_ACC 32
#define G_B0 72
#define G_B1 92
#define G_A 112
#define G_VB 176
#define G_VBO 177
#define G_VA0 178
#define G_VA1 179
#define G_CHK 180
#define GMM(m, nt, B, j) "v_mfma_f32_16x16x32_bf16 " VR(G_ACC + m * 20 + nt * 4, 4) ", " VR(B + nt * 4, 4) ", " VR(G_A + j * 8 + m * 4, 4) ", " VR(G_ACC + m * 20 + nt * 4, 4) "\n\t"
#define GRB(B, nt, ks) "ds_read_b64_tr_b16 " VR(B + nt * 4, 2) ", v" STR(G_VB) " offset:" STR(ks * 5120 + nt * 32) "\n\t" \
                       "ds_read_b64_tr_b16 " VR(B + nt * 4 + 2, 2) ", v" STR(G_VB) " offset:" STR(ks * 5120 + nt * 32 + 1280) "\n\t"
#define GCK(j, i) "v_max3_u32 v" STR(G_CHK) ", v[" STR(G_A + j * 8 + i * 2) "], v[" STR(G_A + j * 8 + i * 2 + 1) "], v" STR(G_CHK) "\n\t"
#define GLD1(j, m, va) "buffer_load_dwordx4 " VR(G_A + (j) * 8 + m * 4, 4) ", v" STR(va) ", %[rs], %[so] offen offset:" STR(m * 256) " sc1\n\t"
#define GADV "s_add_u32 %[so], %[so], 16384\n\ts_cmp_eq_u32 %[so], %[sw]\n\ts_cselect_b32 %[so], 0, %[so]\n\t"
#define GLD2(j) GLD1(j, 0, G_VA0) GLD1(j, 1, G_VA0) GLD1(j + 1, 0, G_VA1) GLD1(j + 1, 1, G_VA1) GADV
#define GNEXT "s_add_u32 %[sl], %[sl], 20480\n\ts_cmp_eq_u32 %[sl], %[lw]\n\ts_cselect_b32 %[sl], %[lr], %[sl]\n\t"
#define GADR "v_add_u32 v" STR(G_VB) ", %[sl], v" STR(G_VBO) "\n\t"
#define GBODY(j, BC, BN, ks, VM, W0, PRE, L0, L1) \
  "s_waitcnt vmcnt(" STR(VM) ") lgkmcnt(" STR(W0) ")\n\t" PRE \
  GMM(0, 0, BC, j) GRB(BN, 0, ks) GMM(1, 0, BC, j) GRB(BN, 1, ks) WL(10) GMM(0, 1, BC, j) GRB(BN, 2, ks) GMM(1, 1, BC, j) GRB(BN, 3, ks) \
  WL(12) GMM(0, 2, BC, j) GRB(BN, 4, ks) GMM(1, 2, BC, j) GCK(j, 0) GCK(j, 1) WL(12) GMM(0, 3, BC, j) GCK(j, 2) GMM(1, 3, BC, j) GCK(j, 3) \
  WL(10) GMM(0, 4, BC, j) L0 GMM(1, 4, BC, j) L1
#define GEVEN(j) GBODY(j, G_B0, G_B1, 1, 14, 8, "", GLD1(j, 0, G_VA0) GNEXT, GLD1(j, 1, G_VA0))
#define GODD(j) GBODY(j, G_B1, G_B0, 0, 14, 8, GADR, GLD1(j, 0, G_VA1), GLD1(j, 1, G_VA1) GADV)
#define GODDB(j) GBODY(j, G_B1, G_B0, 0, 14, 0, "s_barrier\n\t" GADR, GLD1(j, 0, G_VA1), GLD1(j, 1, G_VA1) GADV)
#define TEVEN(j) GBODY(j, G_B0, G_B1, 1, 14 - 2 * j, 8, "", GNEXT, "")
#define TODD(j) GBODY(j, G_B1, G_B0, 0, 14 - 2 * j, 8, GADR, "", "")

DI void g80_compute(const Ctx& c, int last, u32x2 (&res)[5]) {
  int l = c.l; asm volatile("" : "+v"(l));
  const int w = c.w, wm = w & 1, wk = w >> 1, g = l >> 4, q = (l & 15) >> 2, pp = l & 3;
  const unsigned tile = last ? WS_F + c.rt * 196608 : WS_O + c.rt * 81920;
  __builtin_amdgcn_s_barrier();
  __builtin_amdgcn_s_sleep(PDELAY);
  {
    const int hd = l & 31, e = hd & 1, cb = 5 * (hd >> 1) + 2 * e;
    const unsigned po = tile + 63 * 16 + (last ? (hd >> 2) * 24 + (hd & 3) * 6 + 5 : (e ? cb : cb + 2)) * 1024 + (last ? 0 : 8 * e);
#pragma unroll 1
    for (unsigned n = 0; n < SPIN_CAP; ++n) { FENCE(); if (!__any(ld32_d(c.rs, po) == POISON)) break; __builtin_amdgcn_s_sleep(PSLEEP); }
  }
  if (last) STAMP(16); else STAMP(10);
  const unsigned va0 = tile + (8 * wk + g) * 1024 + (32 * wm + (l & 15)) * 16;
  f32x32 acc0 = {}; f32x8 acc1 = {};
  const unsigned r0 = last ? c.rot5 : (5 * c.cs) >> 3;
  unsigned chk = 0, so = r0 * 16384, sl = last ? 0 : L_WO + r0 * 20480, cnt = last ? 2 : 1;
  asm volatile(
      "s_cmp_eq_u32 %[cnt], 1\n\ts_cbranch_scc0 4f\n\t"
      GLD2(6)
      "4:\n\t"
      GLD2(0) GLD2(2) GLD2(4)
      "s_cmp_eq_u32 %[cnt], 1\n\ts_cbranch_scc1 4f\n\t"
      GLD2(6)
      "4:\n\t" TS(0)
      "s_barrier\n\t" TS(1)
      "v_add_u32 v" STR(G_VB) ", %[sl], v" STR(G_VBO) "\n\t"
      GRB(G_B0, 0, 0) GRB(G_B0, 1, 0) GRB(G_B0, 2, 0) GRB(G_B0, 3, 0) GRB(G_B0, 4, 0)
      "s_cmp_eq_u32 %[cnt], 1\n\ts_cbranch_scc1 2f\n"
      "1:\n\t"
      GEVEN(0) GODD(1) GEVEN(2) GODD(3) GEVEN(4) GODD(5)
      "2:\n\t"
      GEVEN(6)
      GODDB(7)
      "s_sub_u32 %[cnt], %[cnt], 1\n\ts_cmp_lg_u32 %[cnt], 0\n\ts_cbranch_scc1 1b\n\t"
      TEVEN(0) TODD(1) TEVEN(2) TODD(3) TEVEN(4) TODD(5) TEVEN(6)
      "s_waitcnt vmcnt(0) lgkmcnt(0)\n\ts_barrier\n\t"
      GMM(0, 0, G_B1, 7) GMM(1, 0, G_B1, 7) GCK(7, 0) GMM(0, 1, G_B1, 7) GCK(7, 1) GMM(1, 1, G_B1, 7) GCK(7, 2) GMM(0, 2, G_B1, 7) GCK(7, 3)
      GMM(1, 2, G_B1, 7) GMM(0, 3, G_B1, 7) GMM(1, 3, G_B1, 7) GMM(0, 4, G_B1, 7) GMM(1, 4, G_B1, 7)
      "s_nop 15\n\t" TS(5)
      : "+{v[32:63]}"(acc0), "+{v[64:71]}"(acc1), "+{v180}"(chk), [so] "+s"(so), [sl] "+s"(sl), [cnt] "+s"(cnt)
      : "{v177}"(c.lds0 + (64 * wk + 16 * (g >> 1) + 4 * (g & 1) + q) * 160 + pp * 8), "{v178}"(va0), "{v179}"(va0 + 4096), [rs] "s"(c.rs),
        [sw] "s"(last ? 196608 : 81920), [lw] "s"(last ? 163840 : L_WO + 102400), [lr] "s"(last ? 0 : L_WO)
      : "memory", "scc", "v176", CL8(160, 161, 162, 163, 164, 165, 166, 167), CL8(168, 169, 170, 171, 172, 173, 174, 175),
        "v72", "v73", "v74", "v75", "v76", "v77", "v78", "v79", "v80", "v81", "v82", "v83", "v84", "v85", "v86", "v87", "v88", "v89", "v90", "v91", "v92", "v93", "v94", "v95",
        "v96", "v97", "v98", "v99", "v100", "v101", "v102", "v103", "v104", "v105", "v106", "v107", "v108", "v109", "v110", "v111", "v112", "v113", "v114", "v115", "v116", "v117", "v118", "v119",
        "v120", "v121", "v122", "v123", "v124", "v125", "v126", "v127", "v128", "v129", "v130", "v131", "v132", "v133", "v134", "v135", "v136", "v137", "v138", "v139", "v140", "v141", "v142", "v143",
        "v144", "v145", "v146", "v147", "v148", "v149", "v150", "v151", "v152", "v153", "v154", "v155", "v156", "v157", "v158", "v159" TS_CLOB);
  if (last) { TS_PUT(0, 25); TS_PUT(1, 26); TS_PUT(2, 27); TS_PUT(3, 28); TS_PUT(4, 29); TS_PUT(5, 30); } else { TS_PUT(0, 19); TS_PUT(1, 20); TS_PUT(4, 21); TS_PUT(5, 22); }
  f32x4 acc[2][5];
#pragma unroll
  for (int i = 0; i < 40; ++i) acc[i / 20][(i % 20) / 4][i % 4] = i < 32 ? acc0[i] : acc1[i - 32];
  if (__any(chk == POISON)) {
    const char* wg = (last ? c.p.w2 : c.p.wo) + c.cs * 160 + (l & 15) * 2;
#pragma unroll
    for (int i = 0; i < 10; ++i) acc[i / 5][i % 5] = f32x4{};
#pragma unroll 1
    for (int st = 0; st < (last ? 24 : 10); ++st) {
      u32x4 a0, a1;
#pragma unroll 1
      for (unsigned n = 0; n < SPIN_CAP; ++n) {
        FENCE();
        a0 = ld128_d(c.rs, va0 + (st >> 1) * 16384 + (st & 1) * 4096); a1 = ld128_d(c.rs, va0 + (st >> 1) * 16384 + (st & 1) * 4096 + 256);
        if (!__any(umax(umax4(a0), umax4(a1)) == POISON)) break;
      }
      const char* wr = wg + (size_t)((st >> 1) * 128 + 64 * wk + (st & 1) * 32 + 8 * g) * 1280;
#pragma unroll
      for (int nt = 0; nt < 5; ++nt) {
        s16x8 bf;
#pragma unroll
        for (int j = 0; j < 8; ++j) bf[j] = *(const GAS short*)(wr + j * 1280 + nt * 32);
        acc[0][nt] = mfma16(bf, __builtin_bit_cast(s16x8, a0), acc[0][nt]); acc[1][nt] = mfma16(bf, __builtin_bit_cast(s16x8, a1), acc[1][nt]);
      }
    }
  }
  lptr ex = c.lds + L_EX + l * 16;
#pragma unroll
  for (int nt = 0; nt < 5; ++nt) *(LAS f32x4*)(ex + w * 5120 + nt * 1024) = wk ? acc[0][nt] : acc[1][nt];
  LDS_BAR();
  if (last) STAMP(17); else STAMP(11);
#pragma unroll
  for (int nt = 0; nt < 5; ++nt) {
    const f32x4 u = (wk ? acc[1][nt] : acc[0][nt]) + *(LAS f32x4*)(ex + (w ^ 2) * 5120 + nt * 1024);
    res[nt] = u32x2{np(cvtpk(u[0] + bflo(res[nt][0]), u[1] + bfhi(res[nt][0]))), np(cvtpk(u[2] + bflo(res[nt][1]), u[3] + bfhi(res[nt][1])))};
  }
  const int row = 32 * wm + 16 * wk + (l & 15);
  if (!last) {
    float s1 = 0.f, s2 = 0.f;
#pragma unroll
    for (int nt = 0; nt < 5; ++nt) { s1 = dot2(res[nt][0], ONES2, dot2(res[nt][1], ONES2, s1)); s2 = dot2(res[nt][0], res[nt][0], dot2(res[nt][1], res[nt][1], s2)); }
    s1 = swap16_add(s1, s1); s2 = swap16_add(s2, s2); s1 = swap32_add(s1, s1); s2 = swap32_add(s2, s2);
    if (l < 16) st64_d(c.rs, WS_RS2 + (c.rt * 8 + c.cs) * 512 + row * 8, u32x2{np(__float_as_uint(s1)), np(__float_as_uint(s2))});
  }
  const rsrc_t ro = __builtin_amdgcn_make_buffer_rsrc(last ? c.p.out : c.p.ws + WS_H, 0, S * D * 2, 0x00020000);
  const unsigned ob = c.rt * 81920 + (last ? c.cs * 160 + row * 1280 : c.cs * 10240 + row * 16), oc = last ? 16 : 1024;
#pragma unroll
  for (int i = 0; i < 3; ++i) {
    const int n1 = i < 2 ? 2 * i + 1 : 4, chunk = 4 * i + (i < 2 ? 2 * (g & 1) : 0) + (g >> 1);
    st128_d(ro, ob + chunk * oc, swap16_pair(res[2 * i][0], res[2 * i][1], res[n1][0], res[n1][1]));
  }
}

#define F_X 80
#define F_Y 108
#define FMM(S, nt, m) "v_mfma_f32_16x16x32_bf16 " VR(32 + nt * 16 + m * 4, 4) ", " VR(S + nt * 4, 4) ", " VR(S + 12 + m * 4, 4) ", " VR(32 + nt * 16 + m * 4, 4) "\n\t"
#define FRA(S, m, ks) "ds_read_b128 " VR(S + 12 + m * 4, 4) ", v140 offset:" STR(ks * 4096 + m * 256) "\n\t"
#define FRB(S, nt, h, ks) "ds_read_b64_tr_b16 " VR(S + nt * 4 + h * 2, 2) ", v[141+" STR(nt) "] offset:" STR(ks * 12288 + h * 3072) "\n\t"
DI void p4_compute(const Ctx& c) {
  int l = c.l; asm volatile("" : "+v"(l));
  const int w = c.w, g = l >> 4, q = (l & 15) >> 2, pp = l & 3;
  unsigned boff[3];
#pragma unroll
  for (int nt = 0; nt < 3; ++nt) boff[nt] = c.lds0 + (16 * (g >> 1) + 4 * (g & 1) + q) * 384 + (((3 * w + nt) ^ (2 * (g & 1) + (q >> 1))) * 32) + pp * 8;
  f32x32 ac0 = {}; f32x16 ac1 = {};
  unsigned ch = 0, sa, sb = 0;
  asm volatile(
      "v_mov_b32 v140, v136\n\tv_mov_b32 v141, v137\n\tv_mov_b32 v142, v138\n\tv_mov_b32 v143, v139\n\t"
      "s_barrier\n\t" TS(0)
      FRB(F_Y, 0, 0, 0) FRB(F_Y, 0, 1, 0) FRA(F_Y, 0, 0) FRA(F_Y, 1, 0) FRA(F_Y, 2, 0) FRA(F_Y, 3, 0) FRB(F_Y, 1, 0, 0) FRB(F_Y, 1, 1, 0) FRB(F_Y, 2, 0, 0) FRB(F_Y, 2, 1, 0)
      "s_branch 2f\n"
      "1:\n\t"
      FMM(F_X, 0, 2) FRB(F_Y, 0, 0, 0) FMM(F_X, 0, 3) FRB(F_Y, 0, 1, 0) FMM(F_X, 1, 0) FRA(F_Y, 0, 0) FMM(F_X, 1, 1) FRA(F_Y, 1, 0) FMM(F_X, 1, 2) FRA(F_Y, 2, 0)
      FMM(F_X, 1, 3) FRA(F_Y, 3, 0) FMM(F_X, 2, 0) FRB(F_Y, 1, 0, 0) FMM(F_X, 2, 1) FRB(F_Y, 1, 1, 0) FMM(F_X, 2, 2) FRB(F_Y, 2, 0, 0) FMM(F_X, 2, 3) FRB(F_Y, 2, 1, 0)
      "2:\n\t"
      WL(7) FMM(F_Y, 0, 0) FRB(F_X, 0, 0, 1) WL(7) FMM(F_Y, 0, 1) FRB(F_X, 0, 1, 1) WL(7) FMM(F_Y, 0, 2) FRA(F_X, 0, 1) WL(7) FMM(F_Y, 0, 3) FRA(F_X, 1, 1)
      WL(6) FMM(F_Y, 1, 0) FRA(F_X, 2, 1) "s_add_u32 %[ch], %[ch], 1\n\t" FMM(F_Y, 1, 1) FRA(F_X, 3, 1) "s_add_u32 %[sb], %[sb], 24576\n\t"
      FMM(F_Y, 1, 2) FRB(F_X, 1, 0, 1) "s_cmp_eq_u32 %[ch], 5\n\t" FMM(F_Y, 1, 3) FRB(F_X, 1, 1, 1)
      WL(8) FMM(F_Y, 2, 0) FRB(F_X, 2, 0, 1) "s_cselect_b32 %[sb], 0, %[sb]\n\t" FMM(F_Y, 2, 1) FRB(F_X, 2, 1, 1) "s_and_b32 %[sa], %[ch], 3\n\t"
      FMM(F_Y, 2, 2) "s_lshl_b32 %[sa], %[sa], 13\n\tv_add_u32 v141, %[sb], v137\n\t" FMM(F_Y, 2, 3) "v_add_u32 v142, %[sb], v138\n\t"
      WL(7) FMM(F_X, 0, 0) "v_add_u32 v143, %[sb], v139\n\t" WL(6) FMM(F_X, 0, 1) "v_add_u32 v140, %[sa], v136\n\ts_cmp_eq_u32 %[ch], 10\n\t"
      WL(0) "s_barrier\n\t"
      "s_cbranch_scc0 1b\n\t"
      FMM(F_X, 0, 2) FMM(F_X, 0, 3) FMM(F_X, 1, 0) FMM(F_X, 1, 1) FMM(F_X, 1, 2) FMM(F_X, 1, 3) FMM(F_X, 2, 0) FMM(F_X, 2, 1) FMM(F_X, 2, 2) FMM(F_X, 2, 3)
      "s_nop 15"
      : "+{v[32:63]}"(ac0), "+{v[64:79]}"(ac1), [ch] "+s"(ch), [sa] "=&s"(sa), [sb] "+s"(sb)
      : "{v136}"(c.lds0 + L4_A + g * 1024 + (l & 15) * 16), "{v137}"(boff[0]), "{v138}"(boff[1]), "{v139}"(boff[2])
      : "memory", "scc", "v140", "v141", "v142", "v143", CL8(80, 81, 82, 83, 84, 85, 86, 87), CL8(88, 89, 90, 91, 92, 93, 94, 95), CL8(96, 97, 98, 99, 100, 101, 102, 103), CL8(104, 105, 106, 107, 108, 109, 110, 111),
        CL8(112, 113, 114, 115, 116, 117, 118, 119), CL8(120, 121, 122, 123, 124, 125, 126, 127), CL8(128, 129, 130, 131, 132, 133, 134, 135) TS_CLOB);
  TS_PUT(0, 13);
  f32x4 acc[4][3];
#pragma unroll
  for (int i = 0; i < 48; ++i) acc[(i >> 2) & 3][i >> 4][i & 3] = i < 32 ? ac0[i] : ac1[i - 32];
  LAS float* ra = (LAS float*)(c.lds + L_RA); LAS float* rb = ra + 64; LAS float* csum = (LAS float*)(c.lds + L_CSUM);
  float a[4], b[4]; f32x4 c4[3];
#pragma unroll
  for (int m = 0; m < 4; ++m) { a[m] = ra[16 * m + (l & 15)]; b[m] = rb[16 * m + (l & 15)]; }
#pragma unroll
  for (int nt = 0; nt < 3; ++nt) c4[nt] = *(LAS f32x4*)(csum + w * 48 + nt * 16 + 4 * g);
  STAMP(14);
#pragma unroll
  for (int nt = 0; nt < 3; ++nt)
#pragma unroll
    for (int mp = 0; mp < 2; ++mp) {
      unsigned pk[2][2];
#pragma unroll
      for (int j = 0; j < 2; ++j) {
        const int m = 2 * mp + j; float y[4];
#pragma unroll
        for (int i = 0; i < 4; ++i) y[i] = fmaxf(fmaf(acc[m][nt][i], a[m], b[m] * c4[nt][i]), 0.f);
        pk[j][0] = cvtpk(y[0], y[1]); pk[j][1] = cvtpk(y[2], y[3]);
      }
      st128_d(c.rs, WS_F + c.rt * 196608 + (c.cs * 24 + w * 6 + nt * 2 + (g >> 1)) * 1024 + (32 * mp + (g & 1) * 16 + (l & 15)) * 16, swap16_pair(pk[0][0], pk[0][1], pk[1][0], pk[1][1]));
    }
}

DI void compute_waves(Ctx& c) {
  const Params& p = c.p;
  read_tag(c);
  const int l = c.l, w = c.w, b = c.b, r = l & 31, hh = l >> 5;
  {
    const int xrow = c.xr0 + 2 * w + hh, rg = c.wi & 7, cg = 15 * (b & 1) + (c.wi >> 3);
    LDS_BAR();
    lptr xs = c.lds + L1_XS + (2 * w + hh) * 1280 + r * 16, ws = c.lds + L1_WS + (w * 64 + l) * 16;
    const u32x4 xv[3] = {*(LAS u32x4*)xs, *(LAS u32x4*)(xs + 512), *(LAS u32x4*)(xs + 1024)};
    u32x4 wv[3] = {*(LAS u32x4*)ws, *(LAS u32x4*)(ws + 4096), {}};
    if (w < 2) wv[2] = *(LAS u32x4*)(ws + 8192);
    row_stats(c, xv, xrow);
    colsum_step1(c.lds + L_PART, l, w, wv);
    LDS_BAR();
    const float v = colsum_step2(c.lds + L_PART, l, w);
    if (l < 16 && c.ct < 15) st64_d(c.rs, WS_CS1 + (rg * 1920 + cg * 64 + 16 * w + l) * 8, u32x2{__float_as_uint(v), (unsigned)c.tag});
    STAMP(1);
    if (c.ct < 10) p1_compute<false>(c);
    else if (c.ct < 15) p1_compute<true>(c);
  }
  STAMP(4);
  p2_compute(c);
  STAMP(9);
  u32x2 res[5];
  {
    const char* xg = p.x + (size_t)((c.rt * 64 + 32 * (w & 1) + 16 * (w >> 1) + (l & 15)) * 1280 + c.cs * 160 + (l >> 4) * 8);
#pragma unroll
    for (int nt = 0; nt < 5; ++nt) res[nt] = *(const GAS u32x2*)(xg + nt * 32);
  }
  int last = 0;
#pragma unroll 1
  for (;;) {
    asm volatile("" : "+s"(last));
    g80_compute(c, last, res);
    if (last) break;
    STAMP(12);
    __builtin_amdgcn_s_barrier();
    p4_compute(c);
    STAMP(15);
    asm volatile(".Lblk_end1:");
    last = 1;
  }
  STAMP(18);
  asm volatile(".Lblk_end2:");
}

struct Park { f32x32 a, b, c, d, e; f32x16 f; f32x4 g; };
#define PARK_REGS(m, pk) m "{v[76:107]}"(pk.a), m "{v[108:139]}"(pk.b), m "{v[140:171]}"(pk.c), m "{v[172:203]}"(pk.d), m "{v[204:235]}"(pk.e), m "{v[236:251]}"(pk.f), m "{v[252:255]}"(pk.g)
#define PKL1(k, j, o) "buffer_load_dwordx4 " VR(76 + k * 20 + j * 4, 4) ", %[v" #j "], %[rs], %[so] offen offset:" STR(o) "\n\t"
#define PKL(k, ch) "s_add_u32 %[so], %[r5], " STR(ch * 163840 - 1966080) "\n\ts_cselect_b32 %[so], %[so], " STR(ch * 163840) "\n\ts_cselect_b32 %[t], 0, %[r5]\n\ts_add_u32 %[so], %[so], %[t]\n\t" PKL1(k, 0, 0) PKL1(k, 1, 1024) PKL1(k, 2, 2048) PKL1(k, 3, 3072) PKL1(k, 4, 0)
#define W80_OPS(r) [rs] "s"(r), [v0] "v"(vo80[0]), [v1] "v"(vo80[1]), [v2] "v"(vo80[2]), [v3] "v"(vo80[3]), [v4] "v"(vo80[4])
#define W80_DMA(r, dst, off) asm volatile("s_mov_b32 m0, %[d]\n\ts_nop 0\n\t" \
    "buffer_load_dwordx4 %[v0], %[rs], %[so] offen lds\n\tbuffer_load_dwordx4 %[v1], %[rs], %[so] offen offset:1024 lds\n\t" \
    "buffer_load_dwordx4 %[v2], %[rs], %[so] offen offset:2048 lds\n\tbuffer_load_dwordx4 %[v3], %[rs], %[so] offen offset:3072 lds\n\t" \
    "s_add_u32 m0, %[d], 4096\n\ts_nop 0\n\tbuffer_load_dwordx4 %[v4], %[rs], %[so] offen lds" :: [d] "s"(dst), [so] "s"(off), W80_OPS(r) : "memory", "scc")
#define PKS1(k, j) "ds_write_b128 %[va], " VR(76 + k * 20 + j * 4, 4) " offset:" STR(j * 1024) "\n\t"
#define PUT(k, slot) asm volatile(PKS1(k, 0) PKS1(k, 1) PKS1(k, 2) PKS1(k, 3) PKS1(k, 4) :: [va] "v"(wa + slot * 20480), PARK_REGS("", pk) : "memory")
#define P1_STATE [sa] "+s"(sa), [sb] "+s"(sb), [md] "+s"(md)
#define P1_CONST [rx] "s"(rx), [rw] "s"(rw), [l0] "s"(lds0 + lw * 4096), [a0] "v"(va[0]), [a1] "v"(va[1]), [a2] "v"(va[2]), [a3] "v"(va[3]), [b0] "v"(vb[0]), [b1x] "v"(vb[1]), [b2] "v"(vb[2]), [b3] "v"(vb[3])
#define P1_LD(v, r, s, o) "buffer_load_dwordx4 %[" v "], %[" r "], %[" s "] offen offset:" #o " lds\n\t"
#define P1_STAGE \
  "s_mov_b32 m0, %[md]\n\ts_add_u32 %[md], %[md], 16384\n\t" \
  P1_LD("a0", "rx", "sa", 0) P1_LD("a1", "rx", "sa", 1024) P1_LD("a2", "rx", "sa", 2048) P1_LD("a3", "rx", "sa", 3072) \
  "s_mov_b32 m0, %[md]\n\ts_add_u32 %[sa], %[sa], 128\n\t" \
  P1_LD("b0", "rw", "sb", 0) P1_LD("b1x", "rw", "sb", 1024) P1_LD("b2", "rw", "sb", 2048) P1_LD("b3", "rw", "sb", 3072) \
  "s_add_u32 %[sb], %[sb], 245760\n\ts_cmp_eq_u32 %[sa], 1280\n\ts_cselect_b32 %[sa], 0, %[sa]\n\ts_cselect_b32 %[sb], 0, %[sb]\n\t" \
  "s_sub_u32 %[md], %[md], %[l0]\n\ts_add_u32 %[md], %[md], 16384\n\ts_and_b32 %[md], %[md], 0x18000\n\ts_add_u32 %[md], %[md], %[l0]\n\t"
#define W1_OPS [rw] "s"(rw1), [dw] "s"(dw), [v0] "v"(vo[0]), [v1] "v"(vo[1]), [v2] "v"(vo[2]), [v3] "v"(vo[3]), [v4] "v"(vo[4]), [v5] "v"(vo[5])
#define LW1(v, o) "buffer_load_dwordx4 %[" v "], %[rw], %[t0] offen offset:" STR(o) " lds\n\t"
#define LW3_0 LW1("v0", 0) LW1("v1", 1024) LW1("v2", 2048)
#define LW3_1 LW1("v3", 0) LW1("v4", 1024) LW1("v5", 2048)
#define LW3(ch, u) "s_add_u32 m0, %[dw], " STR((ch - 5 * (ch / 5)) * 24576 + u * 3072) "\n\ts_mov_b32 %[t0], " STR(ch * 196608) "\n\t" LW3_##u
#define LA(ch) "s_add_u32 m0, %[da], " STR((ch & 3) * 8192) "\n\ts_mov_b32 %[t0], " STR(ch * 8192) "\n\t" \
  "buffer_load_dwordx4 %[va], %[rs], %[t0] offen sc1 lds\n\tbuffer_load_dwordx4 %[va], %[rs], %[t0] offen offset:1024 sc1 lds\n\t"
#define CKR(ch) "ds_read_b128 v[60:63], %[la] offset:" STR((ch & 3) * 8192) "\n\tds_read_b128 v[64:67], %[la] offset:" STR((ch & 3) * 8192 + 1024) "\n\t"
#define CKT "s_waitcnt lgkmcnt(0)\n\tv_max3_u32 v60, v60, v61, v62\n\tv_max3_u32 v64, v63, v64, v65\n\tv_max3_u32 v60, v60, v66, v67\n\tv_max_u32 v60, v60, v64\n\tv_cmp_eq_u32 vcc, -1, v60\n\t"
#define FIX(ch) "s_cbranch_vccz 2f\n\ts_mov_b32 %[t1], 0\n1:\n\ts_sleep 2\n\t" LA(ch) "s_waitcnt vmcnt(0)\n\t" CKR(ch) CKT \
  "s_cbranch_vccz 2f\n\ts_add_u32 %[t1], %[t1], 1\n\ts_cmp_lt_u32 %[t1], 0x40000\n\ts_cbranch_scc1 1b\n2:\n\t"
#define IVL(ch, VA, FETCH, VW) "s_waitcnt vmcnt(" STR(VA) ")\n\t" CKR(ch) FETCH CKT FIX(ch) "s_waitcnt vmcnt(" STR(VW) ")\n\t"
DI u64 poisoned(lptr p) { return __builtin_amdgcn_ballot_w64(umax4(*(const LAS u32x4*)p) == POISON); }

DI void loader_waves(Ctx& c) {
  const Params& p = c.p;
  const int l = c.l, lw = c.w - 4, b = c.b, t4 = lw * 64 + l, cs = c.cs, rt = c.rt;
  const unsigned lds0 = c.lds0, lin = l * 16;
  {
    {
      unsigned vo = lw < 2 ? lin : (l >> 3) * 3840 + (l & 7) * 16;
      unsigned so = lw < 2 ? c.xr0 * 1280 + lw * 5120 : ((c.wi & 7) * 80 + (lw - 2) * 40) * 3840 + (15 * (b & 1) + (c.wi >> 3)) * 128, st = lw < 2 ? 1024 : 8 * 3840;
      int isx = lw;
      asm volatile("" : "+s"(isx), "+v"(vo), "+s"(so), "+s"(st));
      __builtin_amdgcn_sched_barrier(0);
      STAMP(0);
      const char* src = (isx < 2 ? p.x : p.wqkv) + so;
#pragma unroll
      for (int i = 0; i < 5; ++i) glds(src + i * st, vo, lds0 + L1_XS + lw * 5120 + i * 1024);
      __builtin_amdgcn_sched_barrier(0);
    }
    const int rot = (5 * ((c.ct + 2 * c.panel) & 7)) >> 2;
    unsigned va[4], vb[4], sa = rot * 128, sb = rot * 245760, md = lds0 + 65536 + lw * 4096;
#pragma unroll
    for (int i = 0; i < 4; ++i) {
      va[i] = (32 * lw + 8 * i + (l >> 3)) * 1280 + (((l & 7) ^ ((4 * i + (l >> 4)) & 7)) << 4) - i * 1024;
      vb[i] = (16 * lw + 4 * i + (l >> 4)) * 3840 + (((l & 15) ^ (((l >> 4) & 3) << 2)) << 4) - i * 1024;
    }
    const rsrc_t rx = __builtin_amdgcn_make_buffer_rsrc((void*)(p.x + c.panel * 163840), 0, 163840, 0x00020000);
    const rsrc_t rw = __builtin_amdgcn_make_buffer_rsrc((void*)(p.wqkv + c.ct * 256), 0, 640 * 3840 - c.ct * 256, 0x00020000);
    if (c.ct < 15) asm volatile("s_sleep " STR(SDELAY) "\n\t" P1_STAGE "s_waitcnt vmcnt(8)\n\ts_barrier\n\t" P1_STAGE : P1_STATE : P1_CONST : "memory", "scc");
    else wait_bar<0>();
    read_tag(c);
    STAMP(2);
    {
      const char* pc; unsigned len;
      asm volatile("s_getpc_b64 %0\n.Lblk_pc:\n\ts_mov_b32 %1, .Lblk_end1-.Lblk_pc\n\ts_max_i32 %1, %1, .Lblk_end2-.Lblk_pc" : "=s"(pc), "=s"(len) :: "scc");
      const unsigned off = (((b >> 3) * 4 + lw) * 4 + (l & 3)) * 128;
      touch(pc, off < len ? off : 0u, lds0 + L_TOUCH);
    }
    if (c.ct < 15) {
      const O1 o1 = p1_places(c, lw);
      unsigned t, n = 0;
#define P1_PSN(b, s) "buffer_store_dwordx4 %[ps], %[" b "], %[rs], " s " offen sc1\n\t"
#define P1_POISON "s_add_u32 %[t], %[sm], %[sg]\n\t" P1_PSN("p0", "0") P1_PSN("p0", "%[sg]") P1_PSN("p0", "%[sm]") P1_PSN("p0", "%[t]") P1_PSN("p1", "0") P1_PSN("p1", "%[sg]") P1_PSN("p1", "%[sm]") P1_PSN("p1", "%[t]")
      asm volatile(
                   "s_waitcnt vmcnt(9)\n\ts_barrier\n\t" P1_STAGE P1_POISON "s_waitcnt vmcnt(16)\n\ts_barrier\n\t" P1_STAGE "s_waitcnt vmcnt(16)\n"
                   "1:\n\ts_barrier\n\t" P1_STAGE
                   "s_add_u32 %[n], %[n], 1\n\ts_cmp_lt_u32 %[n], 6\n\ts_waitcnt vmcnt(8)\n\ts_cbranch_scc1 1b"
                   : P1_STATE, [t] "=&s"(t), [n] "+s"(n)
                   : P1_CONST, [p0] "v"(o1.base[0]), [p1] "v"(o1.base[1]), [sm] "s"(o1.sm), [sg] "s"(o1.sg), [rs] "s"(c.rs), [ps] "v"(u32x4{POISON, POISON, POISON, POISON}) : "memory", "scc");
      STAMP(5);
      glds_d(p.ws + WS_CS1 + (2 * lw * 1920 + c.ct * 128) * 8, lin, lds0 + L1_CG + lw * 2048);
      glds_d(p.ws + WS_CS1 + ((2 * lw + 1) * 1920 + c.ct * 128) * 8, lin, lds0 + L1_CG + lw * 2048 + 1024);
      glds_m(p.ws + WS_RS1 + (c.panel * 128 + 32 * lw) * 16, lin, lds0 + L1_RG + lw * 512, 0xFFFFFFFFull);
    }
    if (lw == 0 && l < 4) *(LAS unsigned*)(c.lds + L_CNT0 + l * 4) = 0u;
    wait_bar<11>();
    if (c.ct == 15) {
      if (lw == 0 && l == 0) stflag(p.ws, WS_F0 + b * 8, c.tag);
      __builtin_amdgcn_s_sleep(127); __builtin_amdgcn_s_sleep(WOT);
    }
    {
      const unsigned pv = (l & 1) ? 0u : ONES2;
      if (l < 32) *(LAS u32x4*)(c.lds + VRING + (16 * lw + (l >> 1)) * 352 + 320 + (l & 1) * 16) = u32x4{pv, pv, pv, pv};
    }
    if (c.ct < 15) wait_bar<0>();
    STAMP(6);
    if (c.ct < 15) {
      LAS float* ra = (LAS float*)(c.lds + L1_FIN);
      const unsigned tag = (unsigned)c.tag;
      if (lw < 2) {
        u32x4 g = *(LAS u32x4*)(c.lds + L1_RG + t4 * 16);
#pragma unroll 1
        for (unsigned n = 0; (g[1] != tag || g[3] != tag) && n < SPIN_CAP; ++n) { FENCE(); g = ld128_d(c.rs, WS_RS1 + (c.panel * 128 + t4) * 16); }
        const float sc = c.ct < 5 ? QSCALE : 1.f;
        ra[t4] = __uint_as_float(g[0]) * sc; ra[128 + t4] = __uint_as_float(g[2]) * sc;
      } else {
        const int n0 = t4 - 128; float sum = 0.f; bool ok = true;
#pragma unroll
        for (int i = 0; i < 8; ++i) { const u32x2 g = *(LAS u32x2*)(c.lds + L1_CG + i * 1024 + n0 * 8); sum += __uint_as_float(g[0]); ok = ok && g[1] == tag; }
        if (__any(!ok)) {
          sum = 0.f;
#pragma unroll 1
          for (int i = 0; i < 8; ++i) {
            u32x2 g = {0u, ~tag};
#pragma unroll 1
            for (unsigned n = 0; g[1] != tag && n < SPIN_CAP; ++n) { FENCE(); g = ld64_d(c.rs, WS_CS1 + (i * 1920 + c.ct * 128 + n0) * 8); }
            sum += __uint_as_float(g[0]);
          }
        }
        ra[256 + n0] = sum;
      }
    }
    if (c.ct < 15) LDS_BAR();
    if (lw == 0) poll256(p.ws, WS_F0 + l * 8, c.tag);
    STAMP(7);
  }
  Park pk;
  const rsrc_t rw2 = __builtin_amdgcn_make_buffer_rsrc((void*)(p.w2 + cs * 160), 0, FF * D * 2 - cs * 160, 0x00020000);
  unsigned vo80[5];
  {
    const int h = c.h, e = h & 1, cb = 5 * (h >> 1) + 2 * e;
    unsigned vk[3], vv[3]; u64 mv[3], mt[2];
#pragma unroll
    for (int i = 0; i < 3; ++i) {
      const int n = 64 * (3 * lw + i) + l, kg = n / 22, dd = n - 22 * kg;
      vk[i] = WS_QK + ((80 + cb + i) * 2048 + 64 + 64 * lw - 256) * 16 + lin - i * 1024;
      vv[i] = WS_V + (h * 256 + 4 - 32 + kg) * 320 + dd * 16 - i * 1024;
      mv[i] = __builtin_amdgcn_ballot_w64(dd < 20 && kg < 32);
      if (i < 2) mt[i] = __builtin_amdgcn_ballot_w64(dd < 20 && kg >= 28);
    }
    auto issue = [&](int s, bool withk, u64 m0, u64 m1, u64 m2) {
      unsigned t;
      if (withk) asm volatile("s_mov_b32 m0, %[d]\n\ts_lshl_b32 %[t], %[s], 12\n\t"
                              "buffer_load_dwordx4 %[v0], %[rs], %[t] offen sc1 lds\n\tbuffer_load_dwordx4 %[v1], %[rs], %[t] offen offset:1024 sc1 lds\n\tbuffer_load_dwordx4 %[v2], %[rs], %[t] offen offset:2048 sc1 lds"
                              : [t] "=&s"(t) : [d] "s"(lds0 + KRING + (s & 1) * 12288 + lw * 3072), [s] "s"(s + 1), [rs] "s"(c.rs), [v0] "v"(vk[0]), [v1] "v"(vk[1]), [v2] "v"(vk[2]) : "memory", "scc");
      asm volatile("s_mov_b32 m0, %[d]\n\ts_mul_i32 %[t], %[s], 10240\n\t"
                   "s_mov_b64 exec, %[m0]\n\tbuffer_load_dwordx4 %[v0], %[rs], %[t] offen sc1 lds\n\t"
                   "s_mov_b64 exec, %[m1]\n\ts_cbranch_execz 1f\n\tbuffer_load_dwordx4 %[v1], %[rs], %[t] offen offset:1024 sc1 lds\n\t"
                   "s_mov_b64 exec, %[m2]\n\ts_cbranch_execz 1f\n\tbuffer_load_dwordx4 %[v2], %[rs], %[t] offen offset:2048 sc1 lds\n1:\n\ts_mov_b64 exec, -1"
                   : [t] "=&s"(t) : [d] "s"(lds0 + VRING + (s & 1) * 11264 + lw * 3072), [s] "s"(s + 1), [rs] "s"(c.rs), [v0] "v"(vv[0]), [v1] "v"(vv[1]), [v2] "v"(vv[2]),
                     [m0] "s"(m0), [m1] "s"(m1), [m2] "s"(m2) : "memory");
    };
    auto bad = [&](int s, u64 m0, u64 m1, u64 m2) {
      lptr k = c.lds + KRING + (s & 1) * 12288 + lw * 3072 + lin, v = c.lds + VRING + (s & 1) * 11264 + lw * 3072 + lin;
      return (poisoned(k) | poisoned(k + 1024) | poisoned(k + 2048) | (poisoned(v) & m0) | (poisoned(v + 1024) & m1) | (poisoned(v + 2048) & m2)) != 0;
    };
#pragma unroll
    for (int i = 0; i < 5; ++i) { const int n = 64 * i + l, rr = n / 10; vo80[i] = swap23(32 * lw + rr) * 1280 + (n - 10 * rr) * 16 - (i & 3) * 1024; }
    const rsrc_t rwo = __builtin_amdgcn_make_buffer_rsrc((void*)(p.wo + cs * 160), 0, D * D * 2 - cs * 160, 0x00020000);
    const unsigned xw = (b >> 3) * 4 + lw, vt60 = (l < 60 ? l : 0) * 256;
    u32x4 psn = {POISON, POISON, POISON, POISON};
    asm volatile("" : "+v"(psn));
    const int b1 = b < 192 ? b : 0, rg1 = b1 / 24, cg1 = b1 - 24 * rg1;
    unsigned w1o = (t4 >> 3) * 3072 + (l & 7) * 16, cs2o = WS_CS2 + (rg1 * 1536 + cg1 * 64 + 16 * lw + l) * 4;
    const char* bw = (l & 32 ? p.w2 : p.w1) + ((b * 4 + lw) * 15 + umin(l & 31, 14)) * 128;
    asm volatile("" : "+v"(w1o), "+v"(cs2o), "+v"(bw));
    LDS_BAR();
    __builtin_amdgcn_s_sleep(KDELAY);
#pragma unroll 1
    for (unsigned n = 0; n < SPIN_CAP; ++n) {
      if (lw == 3) issue(-1, true, mt[0], mt[1], 0);
      issue(0, true, mv[0], mv[1], mv[2]);
      if (lw != 3) break;
      wait_vm<5>();
      if (!bad(-1, mt[0], mt[1], 0)) break;
      DRAIN();
      __builtin_amdgcn_s_sleep(RDELAY);
    }
    STAMP(8);
    if (lw == 3 && l == 0) *(volatile LAS unsigned*)(c.lds + L_CNT0) = 1u;
#pragma unroll 1
    for (unsigned n = 0; n < SPIN_CAP; ++n) {
      DRAIN();
      if (!bad(0, mv[0], mv[1], mv[2])) break;
      __builtin_amdgcn_s_sleep(RDELAY);
      issue(0, true, mv[0], mv[1], mv[2]);
    }
    STAMP(9);
    __builtin_amdgcn_s_setprio(3);
#pragma unroll 1
    for (int s = 1; s < 5; ++s) {
      LDS_BAR(); STAMPV(20 + s);
      issue(s, true, mv[0], mv[1], mv[2]);
#pragma unroll
      for (int k = 0; k < 3; ++k) {
        const int q = 12 * lw + 3 * (s - 1) + k;
        asm volatile("buffer_store_dwordx4 %0, %1, %2, %3 offen sc1" :: "v"(psn), "v"(lin), "s"(c.rs),
                     "s"(q < 10 ? WS_O + rt * 81920 + cs * 10240 + q * 1024 : q < 20 ? WS_H + rt * 81920 + cs * 10240 + (q - 10) * 1024 : q < 44 ? WS_F + rt * 196608 + cs * 24576 + (q - 20) * 1024 : WS_RS2 + (rt * 8 + cs) * 512) : "memory");
      }
      if (s == 1) {
        wait_vm<3>();
#pragma unroll 1
        for (unsigned n = 0; bad(1, mv[0], mv[1], mv[2]) && n < SPIN_CAP; ++n) { issue(1, true, mv[0], mv[1], mv[2]); DRAIN(); }
        if (lw == 0) {
          poll256(p.ws, WS_F1 + l * 8, c.tag);
          if (b == 0 && l == 0) stflag(p.ws, WS_EPOCH, c.tag);
        }
      } else {
#pragma unroll 1
        for (int k = 2 * s - 4; k < (s < 4 ? 2 * s - 2 : 5); ++k)
        { const int kr = k + (rt & 3) < 5 ? k + (rt & 3) : k + (rt & 3) - 5; W80_DMA(rwo, lds0 + L_WO + kr * 20480 + lw * 5120, kr * 163840); }
        if (s == TSTAGE) touch64(bw, lds0 + L_TOUCH);
        if (s == 4) wait_vm<8 + (TSTAGE == 4)>();
        if (s == TSTAGE) wait_vm<14>(); else wait_vm<13>();
        if (s == 3) {
          const char* src = p.w1 + (rg1 * 80 * 3072 + cg1 * 128);
          glds(src, w1o, lds0 + L_W1B + lw * 1024); glds(src + 32 * 3072, w1o, lds0 + L_W1B + 4096 + lw * 1024);
          if (lw < 2) glds(src + 64 * 3072, w1o, lds0 + L_W1B + 8192 + lw * 1024);
        }
      }
      STAMPV(9 + s);
    }
    u32x4 w1m[3] = {*(LAS u32x4*)(c.lds + L_W1B + t4 * 16), *(LAS u32x4*)(c.lds + L_W1B + 4096 + t4 * 16), {}};
    if (lw < 2) w1m[2] = *(LAS u32x4*)(c.lds + L_W1B + 8192 + t4 * 16);
    colsum_step1(c.lds + L_PART + 1024, l, lw, w1m);
    unsigned so, st;
#define PARK2(m, k0, c0, k1, c1) asm volatile(PKL(k0, c0) PKL(k1, c1) : PARK_REGS(m, pk), [so] "=&s"(so), [t] "=&s"(st) : W80_OPS(rw2), [r5] "s"(c.rot5 * 163840) : "memory", "scc")
    LDS_BAR(); STAMP(25);
    {
      const float cs2 = colsum_step2(c.lds + L_PART + 1024, l, lw);
      if (l < 16 && b < 192) st32_d(c.rs, cs2o, __float_as_uint(cs2));
    }
    issue(5, true, mv[0], mv[1], mv[2]);
    PARK2("=&", 0, 0, 1, 1);
    wait_bar<10>(); STAMP(26);
    publish<10>(c, L_CNTP, 4, WS_FP + b * 8);
    issue(6, true, mv[0], mv[1], mv[2]);
    PARK2("+", 2, 2, 3, 3);
    wait_bar<10>(); STAMP(27);
    issue(7, lw != 3, lw == 3 ? mv[0] & ~mt[0] : mv[0], lw == 3 ? 0 : mv[1], mv[2]);
    PARK2("+", 4, 4, 5, 8);
    wait_bar<10>(); STAMP(28);
    if (lw == 0) poll256(p.ws, WS_FP + l * 8, c.tag);
    PARK2("+", 6, 9, 7, 10);
    if (lw == 1) {
      const int i = l & 7, k = l >> 3;
      touch64(k == 0 ? p.ws + (WS_O + c.qb * 327680 + i * 40960) : k == 1 ? p.ws + (WS_O + rt * 81920 + i * 10240) : k == 2 ? p.ws + (WS_H + rt * 81920 + i * 10240) : k == 3 ? p.ws + (WS_F + rt * 196608 + i * 24576)
              : k == 4 ? p.out + (rt * 81920 + i * 10240) : k == 5 ? p.x + (rt * 81920 + i * 10240) : p.ws + (WS_RS2 + rt * 4096 + i * 512), lds0 + L_TOUCH);
    }
    touch(p.w1 + xw * 15360, vt60, lds0 + L_TOUCH);
    touch(p.w1 + (xw * 15360 + 128), vt60, lds0 + L_TOUCH);
  }
  {
    unsigned vo[6];
#pragma unroll
    for (int i = 0; i < 3; ++i) {
      const int n = 64 * i + l, rr = n / 24, pos = n - 24 * rr;
      vo[i] = ((rr & 3) + 8 * (rr >> 2)) * 3072 + ((((pos >> 1) ^ ((rr >> 1) & 3)) << 1) | (pos & 1)) * 16 - i * 1024;
      vo[3 + i] = vo[i] + 4 * 3072;
    }
    const rsrc_t rw1 = __builtin_amdgcn_make_buffer_rsrc((void*)(p.w1 + cs * 384 + lw * 16 * 3072), 0, D * FF * 2 - cs * 384 - lw * 16 * 3072, 0x00020000);
    const unsigned dw = lds0 + lw * 6144, da = lds0 + L4_A + lw * 2048, va = WS_H + rt * 81920 + lw * 2048 + lin;
    const unsigned so = WS_RS2 + (rt * 8 + (l >> 4)) * 512 + (16 * lw + (l & 15)) * 8;
    unsigned t0, t1r; u32x2 s0, s1;
    LDS_BAR();
    STAMP(17);
    LDS_BAR();
    asm volatile(LW3(0, 0) LW3(0, 1) LW3(1, 0) LW3(1, 1) : [t0] "=&s"(t0) : W1_OPS : "memory");
    if (lw < 3) {
      float t = 0.f;
#pragma unroll
      for (int i = 0; i < 8; ++i) t += __uint_as_float(ld32_d(c.rs, WS_CS2 + (i * 1536 + cs * 192 + t4) * 4));
      *(LAS float*)(c.lds + L_CSUM + t4 * 4) = t;
    }
    asm volatile(PKL(8, 11) : PARK_REGS("+", pk), [so] "=&s"(t0), [t] "=&s"(t1r) : W80_OPS(rw2), [r5] "s"(c.rot5 * 163840) : "memory", "scc");
    LDS_BAR(); LDS_BAR(); LDS_BAR(); LDS_BAR();
    STAMP(18);
    LDS_BAR();
    asm volatile(
        LW3(2, 0) LW3(2, 1) LW3(3, 0) LW3(3, 1)
        "s_mov_b32 %[t1], 0\n"
        "1:\n\ts_sleep " STR(ADELAY) "\n\t" LA(0) LA(1) LA(2) LA(3)
        "s_waitcnt vmcnt(6)\n\t" CKR(0) CKT
        "s_cbranch_vccz 2f\n\ts_waitcnt vmcnt(0)\n\ts_add_u32 %[t1], %[t1], 1\n\ts_cmp_lt_u32 %[t1], 0x40000\n\ts_cbranch_scc1 1b\n"
        "2:\n\ts_barrier\n\t"
        IVL(1, 4, LW3(4, 0) LW3(4, 1), 63) "s_barrier\n\t"
        IVL(2, 8, LA(4) LW3(5, 0), 63) "s_barrier\n\t"
        IVL(3, 11, LA(5) LW3(5, 1), 63) "s_barrier\n\t"
        IVL(4, 8, LA(6) LW3(6, 0) LW3(6, 1), 63) "s_barrier\n\t"
        IVL(5, 11, LA(7) LW3(7, 0), 13) "s_barrier\n\t"
        IVL(6, 11, LA(8) LW3(7, 1) LW3(8, 0), 13) "s_barrier\n\t"
        IVL(7, 11, LA(9) LW3(8, 1), 8) "s_barrier\n\t"
        IVL(8, 11, "buffer_load_dwordx2 %[s0], %[so], %[rs], 0 offen sc1\n\tbuffer_load_dwordx2 %[s1], %[so], %[rs], 0 offen offset:2048 sc1\n\t" LW3(9, 0) LW3(9, 1), 8) "s_barrier\n\t"
        IVL(9, 11, "", 6)
        : [t0] "=&s"(t0), [t1] "=&s"(t1r), [s0] "=&v"(s0), [s1] "=&v"(s1)
        : W1_OPS, [rs] "s"(c.rs), [da] "s"(da), [va] "v"(va), [la] "v"(da + lin), [so] "v"(so) : "memory", "scc", "vcc", "v60", "v61", "v62", "v63", "v64", "v65", "v66", "v67");
    STAMP(29);
#pragma unroll 1
    for (unsigned n = 0; __any(umax(umax(s0[0], s0[1]), umax(s1[0], s1[1])) == POISON) && n < SPIN_CAP; ++n) { FENCE(); s0 = ld64_d(c.rs, so); s1 = ld64_d(c.rs, so + 2048); }
    float t1 = __uint_as_float(s0[0]) + __uint_as_float(s1[0]), t2 = __uint_as_float(s0[1]) + __uint_as_float(s1[1]);
    t1 = swap16_add(t1, t1); t2 = swap16_add(t2, t2); t1 = swap32_add(t1, t1); t2 = swap32_add(t2, t2);
    const float mean = t1 * (1.f / D), var = fmaxf(t2 * (1.f / D) - mean * mean, 0.f), a = __builtin_amdgcn_rsqf(var + LN_EPS);
    if (l < 32) *(LAS float*)(c.lds + L_RA + (l >> 4) * 256 + (16 * lw + (l & 15)) * 4) = l < 16 ? a : -mean * a;
    wait_bar<0>();
  }
  {
    const unsigned wa = lds0 + lw * 5120 + lin;
    {
      const int row = (5 + c.rot5) * 128 + ((b >> 6) * 4 + lw) * 24 + umin(l >> 1, 23);
      touch(p.w2 + cs * 160, (row < FF ? row : row - FF) * 1280 + (l & 1) * 156, lds0 + L_TOUCH);
    }
    LDS_BAR();
    STAMP(30);
    PUT(0, 0); PUT(1, 1); PUT(2, 2);
    LDS_BAR();
#pragma unroll 1
    for (int k = 5; k < 8; ++k) W80_DMA(rw2, lds0 + k * 20480 + lw * 5120, (k + c.rot5 < 12 ? k + c.rot5 : k + c.rot5 - 12) * 163840);
    PUT(3, 3); PUT(4, 4);
    STAMP(31);
    LDS_BAR();
    wait_bar<0>(); PUT(5, 0); PUT(6, 1); PUT(7, 2); PUT(8, 3);
    LDS_BAR(); LDS_BAR(); LDS_BAR();
  }
}

__global__ void __launch_bounds__(512, 2) block_kernel(Params p) {
  extern __shared__ __attribute__((aligned(16))) char smem[];
  Ctx c;
  c.st_lo = c.st_hi = 0;
  STAMP(3 + 20);
  c.p = p; c.lds = (lptr)smem; c.lds0 = (unsigned)(size_t)c.lds; c.l = threadIdx.x & 63; c.w = rfl(threadIdx.x >> 6); c.b = blockIdx.x;
  const int xcd = c.b & 7, j = c.b >> 3;
  const int t1 = 30 * (xcd & 1) + j;
  c.panel = 4 * (xcd >> 1) + (t1 & 3); c.ct = j < 30 ? t1 >> 2 : 15;
  c.h = c.rt = 4 * xcd + (j >> 3); c.qb = c.cs = j & 7;
  c.xr0 = 512 * (xcd >> 1) + 8 * (32 * (xcd & 1) + j); c.wi = j < 30 ? (xcd >> 1) * 30 + j : 0;
  c.rot5 = (3 * c.cs) >> 1; asm volatile("" : "+s"(c.rot5));
  if (__builtin_expect(c.w >= 4, 1)) loader_waves(c);
  else { STAMP(0); compute_waves(c); }
}

extern "C" void kernel_launch(void* const* d_in, const int* in_sizes, int n_in, void* d_out, int out_size, void* d_ws, size_t ws_size, hipStream_t stream) {
  Params p;
  p.x = (const char*)d_in[0]; p.wqkv = (const char*)d_in[1]; p.wo = (const char*)d_in[2]; p.w1 = (const char*)d_in[4]; p.w2 = (const char*)d_in[5];
  p.out = (char*)d_out; p.ws = (char*)d_ws;
  (void)hipFuncSetAttribute((const void*)block_kernel, hipFuncAttributeMaxDynamicSharedMemorySize, LDS_BYTES);
  block_kernel<<<256, 512, LDS_BYTES, stream>>>(p);
}
```

```cpp
#include <hip/hip_runtime.h>
#include <hip/hip_bf16.h>

#define DI __device__ __forceinline__
#define LAS __attribute__((address_space(3)))
#define GAS __attribute__((address_space(1)))
typedef unsigned long long u64;
typedef __attribute__((ext_vector_type(8))) short s16x8;
typedef __attribute__((ext_vector_type(4))) short s16x4;
typedef __attribute__((ext_vector_type(8))) __bf16 bf16x8;
typedef __attribute__((ext_vector_type(16))) __bf16 bf16x16;
typedef __attribute__((ext_vector_type(2))) __bf16 bf16x2;
typedef __attribute__((ext_vector_type(2))) float f32x2;
typedef __attribute__((ext_vector_type(4))) float f32x4;
typedef __attribute__((ext_vector_type(8))) float f32x8;
typedef __attribute__((ext_vector_type(16))) float f32x16;
typedef __attribute__((ext_vector_type(32))) float f32x32;
typedef __attribute__((ext_vector_type(4))) unsigned u32x4;
typedef __attribute__((ext_vector_type(2))) unsigned u32x2;
typedef LAS char* lptr;
typedef __amdgpu_buffer_rsrc_t rsrc_t;

#define V_BY_HEAD
#define SDELAY 10
#define TDELAY 20
#define TSTAGE 4
#define WOT 60
#define KDELAY 36
#define QDELAY 8
#define RDELAY 6
#define LSTW 4
#define PSLEEP 2
#define CPD 30
#define FSLP ""
#define PDELAY3 8
#define PDELAY 2
#define ADELAY 6
constexpr int S = 2048, D = 640, FF = 1536;
constexpr float LN_EPS = 1e-5f;
constexpr float QSCALE = 0.22360679774997896f * 1.4426950408889634f;

constexpr unsigned WS_EPOCH = 0, WS_F0 = 4096, WS_F1 = 6144, WS_FP = 8192;
constexpr unsigned WS_RS1 = 65536;
constexpr unsigned WS_CS1 = WS_RS1 + 2048 * 16;
constexpr unsigned WS_CS2 = WS_CS1 + 8 * 1920 * 8;
constexpr unsigned WS_QK = WS_CS2 + 8 * 1536 * 4;
constexpr unsigned WS_V = WS_QK + 160 * 2048 * 16;
constexpr unsigned WS_RS2 = WS_V + 256 * 640 * 16;
constexpr unsigned WS_O = WS_RS2 + 32 * 8 * 512;
constexpr unsigned WS_H = WS_O + S * D * 2;
constexpr unsigned WS_F = WS_H + S * D * 2;
constexpr unsigned WS_END = WS_F + S * FF * 2;
constexpr int LDS_BYTES = 163840;
constexpr unsigned SPIN_CAP = 1u << 18;
constexpr unsigned POISON = 0xFFFFFFFFu;
constexpr int L1_CG = 131072, L1_RG = L1_CG + 8192, L1_FIN = L1_RG + 2048;
constexpr int L1_XS = 131072, L1_WS = L1_XS + 10240;
constexpr int KRING = 0, VRING = 24576, L_W1B = 47104, L_WO = 57344;
constexpr int L4_A = 122880, L_EX = 98304;
constexpr int L_CNT0 = 159744, L_CNT1 = L_CNT0 + 4, L_CNTP = L_CNT0 + 8, L_CSUM = L_CNT0 + 256, L_RA = L_CSUM + 768, L_TOUCH = L_RA + 512, L_PART = L_TOUCH + 256;

#define TS(i) ""
#define TS_CLOB
#define TS_PUT(i, k)
#define STAMP(k)
#define STAMPV(k)
struct Params { const char* x; const char* wqkv; const char* wo; const char* w1; const char* w2; char* out; char* ws; };
struct Ctx { Params p; lptr lds; unsigned lds0; rsrc_t rs; u64 tag; int l, w, b, panel, ct, h, qb, rt, cs, rot5, xr0, wi; mutable unsigned st_lo, st_hi; };

DI unsigned rfl(unsigned v) { return __builtin_amdgcn_readfirstlane(v); }
DI unsigned cvtpk(float lo, float hi) { f32x2 v = {lo, hi}; return __builtin_bit_cast(unsigned, __builtin_convertvector(v, bf16x2)); }
typedef __attribute__((ext_vector_type(2))) short s16x2;
DI unsigned relu2(unsigned v) { return __builtin_bit_cast(unsigned, __builtin_elementwise_max(__builtin_bit_cast(s16x2, v), s16x2{0, 0})); }
DI float bflo(unsigned v) { return __uint_as_float(v << 16); }
DI float bfhi(unsigned v) { return __uint_as_float(v & 0xffff0000u); }
DI void glds(const char* sbase, unsigned voff, unsigned lds) {
  asm volatile("s_mov_b32 m0, %2\n\ts_nop 0\n\tglobal_load_lds_dwordx4 %0, %1" :: "v"(voff), "s"(sbase), "s"(lds) : "memory");
}
DI void glds_d(const char* sbase, unsigned voff, unsigned lds) {
  asm volatile("s_mov_b32 m0, %2\n\ts_nop 0\n\tglobal_load_lds_dwordx4 %0, %1 sc1" :: "v"(voff), "s"(sbase), "s"(lds) : "memory");
}
DI void glds_m(const char* sbase, unsigned voff, unsigned lds, u64 mask) {
  asm volatile("s_mov_b32 m0, %2\n\ts_mov_b64 exec, %3\n\tglobal_load_lds_dwordx4 %0, %1 sc1\n\ts_mov_b64 exec, -1" :: "v"(voff), "s"(sbase), "s"(lds), "s"(mask) : "memory");
}
DI void touch(const char* sbase, unsigned voff, unsigned lds) {
  asm volatile("s_mov_b32 m0, %2\n\ts_nop 0\n\tglobal_load_lds_dword %0, %1" :: "v"(voff), "s"(sbase), "s"(lds) : "memory");
}
#define TSC ""
DI void touch64(const char* a, unsigned lds) { asm volatile("s_mov_b32 m0, %1\n\ts_nop 0\n\tglobal_load_lds_dword %0, off" TSC :: "v"(a), "s"(lds) : "memory"); }
template <int N> DI void wait_vm() { asm volatile("s_waitcnt vmcnt(%0)" :: "i"(N) : "memory"); }
template <int N> DI void wait_bar() { asm volatile("s_waitcnt vmcnt(%0) lgkmcnt(0)\n\ts_barrier" :: "i"(N) : "memory"); }
#define LDS_BAR() asm volatile("s_waitcnt lgkmcnt(0)\n\ts_barrier" ::: "memory")
#define DRAIN() asm volatile("s_waitcnt vmcnt(0)" ::: "memory")
#define STR_(x) #x
#define STR(x) STR_(x)
#define VR(b, n) "v[" STR(b) ":" STR(b) "+" STR(n) "-1]"
#define WL(n) "s_waitcnt lgkmcnt(" #n ")\n\t"
#define CL8(a, b, c, d, e, f, g, h) "v" #a, "v" #b, "v" #c, "v" #d, "v" #e, "v" #f, "v" #g, "v" #h
DI s16x8 ld128(lptr p) { return *(const LAS s16x8*)p; }
DI s16x4 ldtr(lptr p) { return __builtin_amdgcn_ds_read_tr16_b64_v4i16((LAS s16x4*)p); }
DI s16x8 ldtr2(lptr p, int d) { return __builtin_shufflevector(ldtr(p), ldtr(p + d), 0, 1, 2, 3, 4, 5, 6, 7); }
DI f32x16 mfma32(s16x8 a, s16x8 b, f32x16 c) { return __builtin_amdgcn_mfma_f32_32x32x16_bf16(__builtin_bit_cast(bf16x8, a), __builtin_bit_cast(bf16x8, b), c, 0, 0, 0); }
DI f32x4 mfma16(s16x8 a, s16x8 b, f32x4 c) { return __builtin_amdgcn_mfma_f32_16x16x32_bf16(__builtin_bit_cast(bf16x8, a), __builtin_bit_cast(bf16x8, b), c, 0, 0, 0); }
DI float dot2(unsigned a, unsigned b, float c) { return __builtin_amdgcn_fdot2_f32_bf16(__builtin_bit_cast(bf16x2, a), __builtin_bit_cast(bf16x2, b), c, false); }
constexpr unsigned ONES2 = 0x3f803f80u;
DI void st128_d(rsrc_t rs, unsigned off, u32x4 v) { __builtin_amdgcn_raw_buffer_store_b128(v, rs, off, 0, 16); }
DI void st_poison(rsrc_t rs, unsigned off, unsigned soff) {
  asm volatile("buffer_store_dwordx4 %0, %1, %2, %3 offen sc1\n\ts_nop 1" :: "v"(u32x4{POISON, POISON, POISON, POISON}), "v"(off), "s"(rs), "s"(soff) : "memory");
}
DI void st64_d(rsrc_t rs, unsigned off, u32x2 v) { __builtin_amdgcn_raw_buffer_store_b64(v, rs, off, 0, 16); }
DI void st32_d(rsrc_t rs, unsigned off, unsigned v) { __builtin_amdgcn_raw_buffer_store_b32(v, rs, off, 0, 16); }
DI u32x4 ld128_d(rsrc_t rs, unsigned off) { return __builtin_amdgcn_raw_buffer_load_b128(rs, off, 0, 16); }
DI u32x2 ld64_d(rsrc_t rs, unsigned off) { return __builtin_amdgcn_raw_buffer_load_b64(rs, off, 0, 16); }
DI unsigned ld32_d(rsrc_t rs, unsigned off) { return __builtin_amdgcn_raw_buffer_load_b32(rs, off, 0, 16); }
DI u64 ldflag(const char* ws, unsigned off) { return __hip_atomic_load((GAS u64*)(ws + off), __ATOMIC_RELAXED, __HIP_MEMORY_SCOPE_AGENT); }
DI void stflag(char* ws, unsigned off, u64 v) { __hip_atomic_store((GAS u64*)(ws + off), v, __ATOMIC_RELAXED, __HIP_MEMORY_SCOPE_AGENT); }
DI void read_tag(Ctx& c) {
  const char* ws = c.p.ws; asm volatile("" : "+s"(ws));
  c.rs = __builtin_amdgcn_make_buffer_rsrc(c.p.ws, 0, WS_END, 0x00020000);
  c.tag = *(const __attribute__((address_space(4))) u64*)(ws + WS_EPOCH) + 1;
}
DI void poll256(const char* ws, unsigned off, u64 tag) {
#pragma unroll 1
  for (unsigned n = 0; n < SPIN_CAP; ++n) {
    const u64 a = ldflag(ws, off), b = ldflag(ws, off + 512), c = ldflag(ws, off + 1024), d = ldflag(ws, off + 1536);
    if (__all(a == tag && b == tag && c == tag && d == tag)) break;
    __builtin_amdgcn_s_sleep(4);
  }
}
template <int YOUNGER> DI void publish(const Ctx& c, int cnt_off, unsigned n, unsigned flag_off) {
  wait_vm<YOUNGER>();
  if (c.l == 0) {
    const unsigned old = __hip_atomic_fetch_add((LAS unsigned*)(c.lds + cnt_off), 1u, __ATOMIC_RELAXED, __HIP_MEMORY_SCOPE_WORKGROUP);
    if (old == n - 1) stflag(c.p.ws, flag_off, c.tag);
  }
}
template <int CTRL> DI float dpp_add(float v) { return v + __int_as_float(__builtin_amdgcn_update_dpp(0, __float_as_int(v), CTRL, 0xf, 0xf, true)); }
DI float swap16_add(float a, float b) { const auto t = __builtin_amdgcn_permlane16_swap(__float_as_uint(a), __float_as_uint(b), false, false); return __uint_as_float(t[0]) + __uint_as_float(t[1]); }
DI float swap32_add(float a, float b) { const auto t = __builtin_amdgcn_permlane32_swap(__float_as_uint(a), __float_as_uint(b), false, false); return __uint_as_float(t[0]) + __uint_as_float(t[1]); }
DI float swap32_max(float a) { const auto t = __builtin_amdgcn_permlane32_swap(__float_as_uint(a), __float_as_uint(a), false, false); return fmaxf(__uint_as_float(t[0]), __uint_as_float(t[1])); }
DI float upper_half(float a) { const auto t = __builtin_amdgcn_permlane32_swap(__float_as_uint(a), __float_as_uint(a), false, false); return __uint_as_float(t[1]); }
DI u32x4 swap32_pair(unsigned a0, unsigned a1, unsigned b0, unsigned b1) {
  const auto s0 = __builtin_amdgcn_permlane32_swap(a0, b0, false, false), s1 = __builtin_amdgcn_permlane32_swap(a1, b1, false, false);
  return u32x4{s0[0], s1[0], s0[1], s1[1]};
}
DI u32x4 swap16_pair(unsigned a0, unsigned a1, unsigned b0, unsigned b1) {
  const auto s0 = __builtin_amdgcn_permlane16_swap(a0, b0, false, false), s1 = __builtin_amdgcn_permlane16_swap(a1, b1, false, false);
  return u32x4{s0[0], s1[0], s0[1], s1[1]};
}
DI void colsum_step1(lptr part, int l, int w4, const u32x4 (&m)[3]) {
  float v[8], u[4], t[2];
#pragma unroll
  for (int i = 0; i < 4; ++i) { v[2 * i] = bflo(m[0][i]) + bflo(m[1][i]) + bflo(m[2][i]); v[2 * i + 1] = bfhi(m[0][i]) + bfhi(m[1][i]) + bfhi(m[2][i]); }
#pragma unroll
  for (int i = 0; i < 4; ++i) u[i] = swap32_add(v[i], v[i + 4]);
#pragma unroll
  for (int i = 0; i < 2; ++i) t[i] = dpp_add<0x128>(swap16_add(u[i], u[i + 2]));
  *(LAS f32x2*)(part + (w4 * 64 + (l & 7) * 8 + 4 * (l >> 5) + 2 * ((l >> 4) & 1)) * 4) = f32x2{t[0], t[1]};
}
DI float colsum_step2(lptr part, int l, int w4) {
  float v = *(LAS float*)(part + ((l >> 4) * 64 + 16 * w4 + (l & 15)) * 4);
  v = swap16_add(v, v); return swap32_add(v, v);
}
DI unsigned np(unsigned v) { return v < POISON - 1 ? v : POISON - 1; }
DI u32x4 np(u32x4 v) { return u32x4{np(v[0]), np(v[1]), np(v[2]), np(v[3])}; }
DI unsigned umax(unsigned a, unsigned b) { return a > b ? a : b; }
DI unsigned umin(unsigned a, unsigned b) { return a < b ? a : b; }
DI unsigned umax4(u32x4 v) { return umax(umax(v[0], v[1]), umax(v[2], v[3])); }
#define FENCE() asm volatile("" ::: "memory")
DI f32x16 lo16(const f32x32& s) { return __builtin_shufflevector(s, s, 0, 1, 2, 3, 4, 5, 6, 7, 8, 9, 10, 11, 12, 13, 14, 15); }
DI f32x16 hi16(const f32x32& s) { return __builtin_shufflevector(s, s, 16, 17, 18, 19, 20, 21, 22, 23, 24, 25, 26, 27, 28, 29, 30, 31); }
DI int swap23(int k) { return (k & ~12) | ((k & 4) << 1) | ((k & 8) >> 1); }

DI void row_stats(const Ctx& c, const u32x4 (&xv)[3], int xrow) {
  const int r = c.l & 31;
  float s1 = 0.f, s2 = 0.f;
#pragma unroll
  for (int i = 0; i < 3; ++i)
#pragma unroll
    for (int k = 0; k < 4; ++k) { const unsigned v = (i < 2 || r < 16) ? xv[i][k] : 0u; s1 = dot2(v, ONES2, s1); s2 = dot2(v, v, s2); }
  s1 = dpp_add<0x128>(dpp_add<0x141>(dpp_add<0x4E>(dpp_add<0xB1>(s1)))); s2 = dpp_add<0x128>(dpp_add<0x141>(dpp_add<0x4E>(dpp_add<0xB1>(s2))));
  s1 = swap16_add(s1, s1); s2 = swap16_add(s2, s2);
  const float mean = s1 * (1.f / D), var = fmaxf(s2 * (1.f / D) - mean * mean, 0.f), a = __builtin_amdgcn_rsqf(var + LN_EPS);
  const unsigned tag = (unsigned)c.tag;
  if (r == 0) st128_d(c.rs, WS_RS1 + xrow * 16, u32x4{__float_as_uint(a), tag, __float_as_uint(-mean * a), tag});
}
struct O1 { unsigned base[2], sm, sg; };
DI O1 p1_places(const Ctx& c, int w) {
  const int wm = w >> 1, wn = w & 1, r = c.l & 31, hh = c.l >> 5; const bool vt = c.ct >= 10;
  O1 o;
#pragma unroll
  for (int n = 0; n < 2; ++n) {
    const int f = (c.ct - 10) * 128 + wn * 64 + (n ^ wm) * 32 + r, h = f / 20, d = f - 20 * h, dd = (h & 1) ? (d < 4 ? d + 16 : d - 4) : d;
    o.base[n] = vt ? WS_V + (h * 256 + c.panel * 16 + wm * 8 + hh) * 320 + dd * 16 : WS_QK + ((c.ct * 16 + wn * 8 + (n ^ wm) * 4 + hh) * 2048 + c.panel * 128 + wm * 64 + r) * 16;
  }
  o.sm = vt ? 4 * 320 : 512; o.sg = vt ? 2 * 320 : 2 * 32768;
  return o;
}
DI void spare_poison(const Ctx& c) {
  u32x4 psn = {POISON, POISON, POISON, POISON};
  asm volatile("" : "+v"(psn));
  const unsigned q0 = ((c.b - 240) * 8 + c.w) * 17;
#pragma unroll 1
  for (unsigned q = q0; q < umin(q0 + 17, 2064); ++q) {
    const unsigned v = q - 720;
    asm volatile("buffer_store_dwordx4 %0, %1, %2, %3 offen sc1" :: "v"(psn), "v"(c.l * 16), "s"(c.rs),
                 "s"(q < 720 ? WS_QK + (80 + q / 9) * 32768 + q % 9 * 1024 : q < 1424 ? WS_V + v / 22 * 81920 + v % 22 * 1024 : WS_QK + (q - 1424) * 1024) : "memory");
  }
}
#define Q_X 96
#define Q_Y 128
#define QMK(S, k, m, n) "v_mfma_f32_32x32x16_bf16 " VR(32 + m * 32 + n * 16, 16) ", " VR(S + 16 + k * 8 + n * 4, 4) ", " VR(S + k * 8 + m * 4, 4) ", " VR(32 + m * 32 + n * 16, 16) "\n\t"
#define QMV(S, k, m, n) "v_mfma_f32_32x32x16_bf16 " VR(32 + m * 32 + n * 16, 16) ", " VR(S + k * 8 + m * 4, 4) ", " VR(S + 16 + k * 8 + n * 4, 4) ", " VR(32 + m * 32 + n * 16, 16) "\n\t"
#define QRA(S, k, m, ks) "ds_read_b128 " VR(S + k * 8 + m * 4, 4) ", v[160+" STR(ks) "] offset:" STR(m * 4096) "\n\t"
#define QRB(S, k, n, h, ks) "ds_read_b64_tr_b16 " VR(S + 16 + k * 8 + n * 4 + h * 2, 2) ", v[164+" STR(n) "] offset:" STR((ks) * 4096 + h * 1024) "\n\t"
#define QR0(S, k, ks) QRA(S, k, 0, ks) QRB(S, k, 0, 0, ks)
#define QR1(S, k, ks) QRB(S, k, 0, 1, ks) QRB(S, k, 1, 0, ks)
#define QR2(S, k, ks) QRB(S, k, 1, 1, ks) QRA(S, k, 1, ks)
#define QAD(i) "v_add_u32 v[160+" #i "], %[dl], v[160+" #i "]\n\t"
#define P1_LOOP(MM) \
  QR0(Q_Y, 0, 0) QR1(Q_Y, 0, 0) QR2(Q_Y, 0, 0) QR0(Q_Y, 1, 1) QR1(Q_Y, 1, 1) QR2(Q_Y, 1, 1) \
  "s_branch 2f\n" \
  "1:\n\t" \
  MM(Q_X, 0, 0, 1) QR0(Q_Y, 0, 0) MM(Q_X, 0, 1, 0) QR1(Q_Y, 0, 0) MM(Q_X, 0, 1, 1) QR2(Q_Y, 0, 0) \
  MM(Q_X, 1, 0, 0) QR0(Q_Y, 1, 1) MM(Q_X, 1, 0, 1) QR1(Q_Y, 1, 1) MM(Q_X, 1, 1, 0) QR2(Q_Y, 1, 1) MM(Q_X, 1, 1, 1) \
  "2:\n\t" \
  WL(9) MM(Q_Y, 0, 0, 0) QR0(Q_X, 0, 2) WL(9) MM(Q_Y, 0, 0, 1) QR1(Q_X, 0, 2) WL(10) MM(Q_Y, 0, 1, 0) QR2(Q_X, 0, 2) \
  MM(Q_Y, 0, 1, 1) QR0(Q_X, 1, 3) WL(11) MM(Q_Y, 1, 0, 0) QR1(Q_X, 1, 3) WL(11) MM(Q_Y, 1, 0, 1) QR2(Q_X, 1, 3) \
  WL(12) MM(Q_Y, 1, 1, 0) "s_add_u32 %[st], %[st], 1\n\ts_and_b32 %[dl], %[st], 3\n\ts_cselect_b32 %[dl], 32768, %[wr]\n\t" \
  MM(Q_Y, 1, 1, 1) QAD(0) QAD(1) \
  WL(9) MM(Q_X, 0, 0, 0) QAD(2) QAD(3) QAD(4) QAD(5) \
  "s_cmp_eq_u32 %[st], 12\n\ts_cbranch_scc1 3f\n\t" \
  WL(0) "s_barrier\n\ts_cmp_lg_u32 %[st], %[fk]\n\ts_cbranch_scc1 1b\n\t" \
  "s_mov_b64 exec, 1\n\tbuffer_store_dwordx2 v[168:169], v166, %[rs], 0 offen sc1\n\ts_mov_b64 exec, -1\n\ts_branch 1b\n" \
  "3:\n\t" WL(0) \
  MM(Q_X, 0, 0, 1) MM(Q_X, 0, 1, 0) MM(Q_X, 0, 1, 1) \
  MM(Q_X, 1, 0, 0) MM(Q_X, 1, 0, 1) MM(Q_X, 1, 1, 0) MM(Q_X, 1, 1, 1) \
  "s_nop 15\n\ts_nop 3"
#define P1_OPERANDS \
  : "+{v[32:63]}"(ac0), "+{v[64:95]}"(ac1), "+{v160}"(aoff[0]), "+{v161}"(aoff[1]), "+{v162}"(aoff[2]), "+{v163}"(aoff[3]), "+{v164}"(boff[0]), "+{v165}"(boff[1]), \
    [st] "+s"(st), [dl] "=&s"(dl) \
  : [wr] "s"(-98304), [fk] "s"(w == 0 ? 5 : 0), [rs] "s"(c.rs), "{v166}"(WS_F0 + c.b * 8), "{v[168:169]}"(c.tag) \
  : "memory", "scc", CL8(96, 97, 98, 99, 100, 101, 102, 103), CL8(104, 105, 106, 107, 108, 109, 110, 111), CL8(112, 113, 114, 115, 116, 117, 118, 119), CL8(120, 121, 122, 123, 124, 125, 126, 127), \
    CL8(128, 129, 130, 131, 132, 133, 134, 135), CL8(136, 137, 138, 139, 140, 141, 142, 143), CL8(144, 145, 146, 147, 148, 149, 150, 151), CL8(152, 153, 154, 155, 156, 157, 158, 159)
template <bool VT>
DI void p1_compute(const Ctx& c) {
  const int l = c.l, w = c.w, wm = w >> 1, wn = w & 1, r = l & 31, hh = l >> 5, q = (l & 15) >> 2, pp = l & 3;
  unsigned aoff[4], boff[2];
#pragma unroll
  for (int i = 0; i < 4; ++i) aoff[i] = c.lds0 + 65536 + (wm * 64 + r) * 128 + (((2 * i + hh) ^ ((r >> 1) & 7)) << 4);
#pragma unroll
  for (int n = 0; n < 2; ++n) boff[n] = c.lds0 + 65536 + 16384 + (8 * hh + q) * 256 + (((wn * 8 + (n ^ wm) * 4 + 2 * ((l >> 4) & 1) + (pp >> 1)) ^ (q << 2)) << 4) + 8 * (pp & 1);
  f32x32 ac0 = {}, ac1 = {};
  unsigned st = 2, dl;
  if (VT) asm volatile(P1_LOOP(QMV) P1_OPERANDS); else asm volatile(P1_LOOP(QMK) P1_OPERANDS);
  const f32x16 acc[2][2] = {{lo16(ac0), hi16(ac0)}, {lo16(ac1), hi16(ac1)}};
  LAS float* ra = (LAS float*)(c.lds + L1_FIN); LAS float* rb = ra + 128; LAS float* cs = ra + 256;
  const O1 o1 = p1_places(c, w);
  LDS_BAR();
  STAMP(3);
  float s0[2], s1[2]; f32x4 u4[2][4], v4[2][4];
#pragma unroll
  for (int i = 0; i < 2; ++i) {
    const int row0 = wm * 64 + i * 32, col0 = wn * 64 + (i ^ wm) * 32;
    s0[i] = VT ? cs[col0 + r] : ra[row0 + r]; s1[i] = VT ? 0.f : rb[row0 + r];
#pragma unroll
    for (int g = 0; g < 4; ++g) {
      u4[i][g] = *(LAS f32x4*)(VT ? ra + row0 + 8 * g + 4 * hh : cs + col0 + 8 * g + 4 * hh);
      v4[i][g] = VT ? *(LAS f32x4*)(rb + row0 + 8 * g + 4 * hh) : f32x4{};
    }
  }
  __builtin_amdgcn_sched_barrier(0);
#pragma unroll
  for (int m = 0; m < 2; ++m)
#pragma unroll
    for (int n = 0; n < 2; ++n) {
      unsigned pk[8];
#pragma unroll
      for (int g = 0; g < 4; ++g) {
        float y[4];
#pragma unroll
        for (int i = 0; i < 4; ++i) y[i] = VT ? fmaf(acc[m][n][4 * g + i], u4[m][g][i], v4[m][g][i] * s0[n]) : fmaf(acc[m][n][4 * g + i], s0[m], s1[m] * u4[n][g][i]);
        pk[2 * g] = cvtpk(y[0], y[1]); pk[2 * g + 1] = cvtpk(y[2], y[3]);
      }
#pragma unroll
      for (int gp = 0; gp < 2; ++gp) {
        const u32x4 v = swap32_pair(pk[4 * gp], pk[4 * gp + 1], pk[4 * gp + 2], pk[4 * gp + 3]);
        st128_d(c.rs, o1.base[n] + (m * o1.sm + gp * o1.sg), np(v));
      }
    }
}

#define EX(b, i) "v_exp_f32 v[" STR(b) "+" STR(i) "], v[" STR(b) "+" STR(i) "]\n\t"
#define EX3(b, i) EX(b, i) EX(b, i + 1) EX(b, i + 2)
#define EX4(b, i) EX3(b, i) EX(b, i + 3)
#define R_SA 32
#define R_SB 64
#define R_OA 96
#define R_OB 112
#define R_NA 128
#define R_NB 144
#define R_QA 160
#define R_QB 164
#define R_QR 168
#define R_KR 170
#define R_K 172
#define R_VA 176
#define R_VB 184
#define R_IDX 192
#define R_KAD 193
#define R_KRAD 194
#define R_VAD 195
#define PV(o, vreg, sreg) "v_smfmac_f32_32x32x32_bf16 " VR(o, 16) ", " VR(vreg, 4) ", " VR(sreg, 8) ", v" STR(R_IDX) "\n\t"
#define QK16(dst, q, nm) "v_mfma_f32_32x32x16_bf16 " VR(dst, 16) ", " VR(R_K, 4) ", " VR(q, 4) ", " VR(nm, 16) "\n\t"
#define STEP(C, N, VC, VN, AB, i, MID, KRLOAD) \
  QK16(N, R_QA, R_NA) \
  MID \
  EX4(C, 0) \
  PV(R_OB, VN + 4, N + 24) \
  "ds_read_b128 " VR(VN, 4) ", v" STR(R_VAD) " offset:" STR(i * 1408) "\n\t" \
  "ds_read_b128 " VR(VN + 4, 4) ", v" STR(R_VAD) " offset:" STR(i * 1408 + 704) "\n\t" \
  EX3(C, 4) \
  QK16(N + 16, R_QB, R_NB) \
  "ds_read_b128 " VR(R_K, 4) ", v" STR(R_KAD) " offset:" STR((i / 2) * 3072 + (i & 1) * 512) "\n\t" \
  EX4(C, 7) \
  PV(R_OA, VC, C) \
  "s_waitcnt lgkmcnt(3)\n\t" \
  EX4(C, 11) \
  "v_mfma_f32_32x32x4_2b_bf16 " VR(N, 32) ", " VR(R_KR, 2) ", " VR(R_QR, 2) ", " VR(N, 32) " cbsz:1 abid:" STR(AB) "\n\t" \
  EX4(C, 15) EX4(C, 19) \
  PV(R_OA, VC + 4, C + 8) \
  "s_waitcnt lgkmcnt(0)\n\t" \
  KRLOAD \
  EX4(C, 23) \
  PV(R_OB, VC, C + 16) \
  EX4(C, 27) EX(C, 31)
#define KRL(i) "ds_read_b64 " VR(R_KR, 2) ", v" STR(R_KRAD) " offset:" STR((i / 2) * 3072) "\n\t"
#define STAGE_TOP \
  "s_barrier\n\t" \
  "v_add_u32 v" STR(R_KAD) ", %[dk], v" STR(R_KAD) "\n\tv_add_u32 v" STR(R_KRAD) ", %[dk], v" STR(R_KRAD) "\n\tv_add_u32 v" STR(R_VAD) ", %[dv], v" STR(R_VAD) "\n\t" \
  "s_sub_u32 %[dk], 0, %[dk]\n\ts_sub_u32 %[dv], 0, %[dv]\n\t"

DI f32x32 cat16(const f32x16& a, const f32x16& b) {
  return __builtin_shufflevector(a, b, 0, 1, 2, 3, 4, 5, 6, 7, 8, 9, 10, 11, 12, 13, 14, 15, 16, 17, 18, 19, 20, 21, 22, 23, 24, 25, 26, 27, 28, 29, 30, 31);
}
DI float max16(const f32x16& s) { float m = s[0];
#pragma unroll
  for (int i = 1; i < 16; ++i) m = fmaxf(m, s[i]);
  return m; }
DI f32x16 splat16(float v) { f32x16 s;
#pragma unroll
  for (int i = 0; i < 16; ++i) s[i] = v;
  return s; }
DI bool out_of_range(const f32x16& o) {
  float chk = 0.f;
#pragma unroll
  for (int i = 0; i < 12; ++i) chk += fabsf(o[i]);
  return !(chk < 1e37f) || !(upper_half(o[8]) > 0.f);
}

DI void p2_compute(const Ctx& c, u32x2 (&res)[5]) {
  const int l = c.l, w = c.w, r = l & 31, hh = l >> 5, h = c.h;
  const int e = h & 1, cb = 5 * (h >> 1) + 2 * e, q0 = c.qb * 256 + w * 64;
  unsigned kad = c.lds0 + KRING + 12288 + (e + hh) * 1024 + r * 16, krad = c.lds0 + KRING + 12288 + (e ? 0 : 2048) + l * 16 + 8 * e;
  unsigned vad = c.lds0 + VRING + 11264 + hh * 352 + (r < 21 ? r : 21) * 16;
  LDS_BAR();
  publish<0>(c, L_CNT1, 4, WS_F1 + c.b * 8);
  STAMP(6);
  __builtin_amdgcn_s_sleep(QDELAY);
  const unsigned ko = WS_QK + ((80 + cb + e + hh) * 2048 + r) * 16, kro = WS_QK + ((80 + cb + (e ? 0 : 2)) * 2048 + r) * 16 + 8 * e;
  const unsigned vg = WS_V + (h * 256 + hh) * 320 + (r < 20 ? r : 0) * 16, pad = r == 20 ? ONES2 : 0u;
  s16x8 qa, qb, kf; s16x4 qr, kr; u32x4 k0, t0, t1;
#pragma unroll 1
  for (unsigned n = 0; n < SPIN_CAP; ++n) {
    FENCE();
    const u32x4 ta = ld128_d(c.rs, WS_QK + ((cb + e + hh) * 2048 + q0 + r) * 16), tb = ld128_d(c.rs, WS_QK + ((cb + e + hh) * 2048 + q0 + 32 + r) * 16);
    const u32x2 tr = ld64_d(c.rs, WS_QK + ((e ? cb : cb + 2) * 2048 + q0 + l) * 16 + 8 * e), tk = ld64_d(c.rs, kro + hh * 512);
    k0 = ld128_d(c.rs, ko); const u32x4 k1 = ld128_d(c.rs, ko + 512);
    t0 = ld128_d(c.rs, vg); t1 = ld128_d(c.rs, vg + 640);
    qa = __builtin_bit_cast(s16x8, ta); qb = __builtin_bit_cast(s16x8, tb); qr = __builtin_bit_cast(s16x4, tr);
    kf = __builtin_bit_cast(s16x8, k1); kr = __builtin_bit_cast(s16x4, tk);
    if (!__any(umax(umax(umax(umax4(ta), umax4(tb)), umax(umax4(k0), umax4(k1))), umax(umax(umax4(t0), umax4(t1)), umax(umax(tr[0], tr[1]), umax(tk[0], tk[1])))) == POISON)) break;
    __builtin_amdgcn_s_sleep(RDELAY);
  }
  STAMP(5);
  f32x32 sa = {}, sb = {};
  sa = __builtin_amdgcn_mfma_f32_32x32x4bf16_1k(kr, qr, sa, 1, 0, 0);
  f32x16 s0 = mfma32(__builtin_bit_cast(s16x8, k0), qa, lo16(sa)), s1 = mfma32(__builtin_bit_cast(s16x8, k0), qb, hi16(sa));
  f32x8 va = {}, vb = {};
#pragma unroll
  for (int i = 0; i < 4; ++i) { va[i] = __uint_as_float(r < 20 ? t0[i] : pad); va[4 + i] = __uint_as_float(r < 20 ? t1[i] : pad); }
  f32x16 na = splat16(-swap32_max(max16(s0))), nb = splat16(-swap32_max(max16(s1))), oa = {}, ob = {};
  sa = cat16(s0 + na, s1 + nb);
  unsigned idx = 0xDDDDDDDDu, cnt = 8, dk = -12288, dv = -11264;
  {
    const char* xg = c.p.x + (size_t)((c.rt * 64 + 32 * (w & 1) + 16 * (w >> 1) + (l & 15)) * 1280 + c.cs * 160 + (l >> 4) * 8);
#pragma unroll
    for (int nt = 0; nt < 5; ++nt) res[nt] = *(const GAS u32x2*)(xg + nt * 32);
  }
  STAMP(7);
  asm volatile(
      "s_waitcnt lgkmcnt(0)\n\ts_nop 4\n"
      "1:\n\t"
      STEP(R_SA, R_SB, R_VA, R_VB, 1, 0, STAGE_TOP, KRL(0))
      STEP(R_SB, R_SA, R_VB, R_VA, 0, 1, "", "")
      STEP(R_SA, R_SB, R_VA, R_VB, 1, 2, "", KRL(2))
      STEP(R_SB, R_SA, R_VB, R_VA, 0, 3, "", "")
      STEP(R_SA, R_SB, R_VA, R_VB, 1, 4, "", KRL(4))
      STEP(R_SB, R_SA, R_VB, R_VA, 0, 5, "", "")
      STEP(R_SA, R_SB, R_VA, R_VB, 1, 6, "", KRL(6))
      STEP(R_SB, R_SA, R_VB, R_VA, 0, 7, "", "")
      "s_sub_u32 %[cnt], %[cnt], 1\n\t"
      "s_cmp_lg_u32 %[cnt], 0\n\t"
      "s_cbranch_scc1 1b\n\t"
      "s_waitcnt lgkmcnt(0)\n\t"
      "s_barrier\n\t"
      PV(R_OB, R_VB + 4, R_SB + 24)
      "s_nop 15\n\t"
      : "+{v[32:63]}"(sa), "+{v[64:95]}"(sb), "+{v[96:111]}"(oa), "+{v[112:127]}"(ob), "+{v[170:171]}"(kr), "+{v[172:175]}"(kf), "+{v[176:183]}"(va), "+{v[184:191]}"(vb),
        "+{v193}"(kad), "+{v194}"(krad), "+{v195}"(vad), [cnt] "+s"(cnt), [dk] "+s"(dk), [dv] "+s"(dv)
      : "{v[128:143]}"(na), "{v[144:159]}"(nb), "{v[160:163]}"(qa), "{v[164:167]}"(qb), "{v[168:169]}"(qr), "{v192}"(idx)
      : "memory", "scc");
  STAMP(8);
  if (__any(out_of_range(oa) || out_of_range(ob))) {
    float ma = -INFINITY, mb = -INFINITY;
#pragma unroll 1
    for (int pass = 0; pass < 2; ++pass) {
      na = splat16(pass ? -ma : 0.f); nb = splat16(pass ? -mb : 0.f); oa = splat16(0.f); ob = splat16(0.f);
#pragma unroll 1
      for (int t = 0; t < 64; ++t) {
        const s16x8 k16 = __builtin_bit_cast(s16x8, ld128_d(c.rs, ko + t * 512));
        f32x32 s = cat16(mfma32(k16, qa, na), mfma32(k16, qb, nb));
        s = __builtin_amdgcn_mfma_f32_32x32x4bf16_1k(__builtin_bit_cast(s16x4, ld64_d(c.rs, kro + t * 512)), qr, s, 0, 0, 0);
        if (pass == 0) { ma = fmaxf(ma, max16(lo16(s))); mb = fmaxf(mb, max16(hi16(s))); }
        else {
#pragma unroll
          for (int g = 0; g < 2; ++g) {
            u32x4 vf = ld128_d(c.rs, vg + (4 * t + 2 * g) * 320);
            if (r >= 20) vf = u32x4{pad, pad, pad, pad};
            f32x8 pa, pb;
#pragma unroll
            for (int i = 0; i < 8; ++i) { pa[i] = __builtin_amdgcn_exp2f(s[8 * g + i]); pb[i] = __builtin_amdgcn_exp2f(s[16 + 8 * g + i]); }
            oa = __builtin_amdgcn_smfmac_f32_32x32x32_bf16(__builtin_bit_cast(bf16x8, vf), __builtin_bit_cast(bf16x16, pa), oa, 0xDDDD, 0, 0);
            ob = __builtin_amdgcn_smfmac_f32_32x32x32_bf16(__builtin_bit_cast(bf16x8, vf), __builtin_bit_cast(bf16x16, pb), ob, 0xDDDD, 0, 0);
          }
        }
      }
      if (pass == 0) { ma = swap32_max(ma); mb = swap32_max(mb); }
    }
  }
#pragma unroll
  for (int t = 0; t < 2; ++t) {
    const f32x16& o = t ? ob : oa;
    const float rl = __builtin_amdgcn_rcpf(upper_half(o[8]));
    unsigned pk[6];
#pragma unroll
    for (int i = 0; i < 6; ++i) pk[i] = cvtpk(o[2 * i] * rl, o[2 * i + 1] * rl);
    const unsigned og = WS_O + (q0 >> 6) * 81920 + (32 * t + r) * 16;
    st128_d(c.rs, og + (cb + e + hh) * 1024, np(swap32_pair(pk[0], pk[1], pk[2], pk[3])));
    if (hh == 0) st64_d(c.rs, og + (e ? cb : cb + 2) * 1024 + 8 * e, u32x2{np(pk[4]), np(pk[5])});
  }
}

#define G_ACC 32
#define G_B0 72
#define G_B1 92
#define G_A 112
#define G_VB 176
#define G_VBO 177
#define G_VA0 178
#define G_VA1 180
#define G_CHK 182
#define GMM(m, nt, B, j) "v_mfma_f32_16x16x32_bf16 " VR(G_ACC + m * 20 + nt * 4, 4) ", " VR(B + nt * 4, 4) ", " VR(G_A + j * 8 + m * 4, 4) ", " VR(G_ACC + m * 20 + nt * 4, 4) "\n\t"
#define GRB(B, nt, ks) "ds_read_b64_tr_b16 " VR(B + nt * 4, 2) ", v" STR(G_VB) " offset:" STR(ks * 5120 + nt * 32) "\n\t" \
                       "ds_read_b64_tr_b16 " VR(B + nt * 4 + 2, 2) ", v" STR(G_VB) " offset:" STR(ks * 5120 + nt * 32 + 1280) "\n\t"
#define GCK(j, i) "v_max3_u32 v" STR(G_CHK) ", v[" STR(G_A + j * 8 + i * 2) "], v[" STR(G_A + j * 8 + i * 2 + 1) "], v" STR(G_CHK) "\n\t"
#define GLD1(j, m, va) "buffer_load_dwordx4 " VR(G_A + (j) * 8 + m * 4, 4) ", v[" STR(va + m) "], %[rs], %[so] offen sc1\n\t"
#define GADV "s_add_u32 %[so], %[so], 16384\n\ts_cmp_eq_u32 %[so], %[sw]\n\ts_cselect_b32 %[so], 0, %[so]\n\t"
#define GLD2(j) GLD1(j, 0, G_VA0) GLD1(j, 1, G_VA0) GLD1(j + 1, 0, G_VA1) GLD1(j + 1, 1, G_VA1) GADV
#define GNEXT "s_add_u32 %[sl], %[sl], 20480\n\ts_cmp_eq_u32 %[sl], %[lw]\n\ts_cselect_b32 %[sl], %[lr], %[sl]\n\t"
#define GADR "v_add_u32 v" STR(G_VB) ", %[sl], v" STR(G_VBO) "\n\t"
#define GBODY(j, BC, BN, ks, VM, W0, PRE, L0, L1) \
  "s_waitcnt vmcnt(" STR(VM) ") lgkmcnt(" STR(W0) ")\n\t" PRE \
  GMM(0, 0, BC, j) GRB(BN, 0, ks) GMM(1, 0, BC, j) GRB(BN, 1, ks) WL(10) GMM(0, 1, BC, j) GRB(BN, 2, ks) GMM(1, 1, BC, j) GRB(BN, 3, ks) \
  WL(12) GMM(0, 2, BC, j) GRB(BN, 4, ks) GMM(1, 2, BC, j) GCK(j, 0) GCK(j, 1) WL(12) GMM(0, 3, BC, j) GCK(j, 2) GMM(1, 3, BC, j) GCK(j, 3) \
  WL(10) GMM(0, 4, BC, j) L0 GMM(1, 4, BC, j) L1
#define GEVEN(j) GBODY(j, G_B0, G_B1, 1, 14, 8, "", GLD1(j, 0, G_VA0) GNEXT, GLD1(j, 1, G_VA0))
#define GODD(j) GBODY(j, G_B1, G_B0, 0, 14, 8, GADR, GLD1(j, 0, G_VA1), GLD1(j, 1, G_VA1) GADV)
#define GODDB(j) GBODY(j, G_B1, G_B0, 0, 14, 0, "s_barrier\n\t" GADR, GLD1(j, 0, G_VA1), GLD1(j, 1, G_VA1) GADV)
#define TEVEN(j) GBODY(j, G_B0, G_B1, 1, 14 - 2 * j, 8, "", GNEXT, "")
#define TODD(j) GBODY(j, G_B1, G_B0, 0, 14 - 2 * j, 8, GADR, "", "")

DI void g80_compute(const Ctx& c, int last, u32x2 (&res)[5]) {
  int l = c.l; asm volatile("" : "+v"(l));
  const int w = c.w, wm = w & 1, wk = w >> 1, g = l >> 4, q = (l & 15) >> 2, pp = l & 3;
  const unsigned tile = last ? WS_F + c.rt * 196608 : WS_O + c.rt * 81920;
  __builtin_amdgcn_s_barrier();
  const int row = 32 * wm + 16 * wk + (l & 15);
  f32x32 acc0 = {}; f32x8 acc1;
#pragma unroll
  for (int i = 0; i < 20; ++i) { const float v = (i & 1) ? bfhi(res[i >> 2][(i >> 1) & 1]) : bflo(res[i >> 2][(i >> 1) & 1]); if (i < 12) acc0[20 + i] = v; else acc1[i - 12] = v; }
  asm volatile("" : "+{v[32:63]}"(acc0), "+{v[64:71]}"(acc1));
  __builtin_amdgcn_s_sleep(PDELAY);
  {
    const int hd = l & 31, e = hd & 1, cb = 5 * (hd >> 1) + 2 * e;
    const unsigned po = tile + 63 * 16 + (last ? (hd >> 2) * 24 + (hd & 3) * 6 + 5 : (e ? cb : cb + 2)) * 1024 + (last ? 0 : 8 * e);
#pragma unroll 1
    for (unsigned n = 0; n < SPIN_CAP; ++n) { FENCE(); if (!__any(ld32_d(c.rs, po) == POISON)) break; __builtin_amdgcn_s_sleep(PSLEEP); }
  }
  if (last) STAMP(16); else STAMP(10);
  const unsigned va0 = tile + (8 * wk + g) * 1024 + row * 16;
  const unsigned r0 = last ? c.rot5 : (5 * c.cs) >> 3;
  unsigned chk = 0, so = r0 * 16384, sl = last ? 0 : L_WO + r0 * 20480, cnt = last ? 2 : 1;
  asm volatile(
      "s_cmp_eq_u32 %[cnt], 1\n\ts_cbranch_scc0 4f\n\t"
      GLD2(6)
      "4:\n\t"
      GLD2(0) GLD2(2) TS(0)
      "s_barrier\n\t" TS(1)
      "v_add_u32 v" STR(G_VB) ", %[sl], v" STR(G_VBO) "\n\t"
      GRB(G_B0, 0, 0) GRB(G_B0, 1, 0) GRB(G_B0, 2, 0) GRB(G_B0, 3, 0) GRB(G_B0, 4, 0)
      GLD2(4)
      "s_cmp_eq_u32 %[cnt], 1\n\ts_cbranch_scc1 2f\n\t"
      GLD2(6)

      "1:\n\t"
      GEVEN(0) GODD(1) GEVEN(2) GODD(3) GEVEN(4) GODD(5)
      "2:\n\t"
      GEVEN(6)
      GODDB(7)
      "s_sub_u32 %[cnt], %[cnt], 1\n\ts_cmp_lg_u32 %[cnt], 0\n\ts_cbranch_scc1 1b\n\t"
      TEVEN(0) TODD(1) TEVEN(2) TODD(3) TEVEN(4) TODD(5) TEVEN(6)
      "s_waitcnt vmcnt(0) lgkmcnt(0)\n\ts_barrier\n\t"
      GMM(0, 0, G_B1, 7) GMM(1, 0, G_B1, 7) GCK(7, 0) GMM(0, 1, G_B1, 7) GCK(7, 1) GMM(1, 1, G_B1, 7) GCK(7, 2) GMM(0, 2, G_B1, 7) GCK(7, 3)
      GMM(1, 2, G_B1, 7) GMM(0, 3, G_B1, 7) GMM(1, 3, G_B1, 7) GMM(0, 4, G_B1, 7) GMM(1, 4, G_B1, 7)
      "s_nop 15\n\t" TS(5)
      : "+{v[32:63]}"(acc0), "+{v[64:71]}"(acc1), "+{v182}"(chk), [so] "+s"(so), [sl] "+s"(sl), [cnt] "+s"(cnt)
      : "{v177}"(c.lds0 + (64 * wk + 16 * (g >> 1) + 4 * (g & 1) + q) * 160 + pp * 8), "{v178}"(va0 ^ 256), "{v179}"(va0), "{v180}"((va0 ^ 256) + 4096), "{v181}"(va0 + 4096), [rs] "s"(c.rs),
        [sw] "s"(last ? 196608 : 81920), [lw] "s"(last ? 163840 : L_WO + 102400), [lr] "s"(last ? 0 : L_WO)
      : "memory", "scc", "v176", CL8(160, 161, 162, 163, 164, 165, 166, 167), CL8(168, 169, 170, 171, 172, 173, 174, 175),
        "v72", "v73", "v74", "v75", "v76", "v77", "v78", "v79", "v80", "v81", "v82", "v83", "v84", "v85", "v86", "v87", "v88", "v89", "v90", "v91", "v92", "v93", "v94", "v95",
        "v96", "v97", "v98", "v99", "v100", "v101", "v102", "v103", "v104", "v105", "v106", "v107", "v108", "v109", "v110", "v111", "v112", "v113", "v114", "v115", "v116", "v117", "v118", "v119",
        "v120", "v121", "v122", "v123", "v124", "v125", "v126", "v127", "v128", "v129", "v130", "v131", "v132", "v133", "v134", "v135", "v136", "v137", "v138", "v139", "v140", "v141", "v142", "v143",
        "v144", "v145", "v146", "v147", "v148", "v149", "v150", "v151", "v152", "v153", "v154", "v155", "v156", "v157", "v158", "v159" TS_CLOB);
  if (last) { TS_PUT(0, 25); TS_PUT(1, 26); TS_PUT(2, 27); TS_PUT(3, 28); TS_PUT(4, 29); TS_PUT(5, 30); } else { TS_PUT(0, 19); TS_PUT(1, 20); TS_PUT(4, 21); TS_PUT(5, 22); }
  f32x4 acc[2][5];
#pragma unroll
  for (int i = 0; i < 40; ++i) acc[i / 20][(i % 20) / 4][i % 4] = i < 32 ? acc0[i] : acc1[i - 32];
  if (__any(chk == POISON)) {
    const char* wg = (last ? c.p.w2 : c.p.wo) + c.cs * 160 + (l & 15) * 2;
#pragma unroll
    for (int nt = 0; nt < 5; ++nt) { acc[0][nt] = f32x4{}; acc[1][nt] = f32x4{bflo(res[nt][0]), bfhi(res[nt][0]), bflo(res[nt][1]), bfhi(res[nt][1])}; }
#pragma unroll 1
    for (int st = 0; st < (last ? 24 : 10); ++st) {
      u32x4 a0, a1;
#pragma unroll 1
      for (unsigned n = 0; n < SPIN_CAP; ++n) {
        FENCE();
        a0 = ld128_d(c.rs, (va0 ^ 256) + (st >> 1) * 16384 + (st & 1) * 4096); a1 = ld128_d(c.rs, va0 + (st >> 1) * 16384 + (st & 1) * 4096);
        if (!__any(umax(umax4(a0), umax4(a1)) == POISON)) break;
      }
      const char* wr = wg + (size_t)((st >> 1) * 128 + 64 * wk + (st & 1) * 32 + 8 * g) * 1280;
#pragma unroll
      for (int nt = 0; nt < 5; ++nt) {
        s16x8 bf;
#pragma unroll
        for (int j = 0; j < 8; ++j) bf[j] = *(const GAS short*)(wr + j * 1280 + nt * 32);
        acc[0][nt] = mfma16(bf, __builtin_bit_cast(s16x8, a0), acc[0][nt]); acc[1][nt] = mfma16(bf, __builtin_bit_cast(s16x8, a1), acc[1][nt]);
      }
    }
  }
  lptr ex = c.lds + L_EX + l * 16;
#pragma unroll
  for (int nt = 0; nt < 5; ++nt) *(LAS f32x4*)(ex + w * 5120 + nt * 1024) = acc[0][nt];
  const rsrc_t ro = __builtin_amdgcn_make_buffer_rsrc(last ? c.p.out : c.p.ws + WS_H, 0, S * D * 2, 0x00020000);
  const unsigned ob = c.rt * 81920 + (last ? c.cs * 160 + row * 1280 : c.cs * 10240 + row * 16), oc = last ? 16 : 1024;
  unsigned so3[3];
#pragma unroll
  for (int i = 0; i < 3; ++i) so3[i] = ob + (4 * i + (i < 2 ? 2 * (g & 1) : 0) + (g >> 1)) * oc;
  asm volatile("" : "+v"(so3[0]), "+v"(so3[1]), "+v"(so3[2]));
  LDS_BAR();
  if (last) STAMP(17); else STAMP(11);
  f32x4 o4[5];
#pragma unroll
  for (int nt = 0; nt < 5; ++nt) o4[nt] = *(LAS f32x4*)(ex + (w ^ 2) * 5120 + nt * 1024);
  __builtin_amdgcn_sched_barrier(0);
#pragma unroll
  for (int nt = 0; nt < 5; ++nt) { const f32x4 u = acc[1][nt] + o4[nt]; res[nt] = u32x2{np(cvtpk(u[0], u[1])), np(cvtpk(u[2], u[3]))}; }
#pragma unroll
  for (int i = 0; i < 3; ++i) { const int n1 = i < 2 ? 2 * i + 1 : 4; st128_d(ro, so3[i], swap16_pair(res[2 * i][0], res[2 * i][1], res[n1][0], res[n1][1])); }
  if (!last) {
    STAMP(12);
    __builtin_amdgcn_s_barrier();
    float s1 = 0.f, s2 = 0.f;
#pragma unroll
    for (int nt = 0; nt < 5; ++nt) { s1 = dot2(res[nt][0], ONES2, dot2(res[nt][1], ONES2, s1)); s2 = dot2(res[nt][0], res[nt][0], dot2(res[nt][1], res[nt][1], s2)); }
    s1 = swap16_add(s1, s1); s2 = swap16_add(s2, s2); s1 = swap32_add(s1, s1); s2 = swap32_add(s2, s2);
    if (l < 16) st64_d(c.rs, WS_RS2 + (c.rt * 8 + c.cs) * 512 + row * 8, u32x2{np(__float_as_uint(s1)), np(__float_as_uint(s2))});
  }
}

#define F_X 80
#define F_Y 108
#define FMM(S, nt, m) "v_mfma_f32_16x16x32_bf16 " VR(32 + nt * 16 + m * 4, 4) ", " VR(S + nt * 4, 4) ", " VR(S + 12 + m * 4, 4) ", " VR(32 + nt * 16 + m * 4, 4) "\n\t"
#define FRA(S, m, ks) "ds_read_b128 " VR(S + 12 + m * 4, 4) ", v140 offset:" STR(ks * 4096 + m * 256) "\n\t"
#define FRB(S, nt, h, ks) "ds_read_b64_tr_b16 " VR(S + nt * 4 + h * 2, 2) ", v[141+" STR(nt) "] offset:" STR(ks * 12288 + h * 3072) "\n\t"
#define FRF(m) "ds_read2st64_b32 v[144+2*" #m ":145+2*" #m "], v152 offset0:0 offset1:1\n\tv_add_u32 v152, 64, v152\n\t"
DI void p4_compute(const Ctx& c) {
  int l = c.l; asm volatile("" : "+v"(l));
  const int w = c.w, g = l >> 4, q = (l & 15) >> 2, pp = l & 3;
  unsigned boff[3];
#pragma unroll
  for (int nt = 0; nt < 3; ++nt) boff[nt] = c.lds0 + (16 * (g >> 1) + 4 * (g & 1) + q) * 384 + (((3 * w + nt) ^ (2 * (g & 1) + (q >> 1))) * 32) + pp * 8;
  f32x32 ac0 = {}; f32x16 ac1 = {}; f32x8 ab;
  unsigned ch = 0, sa, sb = 0, rap = c.lds0 + L_RA + (l & 15) * 4;
  f32x4 c4[3];
#pragma unroll
  for (int nt = 0; nt < 3; ++nt) c4[nt] = *(LAS f32x4*)(c.lds + L_CSUM + (w * 48 + nt * 16 + 4 * g) * 4);
  asm volatile("" : "+v"(c4[0]), "+v"(c4[1]), "+v"(c4[2]));
  asm volatile(
      "v_mov_b32 v140, v136\n\tv_mov_b32 v141, v137\n\tv_mov_b32 v142, v138\n\tv_mov_b32 v143, v139\n\t"
      "s_barrier\n\t" TS(0)
      FRB(F_Y, 0, 0, 0) FRB(F_Y, 0, 1, 0) FRA(F_Y, 0, 0) FRA(F_Y, 1, 0) FRA(F_Y, 2, 0) FRA(F_Y, 3, 0) FRB(F_Y, 1, 0, 0) FRB(F_Y, 1, 1, 0) FRB(F_Y, 2, 0, 0) FRB(F_Y, 2, 1, 0)
      "s_branch 2f\n"
      "1:\n\t"
      FMM(F_X, 0, 2) FRB(F_Y, 0, 0, 0) FMM(F_X, 0, 3) FRB(F_Y, 0, 1, 0) FMM(F_X, 1, 0) FRA(F_Y, 0, 0) FMM(F_X, 1, 1) FRA(F_Y, 1, 0) FMM(F_X, 1, 2) FRA(F_Y, 2, 0)
      FMM(F_X, 1, 3) FRA(F_Y, 3, 0) FMM(F_X, 2, 0) FRB(F_Y, 1, 0, 0) FMM(F_X, 2, 1) FRB(F_Y, 1, 1, 0) FMM(F_X, 2, 2) FRB(F_Y, 2, 0, 0) FMM(F_X, 2, 3) FRB(F_Y, 2, 1, 0)
      "2:\n\t"
      WL(7) FMM(F_Y, 0, 0) FRB(F_X, 0, 0, 1) WL(7) FMM(F_Y, 0, 1) FRB(F_X, 0, 1, 1) WL(7) FMM(F_Y, 0, 2) FRA(F_X, 0, 1) WL(7) FMM(F_Y, 0, 3) FRA(F_X, 1, 1)
      WL(6) FMM(F_Y, 1, 0) FRA(F_X, 2, 1) "s_add_u32 %[ch], %[ch], 1\n\t" FMM(F_Y, 1, 1) FRA(F_X, 3, 1) "s_add_u32 %[sb], %[sb], 24576\n\t"
      FMM(F_Y, 1, 2) FRB(F_X, 1, 0, 1) "s_cmp_eq_u32 %[ch], 5\n\t" FMM(F_Y, 1, 3) FRB(F_X, 1, 1, 1)
      WL(8) FMM(F_Y, 2, 0) FRB(F_X, 2, 0, 1) "s_cselect_b32 %[sb], 0, %[sb]\n\t" FMM(F_Y, 2, 1) FRB(F_X, 2, 1, 1) "s_and_b32 %[sa], %[ch], 3\n\t"
      FMM(F_Y, 2, 2) "s_lshl_b32 %[sa], %[sa], 13\n\tv_add_u32 v141, %[sb], v137\n\t" FMM(F_Y, 2, 3) "v_add_u32 v142, %[sb], v138\n\t"
      WL(7) FMM(F_X, 0, 0) "v_add_u32 v143, %[sb], v139\n\t" WL(6) FMM(F_X, 0, 1) "v_add_u32 v140, %[sa], v136\n\ts_cmp_eq_u32 %[ch], 10\n\t"
      WL(0) "s_barrier\n\t"
      "s_cbranch_scc0 1b\n\t"
      FMM(F_X, 0, 2) FRF(0) FMM(F_X, 0, 3) FRF(1) FMM(F_X, 1, 0) FRF(2) FMM(F_X, 1, 1) FRF(3) FMM(F_X, 1, 2) FMM(F_X, 1, 3) FMM(F_X, 2, 0) FMM(F_X, 2, 1) FMM(F_X, 2, 2) FMM(F_X, 2, 3)
      WL(0) "s_nop 7"
      : "+{v[32:63]}"(ac0), "+{v[64:79]}"(ac1), "={v[144:151]}"(ab), "+{v152}"(rap), [ch] "+s"(ch), [sa] "=&s"(sa), [sb] "+s"(sb)
      : "{v136}"(c.lds0 + L4_A + g * 1024 + (l & 15) * 16), "{v137}"(boff[0]), "{v138}"(boff[1]), "{v139}"(boff[2])
      : "memory", "scc", "v140", "v141", "v142", "v143", CL8(80, 81, 82, 83, 84, 85, 86, 87), CL8(88, 89, 90, 91, 92, 93, 94, 95), CL8(96, 97, 98, 99, 100, 101, 102, 103), CL8(104, 105, 106, 107, 108, 109, 110, 111),
        CL8(112, 113, 114, 115, 116, 117, 118, 119), CL8(120, 121, 122, 123, 124, 125, 126, 127), CL8(128, 129, 130, 131, 132, 133, 134, 135) TS_CLOB);
  TS_PUT(0, 13);
  f32x4 acc[4][3];
#pragma unroll
  for (int i = 0; i < 48; ++i) acc[(i >> 2) & 3][i >> 4][i & 3] = i < 32 ? ac0[i] : ac1[i - 32];
  STAMP(14);
#pragma unroll
  for (int nt = 0; nt < 3; ++nt)
#pragma unroll
    for (int mp = 0; mp < 2; ++mp) {
      unsigned pk[2][2];
#pragma unroll
      for (int j = 0; j < 2; ++j) {
        const int m = 2 * mp + j; float y[4];
#pragma unroll
        for (int i = 0; i < 4; ++i) y[i] = fmaf(acc[m][nt][i], ab[2 * m], ab[2 * m + 1] * c4[nt][i]);
        pk[j][0] = relu2(cvtpk(y[0], y[1])); pk[j][1] = relu2(cvtpk(y[2], y[3]));
      }
      st128_d(c.rs, WS_F + c.rt * 196608 + (c.cs * 24 + w * 6 + nt * 2 + (g >> 1)) * 1024 + (32 * mp + (g & 1) * 16 + (l & 15)) * 16, swap16_pair(pk[0][0], pk[0][1], pk[1][0], pk[1][1]));
    }
}

DI void compute_waves(Ctx& c) {
  const Params& p = c.p;
  read_tag(c);
  const int l = c.l, w = c.w, b = c.b, r = l & 31, hh = l >> 5;
  if (c.ct == 15) { __builtin_amdgcn_s_sleep(CPD); spare_poison(c); }
  {
    const int xrow = c.xr0 + 2 * w + hh, rg = c.wi & 7, cg = 15 * (b & 1) + (c.wi >> 3);
    LDS_BAR();
    lptr xs = c.lds + L1_XS + (2 * w + hh) * 1280 + r * 16, ws = c.lds + L1_WS + (w * 64 + l) * 16;
    const u32x4 xv[3] = {*(LAS u32x4*)xs, *(LAS u32x4*)(xs + 512), *(LAS u32x4*)(xs + 1024)};
    u32x4 wv[3] = {*(LAS u32x4*)ws, *(LAS u32x4*)(ws + 4096), {}};
    if (w < 2) wv[2] = *(LAS u32x4*)(ws + 8192);
    row_stats(c, xv, xrow);
    colsum_step1(c.lds + L_PART, l, w, wv);
    LDS_BAR();
    const float v = colsum_step2(c.lds + L_PART, l, w);
    if (l < 16 && c.ct < 15) st64_d(c.rs, WS_CS1 + (rg * 1920 + cg * 64 + 16 * w + l) * 8, u32x2{__float_as_uint(v), (unsigned)c.tag});
    STAMP(1);
    if (c.ct < 10) p1_compute<false>(c);
    else if (c.ct < 15) p1_compute<true>(c);
    else publish<0>(c, L_CNT0 + 12, 8, WS_F0 + c.b * 8);
  }
  STAMP(4);
  u32x2 res[5];
  p2_compute(c, res);
  STAMP(9);
  int last = 0;
#pragma unroll 1
  for (;;) {
    asm volatile("" : "+s"(last));
    g80_compute(c, last, res);
    if (last) break;
    p4_compute(c);
    STAMP(15);
    asm volatile(".Lblk_end1:");
    last = 1;
  }
  STAMP(18);
  asm volatile(".Lblk_end2:");
}

struct Park { f32x32 a, b, c, d, e; f32x16 f; f32x4 g; };
#define PARK_REGS(m, pk) m "{v[76:107]}"(pk.a), m "{v[108:139]}"(pk.b), m "{v[140:171]}"(pk.c), m "{v[172:203]}"(pk.d), m "{v[204:235]}"(pk.e), m "{v[236:251]}"(pk.f), m "{v[252:255]}"(pk.g)
#define PKL1(k, j, o) "buffer_load_dwordx4 " VR(76 + k * 20 + j * 4, 4) ", %[v" #j "], %[rs], %[so] offen offset:" STR(o) "\n\t"
#define PKL(k, ch) "s_add_u32 %[so], %[r5], " STR(ch * 163840 - 1966080) "\n\ts_cselect_b32 %[so], %[so], " STR(ch * 163840) "\n\ts_cselect_b32 %[t], 0, %[r5]\n\ts_add_u32 %[so], %[so], %[t]\n\t" PKL1(k, 0, 0) PKL1(k, 1, 1024) PKL1(k, 2, 2048) PKL1(k, 3, 3072) PKL1(k, 4, 0)
#define W80_OPS(r) [rs] "s"(r), [v0] "v"(vo80[0]), [v1] "v"(vo80[1]), [v2] "v"(vo80[2]), [v3] "v"(vo80[3]), [v4] "v"(vo80[4])
#define W80_DMA(r, dst, off) asm volatile("s_mov_b32 m0, %[d]\n\ts_nop 0\n\t" \
    "buffer_load_dwordx4 %[v0], %[rs], %[so] offen lds\n\tbuffer_load_dwordx4 %[v1], %[rs], %[so] offen offset:1024 lds\n\t" \
    "buffer_load_dwordx4 %[v2], %[rs], %[so] offen offset:2048 lds\n\tbuffer_load_dwordx4 %[v3], %[rs], %[so] offen offset:3072 lds\n\t" \
    "s_add_u32 m0, %[d], 4096\n\ts_nop 0\n\tbuffer_load_dwordx4 %[v4], %[rs], %[so] offen lds" :: [d] "s"(dst), [so] "s"(off), W80_OPS(r) : "memory", "scc")
#define PKS1(k, j) "ds_write_b128 %[va], " VR(76 + k * 20 + j * 4, 4) " offset:" STR(j * 1024) "\n\t"
#define PUT(k, slot) asm volatile(PKS1(k, 0) PKS1(k, 1) PKS1(k, 2) PKS1(k, 3) PKS1(k, 4) :: [va] "v"(wa + slot * 20480), PARK_REGS("", pk) : "memory")
#define P1_STATE [sa] "+s"(sa), [sb] "+s"(sb), [md] "+s"(md)
#define P1_CONST [rx] "s"(rx), [rw] "s"(rw), [l0] "s"(lds0 + lw * 4096), [a0] "v"(va[0]), [a1] "v"(va[1]), [a2] "v"(va[2]), [a3] "v"(va[3]), [b0] "v"(vb[0]), [b1x] "v"(vb[1]), [b2] "v"(vb[2]), [b3] "v"(vb[3])
#define P1_LD(v, r, s, o) "buffer_load_dwordx4 %[" v "], %[" r "], %[" s "] offen offset:" #o " lds\n\t"
#define P1_STAGE \
  "s_mov_b32 m0, %[md]\n\ts_add_u32 %[md], %[md], 16384\n\t" \
  P1_LD("a0", "rx", "sa", 0) P1_LD("a1", "rx", "sa", 1024) P1_LD("a2", "rx", "sa", 2048) P1_LD("a3", "rx", "sa", 3072) \
  "s_mov_b32 m0, %[md]\n\ts_add_u32 %[sa], %[sa], 128\n\t" \
  P1_LD("b0", "rw", "sb", 0) P1_LD("b1x", "rw", "sb", 1024) P1_LD("b2", "rw", "sb", 2048) P1_LD("b3", "rw", "sb", 3072) \
  "s_add_u32 %[sb], %[sb], 245760\n\ts_cmp_eq_u32 %[sa], 1280\n\ts_cselect_b32 %[sa], 0, %[sa]\n\ts_cselect_b32 %[sb], 0, %[sb]\n\t" \
  "s_sub_u32 %[md], %[md], %[l0]\n\ts_add_u32 %[md], %[md], 16384\n\ts_and_b32 %[md], %[md], 0x18000\n\ts_add_u32 %[md], %[md], %[l0]\n\t"
#define W1_OPS [rw] "s"(rw1), [dw] "s"(dw), [v0] "v"(vo[0]), [v1] "v"(vo[1]), [v2] "v"(vo[2]), [v3] "v"(vo[3]), [v4] "v"(vo[4]), [v5] "v"(vo[5])
#define LW1(v, o) "buffer_load_dwordx4 %[" v "], %[rw], %[t0] offen offset:" STR(o) " lds\n\t"
#define LW3_0 LW1("v0", 0) LW1("v1", 1024) LW1("v2", 2048)
#define LW3_1 LW1("v3", 0) LW1("v4", 1024) LW1("v5", 2048)
#define LW3(ch, u) "s_add_u32 m0, %[dw], " STR((ch - 5 * (ch / 5)) * 24576 + u * 3072) "\n\ts_mov_b32 %[t0], " STR(ch * 196608) "\n\t" LW3_##u
#define LA(ch) "s_add_u32 m0, %[da], " STR((ch & 3) * 8192) "\n\ts_mov_b32 %[t0], " STR(ch * 8192) "\n\t" \
  "buffer_load_dwordx4 %[va], %[rs], %[t0] offen sc1 lds\n\tbuffer_load_dwordx4 %[va], %[rs], %[t0] offen offset:1024 sc1 lds\n\t"
#define CKR(ch) "ds_read_b128 v[60:63], %[la] offset:" STR((ch & 3) * 8192) "\n\tds_read_b128 v[64:67], %[la] offset:" STR((ch & 3) * 8192 + 1024) "\n\t"
#define CKT "s_waitcnt lgkmcnt(0)\n\tv_max3_u32 v60, v60, v61, v62\n\tv_max3_u32 v64, v63, v64, v65\n\tv_max3_u32 v60, v60, v66, v67\n\tv_max_u32 v60, v60, v64\n\tv_cmp_eq_u32 vcc, -1, v60\n\t"
#define FIX(ch) "s_cbranch_vccz 2f\n\ts_mov_b32 %[t1], 0\n1:\n\ts_sleep 2\n\t" LA(ch) "s_waitcnt vmcnt(0)\n\t" CKR(ch) CKT \
  "s_cbranch_vccz 2f\n\ts_add_u32 %[t1], %[t1], 1\n\ts_cmp_lt_u32 %[t1], 0x40000\n\ts_cbranch_scc1 1b\n2:\n\t"
#define IVL(ch, VA, FETCH, VW) "s_waitcnt vmcnt(" STR(VA) ")\n\t" CKR(ch) FETCH CKT FIX(ch) "s_waitcnt vmcnt(" STR(VW) ")\n\t"

DI void loader_waves(Ctx& c) {
  const Params& p = c.p;
  const int l = c.l, lw = c.w - 4, b = c.b, t4 = lw * 64 + l, cs = c.cs, rt = c.rt;
  const unsigned lds0 = c.lds0, lin = l * 16;
  {
    {
      unsigned vo = lw < 2 ? lin : (l >> 3) * 3840 + (l & 7) * 16;
      unsigned so = lw < 2 ? c.xr0 * 1280 + lw * 5120 : ((c.wi & 7) * 80 + (lw - 2) * 40) * 3840 + (15 * (b & 1) + (c.wi >> 3)) * 128, st = lw < 2 ? 1024 : 8 * 3840;
      int isx = lw;
      asm volatile("" : "+s"(isx), "+v"(vo), "+s"(so), "+s"(st));
      __builtin_amdgcn_sched_barrier(0);
      STAMP(0);
      const char* src = (isx < 2 ? p.x : p.wqkv) + so;
#pragma unroll
      for (int i = 0; i < 5; ++i) glds(src + i * st, vo, lds0 + L1_XS + lw * 5120 + i * 1024);
      __builtin_amdgcn_sched_barrier(0);
    }
    const int rot = (5 * ((c.ct + 2 * c.panel) & 7)) >> 2;
    unsigned va[4], vb[4], sa = rot * 128, sb = rot * 245760, md = lds0 + 65536 + lw * 4096;
#pragma unroll
    for (int i = 0; i < 4; ++i) {
      va[i] = (32 * lw + 8 * i + (l >> 3)) * 1280 + (((l & 7) ^ ((4 * i + (l >> 4)) & 7)) << 4) - i * 1024;
      vb[i] = (16 * lw + 4 * i + (l >> 4)) * 3840 + (((l & 15) ^ (((l >> 4) & 3) << 2)) << 4) - i * 1024;
    }
    const rsrc_t rx = __builtin_amdgcn_make_buffer_rsrc((void*)(p.x + c.panel * 163840), 0, 163840, 0x00020000);
    const rsrc_t rw = __builtin_amdgcn_make_buffer_rsrc((void*)(p.wqkv + c.ct * 256), 0, 640 * 3840 - c.ct * 256, 0x00020000);
    if (c.ct < 15) asm volatile("s_sleep " STR(SDELAY) "\n\t" P1_STAGE "s_waitcnt vmcnt(8)\n\ts_barrier\n\t" P1_STAGE : P1_STATE : P1_CONST : "memory", "scc");
    else { read_tag(c); __builtin_amdgcn_s_sleep(CPD); spare_poison(c); wait_bar<0>(); }
    read_tag(c);
    STAMP(2);
    {
      const char* pc; unsigned len;
      asm volatile("s_getpc_b64 %0\n.Lblk_pc:\n\ts_mov_b32 %1, .Lblk_end1-.Lblk_pc\n\ts_max_i32 %1, %1, .Lblk_end2-.Lblk_pc" : "=s"(pc), "=s"(len) :: "scc");
      const unsigned off = (((b >> 3) * 4 + lw) * 5 + umin(l, 4)) * 128;
      touch(pc, off < len ? off : 0u, lds0 + L_TOUCH);
    }
    if (c.ct < 15) {
      unsigned n = 0;
      asm volatile("s_waitcnt vmcnt(9)\n\ts_barrier\n\t" P1_STAGE
                   "buffer_store_dwordx4 %[ps], %[p0], %[rs], 0 offen sc1\n\tbuffer_store_dwordx4 %[ps], %[p0], %[rs], 0 offen offset:1024 sc1\n\t"
                   "s_waitcnt vmcnt(10)\n\ts_barrier\n\t" P1_STAGE "s_waitcnt vmcnt(10)\n"
                   "1:\n\ts_barrier\n\t" P1_STAGE
                   "s_add_u32 %[n], %[n], 1\n\ts_cmp_lt_u32 %[n], 6\n\ts_waitcnt vmcnt(8)\n\ts_cbranch_scc1 1b"
                   : P1_STATE, [n] "+s"(n)
                   : P1_CONST, [p0] "v"(WS_QK + (640 + 8 * b + 2 * lw) * 1024 + lin), [rs] "s"(c.rs), [ps] "v"(u32x4{POISON, POISON, POISON, POISON}) : "memory", "scc");
      STAMP(5);
      glds_d(p.ws + WS_CS1 + (2 * lw * 1920 + c.ct * 128) * 8, lin, lds0 + L1_CG + lw * 2048);
      glds_d(p.ws + WS_CS1 + ((2 * lw + 1) * 1920 + c.ct * 128) * 8, lin, lds0 + L1_CG + lw * 2048 + 1024);
      glds_m(p.ws + WS_RS1 + (c.panel * 128 + 32 * lw) * 16, lin, lds0 + L1_RG + lw * 512, 0xFFFFFFFFull);
    }
    if (lw == 0 && l < 4) *(LAS unsigned*)(c.lds + L_CNT0 + l * 4) = 0u;
    wait_bar<11>();
    if (c.ct == 15) { publish<0>(c, L_CNT0 + 12, 8, WS_F0 + c.b * 8); __builtin_amdgcn_s_sleep(WOT); }
    {
      const unsigned pv = (l & 1) ? 0u : ONES2;
      if (l < 32) *(LAS u32x4*)(c.lds + VRING + (16 * lw + (l >> 1)) * 352 + 320 + (l & 1) * 16) = u32x4{pv, pv, pv, pv};
    }
    if (lw == 3) poll256(p.ws, WS_F0 + l * 8, c.tag);
    if (c.ct < 15) wait_bar<0>();
    STAMP(6);
    if (c.ct < 15) {
      LAS float* ra = (LAS float*)(c.lds + L1_FIN);
      const unsigned tag = (unsigned)c.tag;
      if (lw < 2) {
        u32x4 g = *(LAS u32x4*)(c.lds + L1_RG + t4 * 16);
#pragma unroll 1
        for (unsigned n = 0; (g[1] != tag || g[3] != tag) && n < SPIN_CAP; ++n) { FENCE(); g = ld128_d(c.rs, WS_RS1 + (c.panel * 128 + t4) * 16); }
        const float sc = c.ct < 5 ? QSCALE : 1.f;
        ra[t4] = __uint_as_float(g[0]) * sc; ra[128 + t4] = __uint_as_float(g[2]) * sc;
      } else {
        const int n0 = t4 - 128; float sum = 0.f; bool ok = true;
#pragma unroll
        for (int i = 0; i < 8; ++i) { const u32x2 g = *(LAS u32x2*)(c.lds + L1_CG + i * 1024 + n0 * 8); sum += __uint_as_float(g[0]); ok = ok && g[1] == tag; }
        if (__any(!ok)) {
          sum = 0.f;
#pragma unroll 1
          for (int i = 0; i < 8; ++i) {
            u32x2 g = {0u, ~tag};
#pragma unroll 1
            for (unsigned n = 0; g[1] != tag && n < SPIN_CAP; ++n) { FENCE(); g = ld64_d(c.rs, WS_CS1 + (i * 1920 + c.ct * 128 + n0) * 8); }
            sum += __uint_as_float(g[0]);
          }
        }
        ra[256 + n0] = sum;
      }
    }
    STAMP(7);
    if (c.ct < 15) LDS_BAR();
  }
  Park pk;
  const rsrc_t rw2 = __builtin_amdgcn_make_buffer_rsrc((void*)(p.w2 + cs * 160), 0, FF * D * 2 - cs * 160, 0x00020000);
  unsigned vo80[5];
  {
    const int h = c.h, e = h & 1, cb = 5 * (h >> 1) + 2 * e;
    unsigned vk[3], vv[3]; u64 mv[3], mt = 0;
#pragma unroll
    for (int i = 0; i < 3; ++i) {
      const int n = 64 * (3 * lw + i) + l, kg = n / 22, dd = n - 22 * kg;
      vk[i] = WS_QK + ((80 + cb + i) * 2048 + 64 + 64 * lw - 256) * 16 + lin - i * 1024;
      vv[i] = WS_V + (h * 256 + 4 - 32 + kg) * 320 + dd * 16 - i * 1024;
      mv[i] = __builtin_amdgcn_ballot_w64(dd < 20 && kg < 32);
      if (i == 0) mt = __builtin_amdgcn_ballot_w64(dd < 20 && kg < 28);
    }
    auto issue = [&](int s, bool withk, u64 m0, u64 m1, u64 m2) {
      unsigned t;
      if (withk) asm volatile("s_mov_b32 m0, %[d]\n\ts_lshl_b32 %[t], %[s], 12\n\t"
                              "buffer_load_dwordx4 %[v0], %[rs], %[t] offen sc1 lds\n\tbuffer_load_dwordx4 %[v1], %[rs], %[t] offen offset:1024 sc1 lds\n\tbuffer_load_dwordx4 %[v2], %[rs], %[t] offen offset:2048 sc1 lds"
                              : [t] "=&s"(t) : [d] "s"(lds0 + KRING + (s & 1) * 12288 + lw * 3072), [s] "s"(s + 1), [rs] "s"(c.rs), [v0] "v"(vk[0]), [v1] "v"(vk[1]), [v2] "v"(vk[2]) : "memory", "scc");
      asm volatile("s_mov_b32 m0, %[d]\n\ts_mul_i32 %[t], %[s], 10240\n\t"
                   "s_mov_b64 exec, %[m0]\n\tbuffer_load_dwordx4 %[v0], %[rs], %[t] offen sc1 lds\n\t"
                   "s_mov_b64 exec, %[m1]\n\ts_cbranch_execz 1f\n\tbuffer_load_dwordx4 %[v1], %[rs], %[t] offen offset:1024 sc1 lds\n\t"
                   "s_mov_b64 exec, %[m2]\n\ts_cbranch_execz 1f\n\tbuffer_load_dwordx4 %[v2], %[rs], %[t] offen offset:2048 sc1 lds\n1:\n\ts_mov_b64 exec, -1"
                   : [t] "=&s"(t) : [d] "s"(lds0 + VRING + (s & 1) * 11264 + lw * 3072), [s] "s"(s + 1), [rs] "s"(c.rs), [v0] "v"(vv[0]), [v1] "v"(vv[1]), [v2] "v"(vv[2]),
                     [m0] "s"(m0), [m1] "s"(m1), [m2] "s"(m2) : "memory");
    };
    auto bad = [&](int s, u64 m0, u64 m1, u64 m2) {
      u64 any;
      asm volatile("ds_read_b128 v[76:79], %[k]\n\tds_read_b128 v[80:83], %[k] offset:1024\n\tds_read_b128 v[84:87], %[k] offset:2048\n\t"
                   "ds_read_b128 v[88:91], %[v]\n\tds_read_b128 v[92:95], %[v] offset:1024\n\tds_read_b128 v[96:99], %[v] offset:2048\n\t"
                   WL(4) "v_max3_u32 v76, v76, v77, v78\n\tv_max3_u32 v80, v79, v80, v81\n\tv_max3_u32 v76, v76, v82, v83\n\t"
                   WL(3) "v_max3_u32 v80, v80, v84, v85\n\tv_max3_u32 v76, v76, v86, v87\n\t"
                   WL(0) "s_mov_b64 exec, %[m0]\n\tv_max3_u32 v80, v80, v88, v89\n\tv_max3_u32 v76, v76, v90, v91\n\t"
                   "s_mov_b64 exec, %[m1]\n\tv_max3_u32 v80, v80, v92, v93\n\tv_max3_u32 v76, v76, v94, v95\n\t"
                   "s_mov_b64 exec, %[m2]\n\tv_max3_u32 v80, v80, v96, v97\n\tv_max3_u32 v76, v76, v98, v99\n\t"
                   "s_mov_b64 exec, -1\n\tv_max_u32 v76, v76, v80\n\tv_cmp_eq_u32 %[any], -1, v76\n\ts_nop 0"
                   : [any] "=s"(any) : [k] "v"(lds0 + KRING + (s & 1) * 12288 + lw * 3072 + lin), [v] "v"(lds0 + VRING + (s & 1) * 11264 + lw * 3072 + lin), [m0] "s"(m0), [m1] "s"(m1), [m2] "s"(m2)
                   : "memory", CL8(76, 77, 78, 79, 80, 81, 82, 83), CL8(84, 85, 86, 87, 88, 89, 90, 91), CL8(92, 93, 94, 95, 96, 97, 98, 99));
      return any != 0;
    };
#pragma unroll
    for (int i = 0; i < 5; ++i) { const int n = 64 * i + l, rr = n / 10; vo80[i] = swap23(32 * lw + rr) * 1280 + (n - 10 * rr) * 16 - (i & 3) * 1024; }
    const rsrc_t rwo = __builtin_amdgcn_make_buffer_rsrc((void*)(p.wo + cs * 160), 0, D * D * 2 - cs * 160, 0x00020000);
    const unsigned xw = (b >> 3) * 4 + lw, vt60 = (l < 60 ? l : 0) * 256;
    u32x4 psn = {POISON, POISON, POISON, POISON};
    asm volatile("" : "+v"(psn));
    const int b1 = b < 192 ? b : 0, rg1 = b1 / 24, cg1 = b1 - 24 * rg1;
    unsigned w1o = (t4 >> 3) * 3072 + (l & 7) * 16, cs2o = WS_CS2 + (rg1 * 1536 + cg1 * 64 + 16 * lw + l) * 4;
    const char* bw = (l & 32 ? p.w2 : p.w1) + ((b * 4 + lw) * 15 + umin(l & 31, 14)) * 128;
    asm volatile("" : "+v"(w1o), "+v"(cs2o), "+v"(bw), "+v"(vo80[0]), "+v"(vo80[1]), "+v"(vo80[2]), "+v"(vo80[3]), "+v"(vo80[4]));
    LDS_BAR();
    __builtin_amdgcn_s_sleep(KDELAY);
#pragma unroll 1
    for (unsigned n = 0; n < SPIN_CAP; ++n) {
      issue(0, true, mv[0], mv[1], mv[2]);
      DRAIN();
      if (!bad(0, mv[0], mv[1], mv[2])) break;
      __builtin_amdgcn_s_sleep(RDELAY);
    }
    STAMP(9);
    LDS_BAR();
    __builtin_amdgcn_sched_barrier(0);
    __builtin_amdgcn_s_setprio(3);
#pragma unroll 1
    for (int s = 1; s < 5; ++s) {
      if (s > 1) LDS_BAR();
      STAMPV(20 + s);
      issue(s, true, mv[0], mv[1], mv[2]);
#pragma unroll
      for (int k = 0; k < 3; ++k) {
        const int q = 12 * lw + 3 * (s - 1) + k;
        asm volatile("buffer_store_dwordx4 %0, %1, %2, %3 offen sc1" :: "v"(psn), "v"(lin), "s"(c.rs),
                     "s"(q < 10 ? WS_O + rt * 81920 + cs * 10240 + q * 1024 : q < 20 ? WS_H + rt * 81920 + cs * 10240 + (q - 10) * 1024 : q < 44 ? WS_F + rt * 196608 + cs * 24576 + (q - 20) * 1024 : WS_RS2 + (rt * 8 + cs) * 512) : "memory");
      }
      if (s == 1) {
        wait_vm<3>();
#pragma unroll 1
        for (unsigned n = 0; bad(1, mv[0], mv[1], mv[2]) && n < SPIN_CAP; ++n) { issue(1, true, mv[0], mv[1], mv[2]); DRAIN(); }
        if (lw == 0) {
          poll256(p.ws, WS_F1 + l * 8, c.tag);
          if (b == 0 && l == 0) stflag(p.ws, WS_EPOCH, c.tag);
        }
      } else {
#pragma unroll 1
        for (int k = 2 * s - 4; k < (s < 4 ? 2 * s - 2 : 5); ++k)
        { const int kr = k + (rt & 3) < 5 ? k + (rt & 3) : k + (rt & 3) - 5; W80_DMA(rwo, lds0 + L_WO + kr * 20480 + lw * 5120, kr * 163840); }
        if (s == TSTAGE) touch64(bw, lds0 + L_TOUCH);
        if (s == 4) wait_vm<8 + (TSTAGE == 4)>();
        if (s == TSTAGE) wait_vm<14>(); else wait_vm<13>();
        if (s == 3) {
          const char* src = p.w1 + (rg1 * 80 * 3072 + cg1 * 128);
          glds(src, w1o, lds0 + L_W1B + lw * 1024); glds(src + 32 * 3072, w1o, lds0 + L_W1B + 4096 + lw * 1024);
          if (lw < 2) glds(src + 64 * 3072, w1o, lds0 + L_W1B + 8192 + lw * 1024);
        }
      }
      STAMPV(9 + s);
    }
    u32x4 w1m[3] = {*(LAS u32x4*)(c.lds + L_W1B + t4 * 16), *(LAS u32x4*)(c.lds + L_W1B + 4096 + t4 * 16), {}};
    if (lw < 2) w1m[2] = *(LAS u32x4*)(c.lds + L_W1B + 8192 + t4 * 16);
    colsum_step1(c.lds + L_PART + 1024, l, lw, w1m);
    unsigned so, st;
#define PARK2(m, k0, c0, k1, c1) asm volatile(PKL(k0, c0) PKL(k1, c1) : PARK_REGS(m, pk), [so] "=&s"(so), [t] "=&s"(st) : W80_OPS(rw2), [r5] "s"(c.rot5 * 163840) : "memory", "scc")
    LDS_BAR(); STAMP(25);
    {
      const float cs2 = colsum_step2(c.lds + L_PART + 1024, l, lw);
      if (l < 16 && b < 192) st32_d(c.rs, cs2o, __float_as_uint(cs2));
    }
    issue(5, true, mv[0], mv[1], mv[2]);
    PARK2("=&", 0, 0, 1, 1);
    wait_bar<10>(); STAMP(26);
    publish<10>(c, L_CNTP, 4, WS_FP + b * 8);
    issue(6, true, mv[0], mv[1], mv[2]);
    PARK2("+", 2, 2, 3, 3);
    wait_bar<10>(); STAMP(27);
    issue(7, lw != 3, lw == 3 ? mt : mv[0], lw == 3 ? 0 : mv[1], mv[2]);
    PARK2("+", 4, 4, 5, 8);
    wait_bar<10>(); STAMP(28);
    if (lw == 0) poll256(p.ws, WS_FP + l * 8, c.tag);
    PARK2("+", 6, 9, 7, 10);
    if (lw == 1) {
      const int i = l & 7, k = l >> 3;
      touch64(k == 0 ? p.ws + (WS_O + c.qb * 327680 + i * 40960) : k == 1 ? p.ws + (WS_O + rt * 81920 + i * 10240) : k == 2 ? p.ws + (WS_H + rt * 81920 + i * 10240) : k == 3 ? p.ws + (WS_F + rt * 196608 + i * 24576)
              : k == 4 ? p.out + (rt * 81920 + i * 10240) : k == 5 ? p.x + (rt * 81920 + i * 10240) : p.ws + (WS_RS2 + rt * 4096 + i * 512), lds0 + L_TOUCH);
    }
    touch(p.w1 + xw * 15360, vt60, lds0 + L_TOUCH);
    touch(p.w1 + (xw * 15360 + 128), vt60, lds0 + L_TOUCH);
  }
  {
    unsigned vo[6];
#pragma unroll
    for (int i = 0; i < 3; ++i) {
      const int n = 64 * i + l, rr = n / 24, pos = n - 24 * rr;
      vo[i] = ((rr & 3) + 8 * (rr >> 2)) * 3072 + ((((pos >> 1) ^ ((rr >> 1) & 3)) << 1) | (pos & 1)) * 16 - i * 1024;
      vo[3 + i] = vo[i] + 4 * 3072;
    }
    const rsrc_t rw1 = __builtin_amdgcn_make_buffer_rsrc((void*)(p.w1 + cs * 384 + lw * 16 * 3072), 0, D * FF * 2 - cs * 384 - lw * 16 * 3072, 0x00020000);
    const unsigned dw = lds0 + lw * 6144, da = lds0 + L4_A + lw * 2048, va = WS_H + rt * 81920 + lw * 2048 + lin;
    const unsigned so = WS_RS2 + (rt * 8 + (l >> 4)) * 512 + (16 * lw + (l & 15)) * 8;
    unsigned t0, t1r; u32x2 s0, s1;
    LDS_BAR();
    STAMP(17);
    LDS_BAR();
    asm volatile(LW3(0, 0) LW3(0, 1) LW3(1, 0) LW3(1, 1) : [t0] "=&s"(t0) : W1_OPS : "memory");
    if (lw < 3) {
      float t = 0.f;
#pragma unroll
      for (int i = 0; i < 8; ++i) t += __uint_as_float(ld32_d(c.rs, WS_CS2 + (i * 1536 + cs * 192 + t4) * 4));
      *(LAS float*)(c.lds + L_CSUM + t4 * 4) = t;
    }
    asm volatile(PKL(8, 11) : PARK_REGS("+", pk), [so] "=&s"(t0), [t] "=&s"(t1r) : W80_OPS(rw2), [r5] "s"(c.rot5 * 163840) : "memory", "scc");
    LDS_BAR(); LDS_BAR(); LDS_BAR(); LDS_BAR();
    STAMP(18);
    LDS_BAR();
    asm volatile(
        LW3(2, 0) LW3(2, 1) LW3(3, 0) LW3(3, 1)
        "s_mov_b32 %[t1], 0\n"
        "1:\n\ts_sleep " STR(ADELAY) "\n\t" LA(0) LA(1) LA(2) LA(3)
        "s_waitcnt vmcnt(6)\n\t" CKR(0) CKT
        "s_cbranch_vccz 2f\n\ts_waitcnt vmcnt(0)\n\ts_add_u32 %[t1], %[t1], 1\n\ts_cmp_lt_u32 %[t1], 0x40000\n\ts_cbranch_scc1 1b\n"
        "2:\n\ts_barrier\n\t"
        IVL(1, 4, LW3(4, 0) LW3(4, 1), 63) "s_barrier\n\t"
        IVL(2, 8, LA(4) LW3(5, 0), 63) "s_barrier\n\t"
        IVL(3, 11, LA(5) LW3(5, 1), 63) "s_barrier\n\t"
        IVL(4, 8, LA(6) LW3(6, 0) LW3(6, 1), 63) "s_barrier\n\t"
        IVL(5, 11, LA(7) LW3(7, 0), 13) "s_barrier\n\t"
        IVL(6, 11, LA(8) LW3(7, 1) LW3(8, 0), 13) "s_barrier\n\t"
        IVL(7, 11, LA(9) LW3(8, 1), 8) "s_barrier\n\t"
        IVL(8, 11, "buffer_load_dwordx2 %[s0], %[so], %[rs], 0 offen sc1\n\tbuffer_load_dwordx2 %[s1], %[so], %[rs], 0 offen offset:2048 sc1\n\t" LW3(9, 0) LW3(9, 1), 8) "s_barrier\n\t"
        IVL(9, 11, "", 6)
        : [t0] "=&s"(t0), [t1] "=&s"(t1r), [s0] "=&v"(s0), [s1] "=&v"(s1)
        : W1_OPS, [rs] "s"(c.rs), [da] "s"(da), [va] "v"(va), [la] "v"(da + lin), [so] "v"(so) : "memory", "scc", "vcc", "v60", "v61", "v62", "v63", "v64", "v65", "v66", "v67");
    STAMP(29);
#pragma unroll 1
    for (unsigned n = 0; __any(umax(umax(s0[0], s0[1]), umax(s1[0], s1[1])) == POISON) && n < SPIN_CAP; ++n) { FENCE(); s0 = ld64_d(c.rs, so); s1 = ld64_d(c.rs, so + 2048); }
    float t1 = __uint_as_float(s0[0]) + __uint_as_float(s1[0]), t2 = __uint_as_float(s0[1]) + __uint_as_float(s1[1]);
    t1 = swap16_add(t1, t1); t2 = swap16_add(t2, t2); t1 = swap32_add(t1, t1); t2 = swap32_add(t2, t2);
    const float mean = t1 * (1.f / D), var = fmaxf(t2 * (1.f / D) - mean * mean, 0.f), a = __builtin_amdgcn_rsqf(var + LN_EPS);
    if (l < 32) *(LAS float*)(c.lds + L_RA + (l >> 4) * 256 + (16 * lw + (l & 15)) * 4) = l < 16 ? a : -mean * a;
    wait_bar<0>();
  }
  {
    const unsigned wa = lds0 + lw * 5120 + lin;
    {
      const int row = (5 + c.rot5) * 128 + ((b >> 6) * 4 + lw) * 24 + umin(l >> 1, 23);
      touch(p.w2 + cs * 160, (row < FF ? row : row - FF) * 1280 + (l & 1) * 156, lds0 + L_TOUCH);
    }
    LDS_BAR();
    STAMP(30);
    PUT(0, 0); PUT(1, 1); PUT(2, 2);
    LDS_BAR();
#pragma unroll 1
    for (int k = 5; k < 8; ++k) W80_DMA(rw2, lds0 + k * 20480 + lw * 5120, (k + c.rot5 < 12 ? k + c.rot5 : k + c.rot5 - 12) * 163840);
    PUT(3, 3); PUT(4, 4);
    STAMP(31);
    LDS_BAR();
    wait_bar<0>(); PUT(5, 0); PUT(6, 1); PUT(7, 2); PUT(8, 3);
    LDS_BAR(); LDS_BAR(); LDS_BAR();
  }
}

__global__ void __launch_bounds__(512, 2) block_kernel(Params p) {
  extern __shared__ __attribute__((aligned(16))) char smem[];
  Ctx c;
  c.st_lo = c.st_hi = 0;
  STAMP(3 + 20);
  c.p = p; c.lds = (lptr)smem; c.lds0 = (unsigned)(size_t)c.lds; c.l = threadIdx.x & 63; c.w = rfl(threadIdx.x >> 6); c.b = blockIdx.x;
  const int xcd = c.b & 7, j = c.b >> 3;
  const int t1 = 30 * (xcd & 1) + j;
  c.panel = 4 * (xcd >> 1) + (t1 & 3); c.ct = j < 30 ? t1 >> 2 : 15;
  c.h = c.rt = 4 * xcd + (j >> 3); c.qb = c.cs = j & 7;
  c.xr0 = 512 * (xcd >> 1) + 8 * (32 * (xcd & 1) + j); c.wi = j < 30 ? (xcd >> 1) * 30 + j : 0;
  c.rot5 = (3 * c.cs) >> 1; asm volatile("" : "+s"(c.rot5));
  if (__builtin_expect(c.w >= 4, 1)) loader_waves(c);
  else { STAMP(0); compute_waves(c); }
}

extern "C" void kernel_launch(void* const* d_in, const int* in_sizes, int n_in, void* d_out, int out_size, void* d_ws, size_t ws_size, hipStream_t stream) {
  Params p;
  p.x = (const char*)d_in[0]; p.wqkv = (const char*)d_in[1]; p.wo = (const char*)d_in[2]; p.w1 = (const char*)d_in[4]; p.w2 = (const char*)d_in[5];
  p.out = (char*)d_out; p.ws = (char*)d_ws;
  (void)hipFuncSetAttribute((const void*)block_kernel, hipFuncAttributeMaxDynamicSharedMemorySize, LDS_BYTES);
  block_kernel<<<256, 512, LDS_BYTES, stream>>>(p);
}
```

```cpp
#include <hip/hip_runtime.h>
#include <hip/hip_bf16.h>

#define DI __device__ __forceinline__
#define LAS __attribute__((address_space(3)))
#define GAS __attribute__((address_space(1)))
typedef unsigned long long u64;
typedef __attribute__((ext_vector_type(8))) short s16x8;
typedef __attribute__((ext_vector_type(4))) short s16x4;
typedef __attribute__((ext_vector_type(8))) __bf16 bf16x8;
typedef __attribute__((ext_vector_type(16))) __bf16 bf16x16;
typedef __attribute__((ext_vector_type(2))) __bf16 bf16x2;
typedef __attribute__((ext_vector_type(2))) float f32x2;
typedef __attribute__((ext_vector_type(4))) float f32x4;
typedef __attribute__((ext_vector_type(8))) float f32x8;
typedef __attribute__((ext_vector_type(16))) float f32x16;
typedef __attribute__((ext_vector_type(32))) float f32x32;
typedef __attribute__((ext_vector_type(4))) unsigned u32x4;
typedef __attribute__((ext_vector_type(2))) unsigned u32x2;
typedef LAS char* lptr;
typedef __amdgpu_buffer_rsrc_t rsrc_t;

#define V_BY_HEAD
#define SDELAY 10
#define CPD 30
#define WOT 60
#define QDELAY 20
#define KDELAY 36
#define PDELAY 2
#define ADELAY 6
#define RDELAY 6
#define PSLEEP 2
constexpr int S = 2048, D = 640, FF = 1536;
constexpr float LN_EPS = 1e-5f;
constexpr float QSCALE = 0.22360679774997896f * 1.4426950408889634f;

constexpr unsigned WS_EPOCH = 0, WS_F0 = 4096, WS_F1 = 6144, WS_FP = 8192;
constexpr unsigned WS_RS1 = 65536;
constexpr unsigned WS_CS1 = WS_RS1 + 2048 * 16;
constexpr unsigned WS_CS2 = WS_CS1 + 8 * 1920 * 8;
constexpr unsigned WS_QK = WS_CS2 + 8 * 1536 * 4;
constexpr unsigned WS_V = WS_QK + 160 * 2048 * 16;
constexpr unsigned WS_RS2 = WS_V + 256 * 640 * 16;
constexpr unsigned WS_O = WS_RS2 + 32 * 8 * 512;
constexpr unsigned WS_H = WS_O + S * D * 2;
constexpr unsigned WS_F = WS_H + S * D * 2;
constexpr unsigned WS_END = WS_F + S * FF * 2;
constexpr int LDS_BYTES = 163840;
constexpr unsigned SPIN_CAP = 1u << 18;
constexpr unsigned POISON = 0xFFFFFFFFu;
constexpr int L1_CG = 131072, L1_RG = L1_CG + 8192, L1_FIN = L1_RG + 2048;
constexpr int L1_XS = 131072, L1_WS = L1_XS + 10240;
constexpr int KRING = 0, VRING = 24576, L_W1B = 47104, L_WO = 57344;
constexpr int L4_A = 122880, L_EX = 98304;
constexpr int L_CNT0 = 159744, L_CNT1 = L_CNT0 + 4, L_CNTP = L_CNT0 + 8, L_CSUM = L_CNT0 + 256, L_RA = L_CSUM + 768, L_TOUCH = L_RA + 512, L_PART = L_TOUCH + 256;

#define TS(i) ""
#define TS_CLOB
#define TS_PUT(i, k)
#define STAMP(k)
#define STAMPV(k)
struct Params { const char* x; const char* wqkv; const char* wo; const char* w1; const char* w2; char* out; char* ws; };
struct Ctx { Params p; lptr lds; unsigned lds0; rsrc_t rs; u64 tag; int l, w, b, panel, ct, h, qb, rt, cs, rot5, xr0, wi; mutable unsigned st_lo, st_hi; };

DI unsigned rfl(unsigned v) { return __builtin_amdgcn_readfirstlane(v); }
DI unsigned cvtpk(float lo, float hi) { f32x2 v = {lo, hi}; return __builtin_bit_cast(unsigned, __builtin_convertvector(v, bf16x2)); }
typedef __attribute__((ext_vector_type(2))) short s16x2;
DI unsigned relu2(unsigned v) { return __builtin_bit_cast(unsigned, __builtin_elementwise_max(__builtin_bit_cast(s16x2, v), s16x2{0, 0})); }
DI float bflo(unsigned v) { return __uint_as_float(v << 16); }
DI float bfhi(unsigned v) { return __uint_as_float(v & 0xffff0000u); }
DI void glds(const char* sbase, unsigned voff, unsigned lds) {
  asm volatile("s_mov_b32 m0, %2\n\ts_nop 0\n\tglobal_load_lds_dwordx4 %0, %1" :: "v"(voff), "s"(sbase), "s"(lds) : "memory");
}
DI void glds_d(const char* sbase, unsigned voff, unsigned lds) {
  asm volatile("s_mov_b32 m0, %2\n\ts_nop 0\n\tglobal_load_lds_dwordx4 %0, %1 sc1" :: "v"(voff), "s"(sbase), "s"(lds) : "memory");
}
DI void glds_m(const char* sbase, unsigned voff, unsigned lds, u64 mask) {
  asm volatile("s_mov_b32 m0, %2\n\ts_mov_b64 exec, %3\n\tglobal_load_lds_dwordx4 %0, %1 sc1\n\ts_mov_b64 exec, -1" :: "v"(voff), "s"(sbase), "s"(lds), "s"(mask) : "memory");
}
DI void touch(const char* sbase, unsigned voff, unsigned lds) {
  asm volatile("s_mov_b32 m0, %2\n\ts_nop 0\n\tglobal_load_lds_dword %0, %1" :: "v"(voff), "s"(sbase), "s"(lds) : "memory");
}
DI void touch64(const char* a, unsigned lds) { asm volatile("s_mov_b32 m0, %1\n\ts_nop 0\n\tglobal_load_lds_dword %0, off" :: "v"(a), "s"(lds) : "memory"); }
template <int N> DI void wait_vm() { asm volatile("s_waitcnt vmcnt(%0)" :: "i"(N) : "memory"); }
template <int N> DI void wait_bar() { asm volatile("s_waitcnt vmcnt(%0) lgkmcnt(0)\n\ts_barrier" :: "i"(N) : "memory"); }
#define LDS_BAR() asm volatile("s_waitcnt lgkmcnt(0)\n\ts_barrier" ::: "memory")
#define DRAIN() asm volatile("s_waitcnt vmcnt(0)" ::: "memory")
#define STR_(x) #x
#define STR(x) STR_(x)
#define VR(b, n) "v[" STR(b) ":" STR(b) "+" STR(n) "-1]"
#define WL(n) "s_waitcnt lgkmcnt(" #n ")\n\t"
#define CL8(a, b, c, d, e, f, g, h) "v" #a, "v" #b, "v" #c, "v" #d, "v" #e, "v" #f, "v" #g, "v" #h
DI f32x16 mfma32(s16x8 a, s16x8 b, f32x16 c) { return __builtin_amdgcn_mfma_f32_32x32x16_bf16(__builtin_bit_cast(bf16x8, a), __builtin_bit_cast(bf16x8, b), c, 0, 0, 0); }
DI f32x4 mfma16(s16x8 a, s16x8 b, f32x4 c) { return __builtin_amdgcn_mfma_f32_16x16x32_bf16(__builtin_bit_cast(bf16x8, a), __builtin_bit_cast(bf16x8, b), c, 0, 0, 0); }
DI float dot2(unsigned a, unsigned b, float c) { return __builtin_amdgcn_fdot2_f32_bf16(__builtin_bit_cast(bf16x2, a), __builtin_bit_cast(bf16x2, b), c, false); }
constexpr unsigned ONES2 = 0x3f803f80u;
DI void st128_d(rsrc_t rs, unsigned off, u32x4 v) { __builtin_amdgcn_raw_buffer_store_b128(v, rs, off, 0, 16); }
DI void st64_d(rsrc_t rs, unsigned off, u32x2 v) { __builtin_amdgcn_raw_buffer_store_b64(v, rs, off, 0, 16); }
DI void st32_d(rsrc_t rs, unsigned off, unsigned v) { __builtin_amdgcn_raw_buffer_store_b32(v, rs, off, 0, 16); }
DI u32x4 ld128_d(rsrc_t rs, unsigned off) { return __builtin_amdgcn_raw_buffer_load_b128(rs, off, 0, 16); }
DI u32x2 ld64_d(rsrc_t rs, unsigned off) { return __builtin_amdgcn_raw_buffer_load_b64(rs, off, 0, 16); }
DI unsigned ld32_d(rsrc_t rs, unsigned off) { return __builtin_amdgcn_raw_buffer_load_b32(rs, off, 0, 16); }
DI u64 ldflag(const char* ws, unsigned off) { return __hip_atomic_load((GAS u64*)(ws + off), __ATOMIC_RELAXED, __HIP_MEMORY_SCOPE_AGENT); }
DI void stflag(char* ws, unsigned off, u64 v) { __hip_atomic_store((GAS u64*)(ws + off), v, __ATOMIC_RELAXED, __HIP_MEMORY_SCOPE_AGENT); }
DI void read_tag(Ctx& c) {
  const char* ws = c.p.ws; asm volatile("" : "+s"(ws));
  c.rs = __builtin_amdgcn_make_buffer_rsrc(c.p.ws, 0, WS_END, 0x00020000);
  c.tag = *(const __attribute__((address_space(4))) u64*)(ws + WS_EPOCH) + 1;
}
DI void poll256(const char* ws, unsigned off, u64 tag) {
#pragma unroll 1
  for (unsigned n = 0; n < SPIN_CAP; ++n) {
    const u64 a = ldflag(ws, off), b = ldflag(ws, off + 512), c = ldflag(ws, off + 1024), d = ldflag(ws, off + 1536);
    if (__all(a == tag && b == tag && c == tag && d == tag)) break;
    __builtin_amdgcn_s_sleep(4);
  }
}
template <int YOUNGER> DI void publish(const Ctx& c, int cnt_off, unsigned n, unsigned flag_off) {
  wait_vm<YOUNGER>();
  if (c.l == 0) {
    const unsigned old = __hip_atomic_fetch_add((LAS unsigned*)(c.lds + cnt_off), 1u, __ATOMIC_RELAXED, __HIP_MEMORY_SCOPE_WORKGROUP);
    if (old == n - 1) stflag(c.p.ws, flag_off, c.tag);
  }
}
template <int CTRL> DI float dpp_add(float v) { return v + __int_as_float(__builtin_amdgcn_update_dpp(0, __float_as_int(v), CTRL, 0xf, 0xf, true)); }
DI float swap16_add(float a, float b) { const auto t = __builtin_amdgcn_permlane16_swap(__float_as_uint(a), __float_as_uint(b), false, false); return __uint_as_float(t[0]) + __uint_as_float(t[1]); }
DI float swap32_add(float a, float b) { const auto t = __builtin_amdgcn_permlane32_swap(__float_as_uint(a), __float_as_uint(b), false, false); return __uint_as_float(t[0]) + __uint_as_float(t[1]); }
DI float swap32_max(float a) { const auto t = __builtin_amdgcn_permlane32_swap(__float_as_uint(a), __float_as_uint(a), false, false); return fmaxf(__uint_as_float(t[0]), __uint_as_float(t[1])); }
DI float upper_half(float a) { const auto t = __builtin_amdgcn_permlane32_swap(__float_as_uint(a), __float_as_uint(a), false, false); return __uint_as_float(t[1]); }
DI u32x4 swap32_pair(unsigned a0, unsigned a1, unsigned b0, unsigned b1) {
  const auto s0 = __builtin_amdgcn_permlane32_swap(a0, b0, false, false), s1 = __builtin_amdgcn_permlane32_swap(a1, b1, false, false);
  return u32x4{s0[0], s1[0], s0[1], s1[1]};
}
DI u32x4 swap16_pair(unsigned a0, unsigned a1, unsigned b0, unsigned b1) {
  const auto s0 = __builtin_amdgcn_permlane16_swap(a0, b0, false, false), s1 = __builtin_amdgcn_permlane16_swap(a1, b1, false, false);
  return u32x4{s0[0], s1[0], s0[1], s1[1]};
}
DI void colsum_step1(lptr part, int l, int w4, const u32x4 (&m)[3]) {
  float v[8], u[4], t[2];
#pragma unroll
  for (int i = 0; i < 4; ++i) { v[2 * i] = bflo(m[0][i]) + bflo(m[1][i]) + bflo(m[2][i]); v[2 * i + 1] = bfhi(m[0][i]) + bfhi(m[1][i]) + bfhi(m[2][i]); }
#pragma unroll
  for (int i = 0; i < 4; ++i) u[i] = swap32_add(v[i], v[i + 4]);
#pragma unroll
  for (int i = 0; i < 2; ++i) t[i] = dpp_add<0x128>(swap16_add(u[i], u[i + 2]));
  *(LAS f32x2*)(part + (w4 * 64 + (l & 7) * 8 + 4 * (l >> 5) + 2 * ((l >> 4) & 1)) * 4) = f32x2{t[0], t[1]};
}
DI float colsum_step2(lptr part, int l, int w4) {
  float v = *(LAS float*)(part + ((l >> 4) * 64 + 16 * w4 + (l & 15)) * 4);
  v = swap16_add(v, v); return swap32_add(v, v);
}
DI unsigned np(unsigned v) { return v < POISON - 1 ? v : POISON - 1; }
DI u32x4 np(u32x4 v) { return u32x4{np(v[0]), np(v[1]), np(v[2]), np(v[3])}; }
DI unsigned umax(unsigned a, unsigned b) { return a > b ? a : b; }
DI unsigned umin(unsigned a, unsigned b) { return a < b ? a : b; }
DI unsigned umax4(u32x4 v) { return umax(umax(v[0], v[1]), umax(v[2], v[3])); }
#define FENCE() asm volatile("" ::: "memory")
DI f32x16 lo16(const f32x32& s) { return __builtin_shufflevector(s, s, 0, 1, 2, 3, 4, 5, 6, 7, 8, 9, 10, 11, 12, 13, 14, 15); }
DI f32x16 hi16(const f32x32& s) { return __builtin_shufflevector(s, s, 16, 17, 18, 19, 20, 21, 22, 23, 24, 25, 26, 27, 28, 29, 30, 31); }
DI int swap23(int k) { return (k & ~12) | ((k & 4) << 1) | ((k & 8) >> 1); }

DI void row_stats(const Ctx& c, const u32x4 (&xv)[3], int xrow) {
  const int r = c.l & 31;
  float s1 = 0.f, s2 = 0.f;
#pragma unroll
  for (int i = 0; i < 3; ++i)
#pragma unroll
    for (int k = 0; k < 4; ++k) { const unsigned v = (i < 2 || r < 16) ? xv[i][k] : 0u; s1 = dot2(v, ONES2, s1); s2 = dot2(v, v, s2); }
  s1 = dpp_add<0x128>(dpp_add<0x141>(dpp_add<0x4E>(dpp_add<0xB1>(s1)))); s2 = dpp_add<0x128>(dpp_add<0x141>(dpp_add<0x4E>(dpp_add<0xB1>(s2))));
  s1 = swap16_add(s1, s1); s2 = swap16_add(s2, s2);
  const float mean = s1 * (1.f / D), var = fmaxf(s2 * (1.f / D) - mean * mean, 0.f), a = __builtin_amdgcn_rsqf(var + LN_EPS);
  const unsigned tag = (unsigned)c.tag;
  if (r == 0) st128_d(c.rs, WS_RS1 + xrow * 16, u32x4{__float_as_uint(a), tag, __float_as_uint(-mean * a), tag});
}
DI void spare_poison(const Ctx& c) {
  u32x4 psn = {POISON, POISON, POISON, POISON};
  asm volatile("" : "+v"(psn));
  const unsigned q0 = ((c.b - 240) * 8 + c.w) * 17;
#pragma unroll 1
  for (unsigned q = q0; q < umin(q0 + 17, 2064); ++q) {
    const unsigned v = q - 720;
    asm volatile("buffer_store_dwordx4 %0, %1, %2, %3 offen sc1" :: "v"(psn), "v"(c.l * 16), "s"(c.rs),
                 "s"(q < 720 ? WS_QK + (80 + q / 9) * 32768 + q % 9 * 1024 : q < 1424 ? WS_V + v / 22 * 81920 + v % 22 * 1024 : WS_QK + (q - 1424) * 1024) : "memory");
  }
}
#define Q_X 96
#define Q_Y 128
#define QMK(S, m, n) "v_mfma_f32_16x16x32_bf16 " VR(32 + m * 16 + n * 4, 4) ", " VR(S + 16 + n * 4, 4) ", " VR(S + m * 4, 4) ", " VR(32 + m * 16 + n * 4, 4) "\n\t"
#define QMV(S, m, n) "v_mfma_f32_16x16x32_bf16 " VR(32 + m * 16 + n * 4, 4) ", " VR(S + m * 4, 4) ", " VR(S + 16 + n * 4, 4) ", " VR(32 + m * 16 + n * 4, 4) "\n\t"
#define QRA(S, m, ks) "ds_read_b128 " VR(S + m * 4, 4) ", v[160+" STR(ks) "] offset:" STR(m * 2048) "\n\t"
#define QRB(S, n, h, ks) "ds_read_b64_tr_b16 " VR(S + 16 + n * 4 + h * 2, 2) ", v[162+" STR(n) "] offset:" STR(ks * 8192 + h * 1024) "\n\t"
#define QAD(i) "v_add_u32 v[160+" #i "], %[dl], v[160+" #i "]\n\t"
#define P1_LOOP(MM) \
  QRA(Q_Y, 0, 0) QRB(Q_Y, 0, 0, 0) QRB(Q_Y, 0, 1, 0) QRB(Q_Y, 1, 0, 0) QRB(Q_Y, 1, 1, 0) QRA(Q_Y, 1, 0) \
  QRB(Q_Y, 2, 0, 0) QRB(Q_Y, 2, 1, 0) QRB(Q_Y, 3, 0, 0) QRB(Q_Y, 3, 1, 0) QRA(Q_Y, 2, 0) QRA(Q_Y, 3, 0) \
  "s_branch 2f\n" \
  "1:\n\t" \
  MM(Q_X, 1, 0) QRA(Q_Y, 0, 0) MM(Q_X, 1, 1) QRB(Q_Y, 0, 0, 0) MM(Q_X, 0, 2) QRB(Q_Y, 0, 1, 0) MM(Q_X, 1, 2) QRB(Q_Y, 1, 0, 0) \
  MM(Q_X, 0, 3) QRB(Q_Y, 1, 1, 0) MM(Q_X, 1, 3) QRA(Q_Y, 1, 0) MM(Q_X, 2, 0) QRB(Q_Y, 2, 0, 0) MM(Q_X, 2, 1) QRB(Q_Y, 2, 1, 0) \
  MM(Q_X, 2, 2) QRB(Q_Y, 3, 0, 0) MM(Q_X, 2, 3) QRB(Q_Y, 3, 1, 0) MM(Q_X, 3, 0) QRA(Q_Y, 2, 0) MM(Q_X, 3, 1) QRA(Q_Y, 3, 0) MM(Q_X, 3, 2) MM(Q_X, 3, 3) \
  "2:\n\t" \
  WL(9) MM(Q_Y, 0, 0) QRA(Q_X, 0, 1) WL(8) MM(Q_Y, 0, 1) QRB(Q_X, 0, 0, 1) WL(8) MM(Q_Y, 1, 0) QRB(Q_X, 0, 1, 1) MM(Q_Y, 1, 1) QRB(Q_X, 1, 0, 1) \
  WL(8) MM(Q_Y, 0, 2) QRB(Q_X, 1, 1, 1) MM(Q_Y, 1, 2) QRA(Q_X, 1, 1) WL(8) MM(Q_Y, 0, 3) QRB(Q_X, 2, 0, 1) MM(Q_Y, 1, 3) QRB(Q_X, 2, 1, 1) \
  WL(9) MM(Q_Y, 2, 0) QRB(Q_X, 3, 0, 1) MM(Q_Y, 2, 1) QRB(Q_X, 3, 1, 1) MM(Q_Y, 2, 2) QRA(Q_X, 2, 1) MM(Q_Y, 2, 3) QRA(Q_X, 3, 1) \
  WL(12) MM(Q_Y, 3, 0) "s_add_u32 %[st], %[st], 1\n\ts_and_b32 %[dl], %[st], 3\n\ts_cselect_b32 %[dl], 32768, %[wr]\n\t" \
  MM(Q_Y, 3, 1) QAD(0) QAD(1) MM(Q_Y, 3, 2) QAD(2) QAD(3) MM(Q_Y, 3, 3) QAD(4) QAD(5) \
  WL(7) MM(Q_X, 0, 0) "s_cmp_eq_u32 %[st], 12\n\t" WL(0) MM(Q_X, 0, 1) "s_cbranch_scc1 3f\n\t" \
  "s_barrier\n\ts_cmp_lg_u32 %[st], %[fk]\n\ts_cbranch_scc1 1b\n\t" \
  "s_mov_b64 exec, 1\n\tbuffer_store_dwordx2 v[168:169], v166, %[rs], 0 offen sc1\n\ts_mov_b64 exec, -1\n\ts_branch 1b\n" \
  "3:\n\t" \
  MM(Q_X, 1, 0) MM(Q_X, 1, 1) MM(Q_X, 0, 2) MM(Q_X, 1, 2) MM(Q_X, 0, 3) MM(Q_X, 1, 3) MM(Q_X, 2, 0) MM(Q_X, 2, 1) \
  MM(Q_X, 2, 2) MM(Q_X, 2, 3) MM(Q_X, 3, 0) MM(Q_X, 3, 1) MM(Q_X, 3, 2) MM(Q_X, 3, 3) \
  "s_nop 7"
#define P1_OPERANDS \
  : "+{v[32:63]}"(ac0), "+{v[64:95]}"(ac1), "+{v160}"(aoff[0]), "+{v161}"(aoff[1]), "+{v162}"(boff[0]), "+{v163}"(boff[1]), "+{v164}"(boff[2]), "+{v165}"(boff[3]), \
    [st] "+s"(st), [dl] "=&s"(dl) \
  : [wr] "s"(-98304), [fk] "s"(w == 0 ? 5 : 0), [rs] "s"(c.rs), "{v166}"(WS_F0 + c.b * 8), "{v[168:169]}"(c.tag) \
  : "memory", "scc", CL8(96, 97, 98, 99, 100, 101, 102, 103), CL8(104, 105, 106, 107, 108, 109, 110, 111), CL8(112, 113, 114, 115, 116, 117, 118, 119), CL8(120, 121, 122, 123, 124, 125, 126, 127), \
    CL8(128, 129, 130, 131, 132, 133, 134, 135), CL8(136, 137, 138, 139, 140, 141, 142, 143), CL8(144, 145, 146, 147, 148, 149, 150, 151), CL8(152, 153, 154, 155, 156, 157, 158, 159)
template <bool VT>
DI void p1_compute(const Ctx& c) {
  const int l = c.l, w = c.w, wm = w >> 1, wn = w & 1, t = l & 15, g = l >> 4, q = t >> 2, pp = l & 3;
  unsigned aoff[2], boff[4];
#pragma unroll
  for (int i = 0; i < 2; ++i) aoff[i] = c.lds0 + 65536 + (wm * 64 + t) * 128 + (((4 * i + g) ^ (t >> 1)) << 4);
#pragma unroll
  for (int n = 0; n < 4; ++n) boff[n] = c.lds0 + 65536 + 16384 + (8 * g + q) * 256 + (((wn * 8 + 2 * n + (pp >> 1)) ^ ((q << 2) | ((g & 1) << 1))) << 4) + 8 * (pp & 1);
  f32x32 ac0 = {}, ac1 = {};
  unsigned st = 2, dl;
  if (VT) asm volatile(P1_LOOP(QMV) P1_OPERANDS); else asm volatile(P1_LOOP(QMK) P1_OPERANDS);
  LAS float* ra = (LAS float*)(c.lds + L1_FIN); LAS float* rb = ra + 128; LAS float* cs = ra + 256;
  unsigned base[2];
#pragma unroll
  for (int i = 0; i < 2; ++i) {
    const int f = (c.ct - 10) * 128 + wn * 64 + (2 * i + (g & 1)) * 16 + t, h = f / 20, d = f - 20 * h, dd = (h & 1) ? (d < 4 ? d + 16 : d - 4) : d;
    base[i] = VT ? WS_V + (h * 256 + c.panel * 16 + wm * 8 + (g >> 1)) * 320 + dd * 16 : WS_QK + ((c.ct * 16 + wn * 8 + (g >> 1)) * 2048 + c.panel * 128 + wm * 64 + (2 * i + (g & 1)) * 16 + t) * 16;
  }
  asm volatile("" : "+v"(base[0]), "+v"(base[1]));
  LDS_BAR();
  STAMP(3);
  float s0[4], s1[4]; f32x4 u4[4], v4[4];
#pragma unroll
  for (int i = 0; i < 4; ++i) {
    s0[i] = VT ? cs[wn * 64 + 16 * i + t] : ra[wm * 64 + 16 * i + t]; s1[i] = VT ? 0.f : rb[wm * 64 + 16 * i + t];
    u4[i] = *(LAS f32x4*)(VT ? ra + wm * 64 + 16 * i + 4 * g : cs + wn * 64 + 16 * i + 4 * g);
    v4[i] = VT ? *(LAS f32x4*)(rb + wm * 64 + 16 * i + 4 * g) : f32x4{};
  }
  __builtin_amdgcn_sched_barrier(0);
#pragma unroll
  for (int o = 0; o < 4; ++o)
#pragma unroll
    for (int ip = 0; ip < 2; ++ip) {
      unsigned pk[2][2];
#pragma unroll
      for (int j = 0; j < 2; ++j) {
        const int i2 = 2 * ip + j, m = VT ? o : i2, n = VT ? i2 : o; float y[4];
#pragma unroll
        for (int i = 0; i < 4; ++i) {
          const float a = m < 2 ? ac0[m * 16 + n * 4 + i] : ac1[(m - 2) * 16 + n * 4 + i];
          y[i] = VT ? fmaf(a, u4[o][i], v4[o][i] * s0[i2]) : fmaf(a, s0[i2], s1[i2] * u4[o][i]);
        }
        pk[j][0] = cvtpk(y[0], y[1]); pk[j][1] = cvtpk(y[2], y[3]);
      }
      st128_d(c.rs, base[ip] + o * (VT ? 2 * 320 : 2 * 32768), np(swap16_pair(pk[0][0], pk[0][1], pk[1][0], pk[1][1])));
    }
}

#define EX(b, i) "v_exp_f32 v[" STR(b) "+" STR(i) "], v[" STR(b) "+" STR(i) "]\n\t"
#define EX3(b, i) EX(b, i) EX(b, i + 1) EX(b, i + 2)
#define EX4(b, i) EX3(b, i) EX(b, i + 3)
#define R_SA 32
#define R_SB 64
#define R_OA 96
#define R_OB 112
#define R_NA 128
#define R_NB 144
#define R_QA 160
#define R_QB 164
#define R_QR 168
#define R_KR 170
#define R_K 172
#define R_VA 176
#define R_VB 184
#define R_IDX 192
#define R_KAD 193
#define R_KRAD 194
#define R_VAD 195
#define PV(o, vreg, sreg) "v_smfmac_f32_32x32x32_bf16 " VR(o, 16) ", " VR(vreg, 4) ", " VR(sreg, 8) ", v" STR(R_IDX) "\n\t"
#define QK16(dst, q, nm) "v_mfma_f32_32x32x16_bf16 " VR(dst, 16) ", " VR(R_K, 4) ", " VR(q, 4) ", " VR(nm, 16) "\n\t"
#define STEP(C, N, VC, VN, AB, i, MID, KRLOAD) \
  QK16(N, R_QA, R_NA) \
  MID \
  EX4(C, 0) \
  PV(R_OB, VN + 4, N + 24) \
  "ds_read_b128 " VR(VN, 4) ", v" STR(R_VAD) " offset:" STR(i * 1408) "\n\t" \
  "ds_read_b128 " VR(VN + 4, 4) ", v" STR(R_VAD) " offset:" STR(i * 1408 + 704) "\n\t" \
  EX3(C, 4) \
  QK16(N + 16, R_QB, R_NB) \
  "ds_read_b128 " VR(R_K, 4) ", v" STR(R_KAD) " offset:" STR((i / 2) * 3072 + (i & 1) * 512) "\n\t" \
  EX4(C, 7) \
  PV(R_OA, VC, C) \
  "s_waitcnt lgkmcnt(3)\n\t" \
  EX4(C, 11) \
  "v_mfma_f32_32x32x4_2b_bf16 " VR(N, 32) ", " VR(R_KR, 2) ", " VR(R_QR, 2) ", " VR(N, 32) " cbsz:1 abid:" STR(AB) "\n\t" \
  EX4(C, 15) EX4(C, 19) \
  PV(R_OA, VC + 4, C + 8) \
  "s_waitcnt lgkmcnt(0)\n\t" \
  KRLOAD \
  EX4(C, 23) \
  PV(R_OB, VC, C + 16) \
  EX4(C, 27) EX(C, 31)
#define KRL(i) "ds_read_b64 " VR(R_KR, 2) ", v" STR(R_KRAD) " offset:" STR((i / 2) * 3072) "\n\t"
#define STAGE_TOP \
  "s_barrier\n\t" \
  "v_add_u32 v" STR(R_KAD) ", %[dk], v" STR(R_KAD) "\n\tv_add_u32 v" STR(R_KRAD) ", %[dk], v" STR(R_KRAD) "\n\tv_add_u32 v" STR(R_VAD) ", %[dv], v" STR(R_VAD) "\n\t" \
  "s_sub_u32 %[dk], 0, %[dk]\n\ts_sub_u32 %[dv], 0, %[dv]\n\t"

DI f32x32 cat16(const f32x16& a, const f32x16& b) {
  return __builtin_shufflevector(a, b, 0, 1, 2, 3, 4, 5, 6, 7, 8, 9, 10, 11, 12, 13, 14, 15, 16, 17, 18, 19, 20, 21, 22, 23, 24, 25, 26, 27, 28, 29, 30, 31);
}
DI float max16(const f32x16& s) { float m = s[0];
#pragma unroll
  for (int i = 1; i < 16; ++i) m = fmaxf(m, s[i]);
  return m; }
DI f32x16 splat16(float v) { f32x16 s;
#pragma unroll
  for (int i = 0; i < 16; ++i) s[i] = v;
  return s; }
DI bool out_of_range(const f32x16& o) {
  float chk = 0.f;
#pragma unroll
  for (int i = 0; i < 12; ++i) chk += fabsf(o[i]);
  return !(chk < 1e37f) || !(upper_half(o[8]) > 0.f);
}

DI void p2_compute(const Ctx& c, u32x2 (&res)[5]) {
  const int l = c.l, w = c.w, r = l & 31, hh = l >> 5, h = c.h;
  const int e = h & 1, cb = 5 * (h >> 1) + 2 * e, q0 = c.qb * 256 + w * 64;
  unsigned kad = c.lds0 + KRING + 12288 + (e + hh) * 1024 + r * 16, krad = c.lds0 + KRING + 12288 + (e ? 0 : 2048) + l * 16 + 8 * e;
  unsigned vad = c.lds0 + VRING + 11264 + hh * 352 + (r < 21 ? r : 21) * 16;
  LDS_BAR();
  __builtin_amdgcn_s_sleep(QDELAY);
  const unsigned ko = WS_QK + ((80 + cb + e + hh) * 2048 + r) * 16, kro = WS_QK + ((80 + cb + (e ? 0 : 2)) * 2048 + r) * 16 + 8 * e;
  const unsigned vg = WS_V + (h * 256 + hh) * 320 + (r < 20 ? r : 0) * 16, pad = r == 20 ? ONES2 : 0u;
  s16x8 qa, qb, kf; s16x4 qr, kr; u32x4 k0, t0, t1;
#pragma unroll 1
  for (unsigned n = 0; n < SPIN_CAP; ++n) {
    FENCE();
    const u32x4 ta = ld128_d(c.rs, WS_QK + ((cb + e + hh) * 2048 + q0 + r) * 16), tb = ld128_d(c.rs, WS_QK + ((cb + e + hh) * 2048 + q0 + 32 + r) * 16);
    const u32x2 tr = ld64_d(c.rs, WS_QK + ((e ? cb : cb + 2) * 2048 + q0 + l) * 16 + 8 * e), tk = ld64_d(c.rs, kro + hh * 512);
    k0 = ld128_d(c.rs, ko); const u32x4 k1 = ld128_d(c.rs, ko + 512);
    t0 = ld128_d(c.rs, vg); t1 = ld128_d(c.rs, vg + 640);
    if (n == 0) publish<8>(c, L_CNT1, 4, WS_F1 + c.b * 8);
    qa = __builtin_bit_cast(s16x8, ta); qb = __builtin_bit_cast(s16x8, tb); qr = __builtin_bit_cast(s16x4, tr);
    kf = __builtin_bit_cast(s16x8, k1); kr = __builtin_bit_cast(s16x4, tk);
    if (!__any(umax(umax(umax(umax4(ta), umax4(tb)), umax(umax4(k0), umax4(k1))), umax(umax(umax4(t0), umax4(t1)), umax(umax(tr[0], tr[1]), umax(tk[0], tk[1])))) == POISON)) break;
    __builtin_amdgcn_s_sleep(RDELAY);
  }
  STAMP(5);
  f32x32 sa = {}, sb = {};
  sa = __builtin_amdgcn_mfma_f32_32x32x4bf16_1k(kr, qr, sa, 1, 0, 0);
  f32x16 s0 = mfma32(__builtin_bit_cast(s16x8, k0), qa, lo16(sa)), s1 = mfma32(__builtin_bit_cast(s16x8, k0), qb, hi16(sa));
  f32x8 va = {}, vb = {};
#pragma unroll
  for (int i = 0; i < 4; ++i) { va[i] = __uint_as_float(r < 20 ? t0[i] : pad); va[4 + i] = __uint_as_float(r < 20 ? t1[i] : pad); }
  f32x16 na = splat16(-swap32_max(max16(s0))), nb = splat16(-swap32_max(max16(s1))), oa = {}, ob = {};
  sa = cat16(s0 + na, s1 + nb);
  unsigned idx = 0xDDDDDDDDu, cnt = 8, dk = -12288, dv = -11264;
  {
    const char* xg = c.p.x + (size_t)((c.rt * 64 + 32 * (w & 1) + 16 * (w >> 1) + (l & 15)) * 1280 + c.cs * 160 + (l >> 4) * 8);
#pragma unroll
    for (int nt = 0; nt < 5; ++nt) res[nt] = *(const GAS u32x2*)(xg + nt * 32);
  }
  STAMP(7);
  asm volatile(
      "s_waitcnt lgkmcnt(0)\n\ts_nop 4\n"
      "1:\n\t"
      STEP(R_SA, R_SB, R_VA, R_VB, 1, 0, STAGE_TOP, KRL(0))
      STEP(R_SB, R_SA, R_VB, R_VA, 0, 1, "", "")
      STEP(R_SA, R_SB, R_VA, R_VB, 1, 2, "", KRL(2))
      STEP(R_SB, R_SA, R_VB, R_VA, 0, 3, "", "")
      STEP(R_SA, R_SB, R_VA, R_VB, 1, 4, "", KRL(4))
      STEP(R_SB, R_SA, R_VB, R_VA, 0, 5, "", "")
      STEP(R_SA, R_SB, R_VA, R_VB, 1, 6, "", KRL(6))
      STEP(R_SB, R_SA, R_VB, R_VA, 0, 7, "", "")
      "s_sub_u32 %[cnt], %[cnt], 1\n\t"
      "s_cmp_lg_u32 %[cnt], 0\n\t"
      "s_cbranch_scc1 1b\n\t"
      "s_waitcnt lgkmcnt(0)\n\t"
      "s_barrier\n\t"
      PV(R_OB, R_VB + 4, R_SB + 24)
      "s_nop 15\n\t"
      : "+{v[32:63]}"(sa), "+{v[64:95]}"(sb), "+{v[96:111]}"(oa), "+{v[112:127]}"(ob), "+{v[170:171]}"(kr), "+{v[172:175]}"(kf), "+{v[176:183]}"(va), "+{v[184:191]}"(vb),
        "+{v193}"(kad), "+{v194}"(krad), "+{v195}"(vad), [cnt] "+s"(cnt), [dk] "+s"(dk), [dv] "+s"(dv)
      : "{v[128:143]}"(na), "{v[144:159]}"(nb), "{v[160:163]}"(qa), "{v[164:167]}"(qb), "{v[168:169]}"(qr), "{v192}"(idx)
      : "memory", "scc");
  STAMP(8);
  if (__any(out_of_range(oa) || out_of_range(ob))) {
    float ma = -INFINITY, mb = -INFINITY;
#pragma unroll 1
    for (int pass = 0; pass < 2; ++pass) {
      na = splat16(pass ? -ma : 0.f); nb = splat16(pass ? -mb : 0.f); oa = splat16(0.f); ob = splat16(0.f);
#pragma unroll 1
      for (int t = 0; t < 64; ++t) {
        const s16x8 k16 = __builtin_bit_cast(s16x8, ld128_d(c.rs, ko + t * 512));
        f32x32 s = cat16(mfma32(k16, qa, na), mfma32(k16, qb, nb));
        s = __builtin_amdgcn_mfma_f32_32x32x4bf16_1k(__builtin_bit_cast(s16x4, ld64_d(c.rs, kro + t * 512)), qr, s, 0, 0, 0);
        if (pass == 0) { ma = fmaxf(ma, max16(lo16(s))); mb = fmaxf(mb, max16(hi16(s))); }
        else {
#pragma unroll
          for (int g = 0; g < 2; ++g) {
            u32x4 vf = ld128_d(c.rs, vg + (4 * t + 2 * g) * 320);
            if (r >= 20) vf = u32x4{pad, pad, pad, pad};
            f32x8 pa, pb;
#pragma unroll
            for (int i = 0; i < 8; ++i) { pa[i] = __builtin_amdgcn_exp2f(s[8 * g + i]); pb[i] = __builtin_amdgcn_exp2f(s[16 + 8 * g + i]); }
            oa = __builtin_amdgcn_smfmac_f32_32x32x32_bf16(__builtin_bit_cast(bf16x8, vf), __builtin_bit_cast(bf16x16, pa), oa, 0xDDDD, 0, 0);
            ob = __builtin_amdgcn_smfmac_f32_32x32x32_bf16(__builtin_bit_cast(bf16x8, vf), __builtin_bit_cast(bf16x16, pb), ob, 0xDDDD, 0, 0);
          }
        }
      }
      if (pass == 0) { ma = swap32_max(ma); mb = swap32_max(mb); }
    }
  }
#pragma unroll
  for (int t = 0; t < 2; ++t) {
    const f32x16& o = t ? ob : oa;
    const float rl = __builtin_amdgcn_rcpf(upper_half(o[8]));
    unsigned pk[6];
#pragma unroll
    for (int i = 0; i < 6; ++i) pk[i] = cvtpk(o[2 * i] * rl, o[2 * i + 1] * rl);
    const unsigned og = WS_O + (q0 >> 6) * 81920 + (32 * t + r) * 16;
    st128_d(c.rs, og + (cb + e + hh) * 1024, np(swap32_pair(pk[0], pk[1], pk[2], pk[3])));
    if (hh == 0) st64_d(c.rs, og + (e ? cb : cb + 2) * 1024 + 8 * e, u32x2{np(pk[4]), np(pk[5])});
  }
}

#define G_ACC 32
#define G_B0 72
#define G_B1 92
#define G_A 112
#define G_VB 176
#define G_VBO 177
#define G_VA0 178
#define G_VA1 180
#define G_CHK 182
#define GMM(m, nt, B, j) "v_mfma_f32_16x16x32_bf16 " VR(G_ACC + m * 20 + nt * 4, 4) ", " VR(B + nt * 4, 4) ", " VR(G_A + j * 8 + m * 4, 4) ", " VR(G_ACC + m * 20 + nt * 4, 4) "\n\t"
#define GRB(B, nt, ks) "ds_read_b64_tr_b16 " VR(B + nt * 4, 2) ", v" STR(G_VB) " offset:" STR(ks * 5120 + nt * 32) "\n\t" \
                       "ds_read_b64_tr_b16 " VR(B + nt * 4 + 2, 2) ", v" STR(G_VB) " offset:" STR(ks * 5120 + nt * 32 + 1280) "\n\t"
#define GCK(j, i) "v_max3_u32 v" STR(G_CHK) ", v[" STR(G_A + j * 8 + i * 2) "], v[" STR(G_A + j * 8 + i * 2 + 1) "], v" STR(G_CHK) "\n\t"
#define GLD1(j, m, va) "buffer_load_dwordx4 " VR(G_A + (j) * 8 + m * 4, 4) ", v[" STR(va + m) "], %[rs], %[so] offen sc1\n\t"
#define GAD0 "s_add_u32 %[so], %[so], 16384\n\t"
#define GAD1 "s_cmp_eq_u32 %[so], %[sw]\n\t"
#define GAD2 "s_cselect_b32 %[so], 0, %[so]\n\t"
#define GLD2(j) GLD1(j, 0, G_VA0) GLD1(j, 1, G_VA0) GLD1(j + 1, 0, G_VA1) GLD1(j + 1, 1, G_VA1)
#define GLD2A(j) GLD2(j) GAD0 GAD1 GAD2
#define GNX0 "s_add_u32 %[sl], %[sl], 20480\n\t"
#define GNX1 "s_cmp_eq_u32 %[sl], %[lw]\n\t"
#define GNX2 "s_cselect_b32 %[sl], %[lr], %[sl]\n\t"
#define GADR "v_add_u32 v" STR(G_VB) ", %[sl], v" STR(G_VBO) "\n\t"
#define GBODY(j, BC, BN, ks, VM, W0, PRE, S0, S1, S2, L0, L1) \
  "s_waitcnt vmcnt(" STR(VM) ") lgkmcnt(" STR(W0) ")\n\t" PRE \
  GMM(0, 0, BC, j) GRB(BN, 0, ks) GMM(1, 0, BC, j) GRB(BN, 1, ks) GMM(0, 1, BC, j) GRB(BN, 2, ks) GMM(1, 1, BC, j) GRB(BN, 3, ks) \
  GMM(0, 2, BC, j) GRB(BN, 4, ks) GMM(1, 2, BC, j) S0 WL(10) GMM(0, 3, BC, j) GCK(j, 0) S1 GMM(1, 3, BC, j) GCK(j, 1) S2 \
  GMM(0, 4, BC, j) GCK(j, 2) L0 GMM(1, 4, BC, j) GCK(j, 3) L1
#define GEVEN(j) GBODY(j, G_B0, G_B1, 1, 14, 4, "", GAD0, GAD1, GAD2, GLD1(j, 0, G_VA0), GLD1(j, 1, G_VA0))
#define GODD(j) GBODY(j, G_B1, G_B0, 0, 14, 4, GADR, GNX0, GNX1, GNX2, GLD1(j, 0, G_VA1), GLD1(j, 1, G_VA1))
#define GODDB(j) GBODY(j, G_B1, G_B0, 0, 14, 0, "s_barrier\n\t" GADR, GNX0, GNX1, GNX2, GLD1(j, 0, G_VA1), GLD1(j, 1, G_VA1))
#define TEVEN(j) GBODY(j, G_B0, G_B1, 1, 14 - 2 * j, 4, "", "", "", "", "", "")
#define TODD(j) GBODY(j, G_B1, G_B0, 0, 14 - 2 * j, 4, GADR, GNX0, GNX1, GNX2, "", "")

DI void g80_compute(const Ctx& c, int last, u32x2 (&res)[5]) {
  int l = c.l; asm volatile("" : "+v"(l));
  const int w = c.w, wm = w & 1, wk = w >> 1, g = l >> 4, q = (l & 15) >> 2, pp = l & 3;
  const unsigned tile = last ? WS_F + c.rt * 196608 : WS_O + c.rt * 81920;
  __builtin_amdgcn_s_barrier();
  const int row = 32 * wm + 16 * wk + (l & 15);
  f32x32 acc0 = {}; f32x8 acc1;
#pragma unroll
  for (int i = 0; i < 20; ++i) { const float v = (i & 1) ? bfhi(res[i >> 2][(i >> 1) & 1]) : bflo(res[i >> 2][(i >> 1) & 1]); if (i < 12) acc0[20 + i] = v; else acc1[i - 12] = v; }
  asm volatile("" : "+{v[32:63]}"(acc0), "+{v[64:71]}"(acc1));
  __builtin_amdgcn_s_sleep(PDELAY);
  {
    const int hd = l & 31, e = hd & 1, cb = 5 * (hd >> 1) + 2 * e;
    const unsigned po = tile + (last ? ((hd >> 2) * 24 + (hd & 3) * 6 + 5) * 1024 + 63 * 16 : (e ? cb : cb + 2) * 1024 + 63 * 16 + 8 * e);
#pragma unroll 1
    for (unsigned n = 0; n < SPIN_CAP; ++n) { FENCE(); if (!__any(ld32_d(c.rs, po) == POISON)) break; __builtin_amdgcn_s_sleep(PSLEEP); }
  }
  if (last) STAMP(16); else STAMP(10);
  const unsigned va0 = tile + (8 * wk + g) * 1024 + row * 16;
  const unsigned r0 = last ? c.rot5 : (5 * c.cs) >> 3;
  unsigned chk = 0, so = r0 * 16384, sl = last ? 0 : L_WO + r0 * 20480, cnt = last ? 2 : 1;
  asm volatile(
      "s_cmp_eq_u32 %[cnt], 1\n\ts_cbranch_scc0 4f\n\t"
      GLD2A(6)
      "4:\n\t"
      GLD2A(0) GLD2A(2) TS(0)
      "s_barrier\n\t" TS(1)
      GADR
      GRB(G_B0, 0, 0) GRB(G_B0, 1, 0) GRB(G_B0, 2, 0) GRB(G_B0, 3, 0) GRB(G_B0, 4, 0)
      GNX0 GNX1 GNX2
      GLD2(4)
      "s_cmp_eq_u32 %[cnt], 1\n\ts_cbranch_scc1 2f\n\t"
      GAD0 GAD1 GAD2 GLD2(6)
      "1:\n\t"
      GEVEN(0) GODD(1) GEVEN(2) GODD(3) GEVEN(4) GODD(5)
      "2:\n\t"
      GEVEN(6)
      GODDB(7)
      "s_sub_u32 %[cnt], %[cnt], 1\n\ts_cmp_lg_u32 %[cnt], 0\n\ts_cbranch_scc1 1b\n\t"
      TEVEN(0) TODD(1) TEVEN(2) TODD(3) TEVEN(4) TODD(5) TEVEN(6)
      "s_waitcnt vmcnt(0) lgkmcnt(0)\n\ts_barrier\n\t"
      GMM(0, 0, G_B1, 7) GMM(1, 0, G_B1, 7) GCK(7, 0) GMM(0, 1, G_B1, 7) GCK(7, 1) GMM(1, 1, G_B1, 7) GCK(7, 2) GMM(0, 2, G_B1, 7) GCK(7, 3)
      GMM(1, 2, G_B1, 7) GMM(0, 3, G_B1, 7) GMM(1, 3, G_B1, 7) GMM(0, 4, G_B1, 7) GMM(1, 4, G_B1, 7)
      "s_nop 15\n\t" TS(5)
      : "+{v[32:63]}"(acc0), "+{v[64:71]}"(acc1), "+{v182}"(chk), [so] "+s"(so), [sl] "+s"(sl), [cnt] "+s"(cnt)
      : "{v177}"(c.lds0 + (64 * wk + 16 * (g >> 1) + 4 * (g & 1) + q) * 160 + pp * 8), "{v178}"(va0 ^ 256), "{v179}"(va0), "{v180}"((va0 ^ 256) + 4096), "{v181}"(va0 + 4096), [rs] "s"(c.rs),
        [sw] "s"(last ? 196608 : 81920), [lw] "s"(last ? 163840 : L_WO + 102400), [lr] "s"(last ? 0 : L_WO)
      : "memory", "scc", "v176", CL8(160, 161, 162, 163, 164, 165, 166, 167), CL8(168, 169, 170, 171, 172, 173, 174, 175),
        "v72", "v73", "v74", "v75", "v76", "v77", "v78", "v79", "v80", "v81", "v82", "v83", "v84", "v85", "v86", "v87", "v88", "v89", "v90", "v91", "v92", "v93", "v94", "v95",
        "v96", "v97", "v98", "v99", "v100", "v101", "v102", "v103", "v104", "v105", "v106", "v107", "v108", "v109", "v110", "v111", "v112", "v113", "v114", "v115", "v116", "v117", "v118", "v119",
        "v120", "v121", "v122", "v123", "v124", "v125", "v126", "v127", "v128", "v129", "v130", "v131", "v132", "v133", "v134", "v135", "v136", "v137", "v138", "v139", "v140", "v141", "v142", "v143",
        "v144", "v145", "v146", "v147", "v148", "v149", "v150", "v151", "v152", "v153", "v154", "v155", "v156", "v157", "v158", "v159" TS_CLOB);
  if (last) { TS_PUT(0, 25); TS_PUT(1, 26); TS_PUT(2, 27); TS_PUT(3, 28); TS_PUT(4, 29); TS_PUT(5, 30); } else { TS_PUT(0, 19); TS_PUT(1, 20); TS_PUT(4, 21); TS_PUT(5, 22); }
  f32x4 acc[2][5];
#pragma unroll
  for (int i = 0; i < 40; ++i) acc[i / 20][(i % 20) / 4][i % 4] = i < 32 ? acc0[i] : acc1[i - 32];
  if (__any(chk == POISON)) {
    const char* wg = (last ? c.p.w2 : c.p.wo) + c.cs * 160 + (l & 15) * 2;
#pragma unroll
    for (int nt = 0; nt < 5; ++nt) { acc[0][nt] = f32x4{}; acc[1][nt] = f32x4{bflo(res[nt][0]), bfhi(res[nt][0]), bflo(res[nt][1]), bfhi(res[nt][1])}; }
#pragma unroll 1
    for (int st = 0; st < (last ? 24 : 10); ++st) {
      u32x4 a0, a1;
#pragma unroll 1
      for (unsigned n = 0; n < SPIN_CAP; ++n) {
        FENCE();
        a0 = ld128_d(c.rs, (va0 ^ 256) + (st >> 1) * 16384 + (st & 1) * 4096); a1 = ld128_d(c.rs, va0 + (st >> 1) * 16384 + (st & 1) * 4096);
        if (!__any(umax(umax4(a0), umax4(a1)) == POISON)) break;
      }
      const char* wr = wg + (size_t)((st >> 1) * 128 + 64 * wk + (st & 1) * 32 + 8 * g) * 1280;
#pragma unroll
      for (int nt = 0; nt < 5; ++nt) {
        s16x8 bf;
#pragma unroll
        for (int j = 0; j < 8; ++j) bf[j] = *(const GAS short*)(wr + j * 1280 + nt * 32);
        acc[0][nt] = mfma16(bf, __builtin_bit_cast(s16x8, a0), acc[0][nt]); acc[1][nt] = mfma16(bf, __builtin_bit_cast(s16x8, a1), acc[1][nt]);
      }
    }
  }
  lptr ex = c.lds + L_EX + l * 16;
#pragma unroll
  for (int nt = 0; nt < 5; ++nt) *(LAS f32x4*)(ex + w * 5120 + nt * 1024) = acc[0][nt];
  const rsrc_t ro = __builtin_amdgcn_make_buffer_rsrc(last ? c.p.out : c.p.ws + WS_H, 0, S * D * 2, 0x00020000);
  const unsigned ob = c.rt * 81920 + (last ? c.cs * 160 + row * 1280 : c.cs * 10240 + row * 16), oc = last ? 16 : 1024;
  unsigned so3[3];
#pragma unroll
  for (int i = 0; i < 3; ++i) so3[i] = ob + (4 * i + (i < 2 ? 2 * (g & 1) : 0) + (g >> 1)) * oc;
  asm volatile("" : "+v"(so3[0]), "+v"(so3[1]), "+v"(so3[2]));
  LDS_BAR();
  if (last) STAMP(17); else STAMP(11);
  f32x4 o4[5];
#pragma unroll
  for (int nt = 0; nt < 5; ++nt) o4[nt] = *(LAS f32x4*)(ex + (w ^ 2) * 5120 + nt * 1024);
  __builtin_amdgcn_sched_barrier(0);
#pragma unroll
  for (int nt = 0; nt < 5; ++nt) { const f32x4 u = acc[1][nt] + o4[nt]; res[nt] = u32x2{np(cvtpk(u[0], u[1])), np(cvtpk(u[2], u[3]))}; }
  u32x4 pc[3];
#pragma unroll
  for (int i = 0; i < 3; ++i) { const int n1 = i < 2 ? 2 * i + 1 : 4; pc[i] = swap16_pair(res[2 * i][0], res[2 * i][1], res[n1][0], res[n1][1]); }
  if (last) {
#pragma unroll
    for (int i = 0; i < 3; ++i) __builtin_amdgcn_raw_buffer_store_b128(pc[i], ro, so3[i], 0, 0);
  } else {
#pragma unroll
    for (int i = 0; i < 3; ++i) st128_d(ro, so3[i], pc[i]);
    STAMP(12);
    __builtin_amdgcn_s_barrier();
    float s1 = 0.f, s2 = 0.f;
#pragma unroll
    for (int nt = 0; nt < 5; ++nt) { s1 = dot2(res[nt][0], ONES2, dot2(res[nt][1], ONES2, s1)); s2 = dot2(res[nt][0], res[nt][0], dot2(res[nt][1], res[nt][1], s2)); }
    s1 = swap16_add(s1, s1); s2 = swap16_add(s2, s2); s1 = swap32_add(s1, s1); s2 = swap32_add(s2, s2);
    if (l < 16) st64_d(c.rs, WS_RS2 + (c.rt * 8 + c.cs) * 512 + row * 8, u32x2{np(__float_as_uint(s1)), np(__float_as_uint(s2))});
  }
}

#define F_X 80
#define F_Y 108
#define FMM(S, nt, m) "v_mfma_f32_16x16x32_bf16 " VR(32 + nt * 16 + m * 4, 4) ", " VR(S + nt * 4, 4) ", " VR(S + 12 + m * 4, 4) ", " VR(32 + nt * 16 + m * 4, 4) "\n\t"
#define FRA(S, m, ks) "ds_read_b128 " VR(S + 12 + m * 4, 4) ", v140 offset:" STR(ks * 4096 + m * 256) "\n\t"
#define FRB(S, nt, h, ks) "ds_read_b64_tr_b16 " VR(S + nt * 4 + h * 2, 2) ", v[141+" STR(nt) "] offset:" STR(ks * 12288 + h * 3072) "\n\t"
#define FRF(m) "ds_read2st64_b32 v[144+2*" #m ":145+2*" #m "], v152 offset0:0 offset1:1\n\tv_add_u32 v152, 64, v152\n\t"
DI void p4_compute(const Ctx& c) {
  int l = c.l; asm volatile("" : "+v"(l));
  const int w = c.w, g = l >> 4, q = (l & 15) >> 2, pp = l & 3;
  unsigned boff[3];
#pragma unroll
  for (int nt = 0; nt < 3; ++nt) boff[nt] = c.lds0 + (16 * (g >> 1) + 4 * (g & 1) + q) * 384 + (((3 * w + nt) ^ (2 * (g & 1) + (q >> 1))) * 32) + pp * 8;
  f32x32 ac0 = {}; f32x16 ac1 = {}; f32x8 ab;
  unsigned ch = 0, sa, sb = 0, rap = c.lds0 + L_RA + (l & 15) * 4;
  f32x4 c4[3];
#pragma unroll
  for (int nt = 0; nt < 3; ++nt) c4[nt] = *(LAS f32x4*)(c.lds + L_CSUM + (w * 48 + nt * 16 + 4 * g) * 4);
  asm volatile("" : "+v"(c4[0]), "+v"(c4[1]), "+v"(c4[2]));
  asm volatile(
      "v_mov_b32 v140, v136\n\tv_mov_b32 v141, v137\n\tv_mov_b32 v142, v138\n\tv_mov_b32 v143, v139\n\t"
      "s_barrier\n\t" TS(0)
      FRB(F_Y, 0, 0, 0) FRB(F_Y, 0, 1, 0) FRA(F_Y, 0, 0) FRA(F_Y, 1, 0) FRA(F_Y, 2, 0) FRA(F_Y, 3, 0) FRB(F_Y, 1, 0, 0) FRB(F_Y, 1, 1, 0) FRB(F_Y, 2, 0, 0) FRB(F_Y, 2, 1, 0)
      "s_branch 2f\n"
      "1:\n\t"
      FMM(F_X, 0, 2) FRB(F_Y, 0, 0, 0) FMM(F_X, 0, 3) FRB(F_Y, 0, 1, 0) FMM(F_X, 1, 0) FRA(F_Y, 0, 0) FMM(F_X, 1, 1) FRA(F_Y, 1, 0) FMM(F_X, 1, 2) FRA(F_Y, 2, 0)
      FMM(F_X, 1, 3) FRA(F_Y, 3, 0) FMM(F_X, 2, 0) FRB(F_Y, 1, 0, 0) FMM(F_X, 2, 1) FRB(F_Y, 1, 1, 0) FMM(F_X, 2, 2) FRB(F_Y, 2, 0, 0) FMM(F_X, 2, 3) FRB(F_Y, 2, 1, 0)
      "2:\n\t"
      WL(7) FMM(F_Y, 0, 0) FRB(F_X, 0, 0, 1) WL(7) FMM(F_Y, 0, 1) FRB(F_X, 0, 1, 1) WL(7) FMM(F_Y, 0, 2) FRA(F_X, 0, 1) WL(7) FMM(F_Y, 0, 3) FRA(F_X, 1, 1)
      WL(6) FMM(F_Y, 1, 0) FRA(F_X, 2, 1) "s_add_u32 %[ch], %[ch], 1\n\t" FMM(F_Y, 1, 1) FRA(F_X, 3, 1) "s_add_u32 %[sb], %[sb], 24576\n\t"
      FMM(F_Y, 1, 2) FRB(F_X, 1, 0, 1) "s_cmp_eq_u32 %[ch], 5\n\t" FMM(F_Y, 1, 3) FRB(F_X, 1, 1, 1)
      WL(8) FMM(F_Y, 2, 0) FRB(F_X, 2, 0, 1) "s_cselect_b32 %[sb], 0, %[sb]\n\t" FMM(F_Y, 2, 1) FRB(F_X, 2, 1, 1) "s_and_b32 %[sa], %[ch], 3\n\t"
      FMM(F_Y, 2, 2) "s_lshl_b32 %[sa], %[sa], 13\n\tv_add_u32 v141, %[sb], v137\n\t" FMM(F_Y, 2, 3) "v_add_u32 v142, %[sb], v138\n\t"
      WL(7) FMM(F_X, 0, 0) "v_add_u32 v143, %[sb], v139\n\t" WL(6) FMM(F_X, 0, 1) "v_add_u32 v140, %[sa], v136\n\ts_cmp_eq_u32 %[ch], 10\n\t"
      WL(0) "s_barrier\n\t"
      "s_cbranch_scc0 1b\n\t"
      FMM(F_X, 0, 2) FRF(0) FMM(F_X, 0, 3) FRF(1) FMM(F_X, 1, 0) FRF(2) FMM(F_X, 1, 1) FRF(3) FMM(F_X, 1, 2) FMM(F_X, 1, 3) FMM(F_X, 2, 0) FMM(F_X, 2, 1) FMM(F_X, 2, 2) FMM(F_X, 2, 3)
      WL(0) "s_nop 7"
      : "+{v[32:63]}"(ac0), "+{v[64:79]}"(ac1), "={v[144:151]}"(ab), "+{v152}"(rap), [ch] "+s"(ch), [sa] "=&s"(sa), [sb] "+s"(sb)
      : "{v136}"(c.lds0 + L4_A + g * 1024 + (l & 15) * 16), "{v137}"(boff[0]), "{v138}"(boff[1]), "{v139}"(boff[2])
      : "memory", "scc", "v140", "v141", "v142", "v143", CL8(80, 81, 82, 83, 84, 85, 86, 87), CL8(88, 89, 90, 91, 92, 93, 94, 95), CL8(96, 97, 98, 99, 100, 101, 102, 103), CL8(104, 105, 106, 107, 108, 109, 110, 111),
        CL8(112, 113, 114, 115, 116, 117, 118, 119), CL8(120, 121, 122, 123, 124, 125, 126, 127), CL8(128, 129, 130, 131, 132, 133, 134, 135) TS_CLOB);
  TS_PUT(0, 13);
  f32x4 acc[4][3];
#pragma unroll
  for (int i = 0; i < 48; ++i) acc[(i >> 2) & 3][i >> 4][i & 3] = i < 32 ? ac0[i] : ac1[i - 32];
  STAMP(14);
#pragma unroll
  for (int nt = 0; nt < 3; ++nt)
#pragma unroll
    for (int mp = 0; mp < 2; ++mp) {
      unsigned pk[2][2];
#pragma unroll
      for (int j = 0; j < 2; ++j) {
        const int m = 2 * mp + j; float y[4];
#pragma unroll
        for (int i = 0; i < 4; ++i) y[i] = fmaf(acc[m][nt][i], ab[2 * m], ab[2 * m + 1] * c4[nt][i]);
        pk[j][0] = relu2(cvtpk(y[0], y[1])); pk[j][1] = relu2(cvtpk(y[2], y[3]));
      }
      st128_d(c.rs, WS_F + c.rt * 196608 + (c.cs * 24 + w * 6 + nt * 2 + (g >> 1)) * 1024 + (32 * mp + (g & 1) * 16 + (l & 15)) * 16, swap16_pair(pk[0][0], pk[0][1], pk[1][0], pk[1][1]));
    }
}

DI void compute_waves(Ctx& c) {
  const Params& p = c.p;
  read_tag(c);
  const int l = c.l, w = c.w, b = c.b, r = l & 31, hh = l >> 5;
  if (c.ct == 15) { __builtin_amdgcn_s_sleep(CPD); spare_poison(c); }
  {
    const int xrow = c.xr0 + 2 * w + hh, rg = c.wi & 7, cg = 15 * (b & 1) + (c.wi >> 3);
    LDS_BAR();
    lptr xs = c.lds + L1_XS + (2 * w + hh) * 1280 + r * 16, ws = c.lds + L1_WS + (w * 64 + l) * 16;
    const u32x4 xv[3] = {*(LAS u32x4*)xs, *(LAS u32x4*)(xs + 512), *(LAS u32x4*)(xs + 1024)};
    u32x4 wv[3] = {*(LAS u32x4*)ws, *(LAS u32x4*)(ws + 4096), {}};
    if (w < 2) wv[2] = *(LAS u32x4*)(ws + 8192);
    row_stats(c, xv, xrow);
    colsum_step1(c.lds + L_PART, l, w, wv);
    LDS_BAR();
    const float v = colsum_step2(c.lds + L_PART, l, w);
    if (l < 16 && c.ct < 15) st64_d(c.rs, WS_CS1 + (rg * 1920 + cg * 64 + 16 * w + l) * 8, u32x2{__float_as_uint(v), (unsigned)c.tag});
    STAMP(1);
    if (c.ct < 10) p1_compute<false>(c);
    else if (c.ct < 15) p1_compute<true>(c);
    else publish<0>(c, L_CNT0 + 12, 8, WS_F0 + c.b * 8);
  }
  STAMP(4);
  u32x2 res[5];
  p2_compute(c, res);
  STAMP(9);
  int last = 0;
#pragma unroll 1
  for (;;) {
    asm volatile("" : "+s"(last));
    g80_compute(c, last, res);
    if (last) break;
    p4_compute(c);
    STAMP(15);
    asm volatile(".Lblk_end1:");
    last = 1;
  }
  STAMP(18);
  asm volatile(".Lblk_end2:");
}

struct Park { f32x32 a, b, c, d, e; f32x16 f; f32x4 g; };
#define PARK_REGS(m, pk) m "{v[76:107]}"(pk.a), m "{v[108:139]}"(pk.b), m "{v[140:171]}"(pk.c), m "{v[172:203]}"(pk.d), m "{v[204:235]}"(pk.e), m "{v[236:251]}"(pk.f), m "{v[252:255]}"(pk.g)
#define PKL1(k, j, o) "buffer_load_dwordx4 " VR(76 + k * 20 + j * 4, 4) ", %[v" #j "], %[rs], %[so] offen offset:" STR(o) "\n\t"
#define PKL(k, ch) "s_add_u32 %[so], %[r5], " STR(ch * 163840 - 1966080) "\n\ts_cselect_b32 %[so], %[so], " STR(ch * 163840) "\n\ts_cselect_b32 %[t], 0, %[r5]\n\ts_add_u32 %[so], %[so], %[t]\n\t" PKL1(k, 0, 0) PKL1(k, 1, 1024) PKL1(k, 2, 2048) PKL1(k, 3, 3072) PKL1(k, 4, 0)
#define W80_OPS(r) [rs] "s"(r), [v0] "v"(vo80[0]), [v1] "v"(vo80[1]), [v2] "v"(vo80[2]), [v3] "v"(vo80[3]), [v4] "v"(vo80[4])
#define W80_DMA(r, dst, off) asm volatile("s_mov_b32 m0, %[d]\n\ts_nop 0\n\t" \
    "buffer_load_dwordx4 %[v0], %[rs], %[so] offen lds\n\tbuffer_load_dwordx4 %[v1], %[rs], %[so] offen offset:1024 lds\n\t" \
    "buffer_load_dwordx4 %[v2], %[rs], %[so] offen offset:2048 lds\n\tbuffer_load_dwordx4 %[v3], %[rs], %[so] offen offset:3072 lds\n\t" \
    "s_add_u32 m0, %[d], 4096\n\ts_nop 0\n\tbuffer_load_dwordx4 %[v4], %[rs], %[so] offen lds" :: [d] "s"(dst), [so] "s"(off), W80_OPS(r) : "memory", "scc")
#define PKS1(k, j) "ds_write_b128 %[va], " VR(76 + k * 20 + j * 4, 4) " offset:" STR(j * 1024) "\n\t"
#define PUT(k, slot) asm volatile(PKS1(k, 0) PKS1(k, 1) PKS1(k, 2) PKS1(k, 3) PKS1(k, 4) :: [va] "v"(wa + slot * 20480), PARK_REGS("", pk) : "memory")
#define P1_STATE [sa] "+s"(sa), [sb] "+s"(sb), [md] "+s"(md)
#define P1_CONST [rx] "s"(rx), [rw] "s"(rw), [l0] "s"(lds0 + lw * 4096), [a0] "v"(va[0]), [a1] "v"(va[1]), [a2] "v"(va[2]), [a3] "v"(va[3]), [b0] "v"(vb[0]), [b1x] "v"(vb[1]), [b2] "v"(vb[2]), [b3] "v"(vb[3])
#define P1_LD(v, r, s, o) "buffer_load_dwordx4 %[" v "], %[" r "], %[" s "] offen offset:" #o " lds\n\t"
#define P1_STAGE \
  "s_mov_b32 m0, %[md]\n\ts_add_u32 %[md], %[md], 16384\n\t" \
  P1_LD("a0", "rx", "sa", 0) P1_LD("a1", "rx", "sa", 1024) P1_LD("a2", "rx", "sa", 2048) P1_LD("a3", "rx", "sa", 3072) \
  "s_mov_b32 m0, %[md]\n\ts_add_u32 %[sa], %[sa], 128\n\t" \
  P1_LD("b0", "rw", "sb", 0) P1_LD("b1x", "rw", "sb", 1024) P1_LD("b2", "rw", "sb", 2048) P1_LD("b3", "rw", "sb", 3072) \
  "s_add_u32 %[sb], %[sb], 245760\n\ts_cmp_eq_u32 %[sa], 1280\n\ts_cselect_b32 %[sa], 0, %[sa]\n\ts_cselect_b32 %[sb], 0, %[sb]\n\t" \
  "s_sub_u32 %[md], %[md], %[l0]\n\ts_add_u32 %[md], %[md], 16384\n\ts_and_b32 %[md], %[md], 0x18000\n\ts_add_u32 %[md], %[md], %[l0]\n\t"
#define W1_OPS [rw] "s"(rw1), [dw] "s"(dw), [v0] "v"(vo[0]), [v1] "v"(vo[1]), [v2] "v"(vo[2]), [v3] "v"(vo[3]), [v4] "v"(vo[4]), [v5] "v"(vo[5])
#define LW1(v, o) "buffer_load_dwordx4 %[" v "], %[rw], %[t0] offen offset:" STR(o) " lds\n\t"
#define LW3_0 LW1("v0", 0) LW1("v1", 1024) LW1("v2", 2048)
#define LW3_1 LW1("v3", 0) LW1("v4", 1024) LW1("v5", 2048)
#define LW3(ch, u) "s_add_u32 m0, %[dw], " STR((ch - 5 * (ch / 5)) * 24576 + u * 3072) "\n\ts_mov_b32 %[t0], " STR(ch * 196608) "\n\t" LW3_##u
#define LA(ch) "s_add_u32 m0, %[da], " STR((ch & 3) * 8192) "\n\ts_mov_b32 %[t0], " STR(ch * 8192) "\n\t" \
  "buffer_load_dwordx4 %[va], %[rs], %[t0] offen sc1 lds\n\tbuffer_load_dwordx4 %[va], %[rs], %[t0] offen offset:1024 sc1 lds\n\t"
#define CKR(ch) "ds_read_b128 v[60:63], %[la] offset:" STR((ch & 3) * 8192) "\n\tds_read_b128 v[64:67], %[la] offset:" STR((ch & 3) * 8192 + 1024) "\n\t"
#define CKT "s_waitcnt lgkmcnt(0)\n\tv_max3_u32 v60, v60, v61, v62\n\tv_max3_u32 v64, v63, v64, v65\n\tv_max3_u32 v60, v60, v66, v67\n\tv_max_u32 v60, v60, v64\n\tv_cmp_eq_u32 vcc, -1, v60\n\t"
#define FIX(ch) "s_cbranch_vccz 2f\n\ts_mov_b32 %[t1], 0\n1:\n\ts_sleep 2\n\t" LA(ch) "s_waitcnt vmcnt(0)\n\t" CKR(ch) CKT \
  "s_cbranch_vccz 2f\n\ts_add_u32 %[t1], %[t1], 1\n\ts_cmp_lt_u32 %[t1], 0x40000\n\ts_cbranch_scc1 1b\n2:\n\t"
#define IVL(ch, VA, FETCH, VW) "s_waitcnt vmcnt(" STR(VA) ")\n\t" CKR(ch) FETCH CKT FIX(ch) "s_waitcnt vmcnt(" STR(VW) ")\n\t"

DI void loader_waves(Ctx& c) {
  const Params& p = c.p;
  const int l = c.l, lw = c.w - 4, b = c.b, t4 = lw * 64 + l, cs = c.cs, rt = c.rt;
  const unsigned lds0 = c.lds0, lin = l * 16;
  {
    {
      unsigned vo = lw < 2 ? lin : (l >> 3) * 3840 + (l & 7) * 16;
      unsigned so = lw < 2 ? c.xr0 * 1280 + lw * 5120 : ((c.wi & 7) * 80 + (lw - 2) * 40) * 3840 + (15 * (b & 1) + (c.wi >> 3)) * 128, st = lw < 2 ? 1024 : 8 * 3840;
      int isx = lw;
      asm volatile("" : "+s"(isx), "+v"(vo), "+s"(so), "+s"(st));
      __builtin_amdgcn_sched_barrier(0);
      STAMP(0);
      const char* src = (isx < 2 ? p.x : p.wqkv) + so;
#pragma unroll
      for (int i = 0; i < 5; ++i) glds(src + i * st, vo, lds0 + L1_XS + lw * 5120 + i * 1024);
      __builtin_amdgcn_sched_barrier(0);
    }
    const int rot = (5 * ((c.ct + 2 * c.panel) & 7)) >> 2;
    unsigned va[4], vb[4], sa = rot * 128, sb = rot * 245760, md = lds0 + 65536 + lw * 4096;
#pragma unroll
    for (int i = 0; i < 4; ++i) {
      va[i] = (32 * lw + 8 * i + (l >> 3)) * 1280 + (((l & 7) ^ ((4 * i + (l >> 4)) & 7)) << 4) - i * 1024;
      vb[i] = (16 * lw + 4 * i + (l >> 4)) * 3840 + (((l & 15) ^ (((l >> 4) << 2) | (i & 2))) << 4) - i * 1024;
    }
    const rsrc_t rx = __builtin_amdgcn_make_buffer_rsrc((void*)(p.x + c.panel * 163840), 0, 163840, 0x00020000);
    const rsrc_t rw = __builtin_amdgcn_make_buffer_rsrc((void*)(p.wqkv + c.ct * 256), 0, 640 * 3840 - c.ct * 256, 0x00020000);
    if (c.ct < 15) asm volatile("s_sleep " STR(SDELAY) "\n\t" P1_STAGE "s_waitcnt vmcnt(8)\n\ts_barrier\n\t" P1_STAGE : P1_STATE : P1_CONST : "memory", "scc");
    else { read_tag(c); __builtin_amdgcn_s_sleep(CPD); spare_poison(c); wait_bar<0>(); }
    read_tag(c);
    STAMP(2);
    {
      const char* pc; unsigned len;
      asm volatile("s_getpc_b64 %0\n.Lblk_pc:\n\ts_mov_b32 %1, .Lblk_end1-.Lblk_pc\n\ts_max_i32 %1, %1, .Lblk_end2-.Lblk_pc" : "=s"(pc), "=s"(len) :: "scc");
      const unsigned off = (((b >> 3) * 4 + lw) * 5 + umin(l, 4)) * 128;
      touch(pc, off < len ? off : 0u, lds0 + L_TOUCH);
    }
    if (c.ct < 15) {
      unsigned n = 0;
      asm volatile("s_waitcnt vmcnt(9)\n\ts_barrier\n\t" P1_STAGE
                   "buffer_store_dwordx4 %[ps], %[p0], %[rs], 0 offen sc1\n\tbuffer_store_dwordx4 %[ps], %[p0], %[rs], 0 offen offset:1024 sc1\n\t"
                   "s_waitcnt vmcnt(10)\n\ts_barrier\n\t" P1_STAGE "s_waitcnt vmcnt(10)\n"
                   "1:\n\ts_barrier\n\t" P1_STAGE
                   "s_add_u32 %[n], %[n], 1\n\ts_cmp_lt_u32 %[n], 6\n\ts_waitcnt vmcnt(8)\n\ts_cbranch_scc1 1b"
                   : P1_STATE, [n] "+s"(n)
                   : P1_CONST, [p0] "v"(WS_QK + (640 + 8 * b + 2 * lw) * 1024 + lin), [rs] "s"(c.rs), [ps] "v"(u32x4{POISON, POISON, POISON, POISON}) : "memory", "scc");
      STAMP(5);
      glds_d(p.ws + WS_CS1 + (2 * lw * 1920 + c.ct * 128) * 8, lin, lds0 + L1_CG + lw * 2048);
      glds_d(p.ws + WS_CS1 + ((2 * lw + 1) * 1920 + c.ct * 128) * 8, lin, lds0 + L1_CG + lw * 2048 + 1024);
      glds_m(p.ws + WS_RS1 + (c.panel * 128 + 32 * lw) * 16, lin, lds0 + L1_RG + lw * 512, 0xFFFFFFFFull);
    }
    if (lw == 0 && l < 4) *(LAS unsigned*)(c.lds + L_CNT0 + l * 4) = 0u;
    wait_bar<11>();
    if (c.ct == 15) { publish<0>(c, L_CNT0 + 12, 8, WS_F0 + c.b * 8); __builtin_amdgcn_s_sleep(WOT); }
    {
      const unsigned pv = (l & 1) ? 0u : ONES2;
      if (l < 32) *(LAS u32x4*)(c.lds + VRING + (16 * lw + (l >> 1)) * 352 + 320 + (l & 1) * 16) = u32x4{pv, pv, pv, pv};
    }
    if (lw == 3) poll256(p.ws, WS_F0 + l * 8, c.tag);
    if (c.ct < 15) wait_bar<0>();
    STAMP(6);
    if (c.ct < 15) {
      LAS float* ra = (LAS float*)(c.lds + L1_FIN);
      const unsigned tag = (unsigned)c.tag;
      if (lw < 2) {
        u32x4 g = *(LAS u32x4*)(c.lds + L1_RG + t4 * 16);
#pragma unroll 1
        for (unsigned n = 0; (g[1] != tag || g[3] != tag) && n < SPIN_CAP; ++n) { FENCE(); g = ld128_d(c.rs, WS_RS1 + (c.panel * 128 + t4) * 16); }
        const float sc = c.ct < 5 ? QSCALE : 1.f;
        ra[t4] = __uint_as_float(g[0]) * sc; ra[128 + t4] = __uint_as_float(g[2]) * sc;
      } else {
        const int n0 = t4 - 128; float sum = 0.f; bool ok = true;
#pragma unroll
        for (int i = 0; i < 8; ++i) { const u32x2 g = *(LAS u32x2*)(c.lds + L1_CG + i * 1024 + n0 * 8); sum += __uint_as_float(g[0]); ok = ok && g[1] == tag; }
        if (__any(!ok)) {
          sum = 0.f;
#pragma unroll 1
          for (int i = 0; i < 8; ++i) {
            u32x2 g = {0u, ~tag};
#pragma unroll 1
            for (unsigned n = 0; g[1] != tag && n < SPIN_CAP; ++n) { FENCE(); g = ld64_d(c.rs, WS_CS1 + (i * 1920 + c.ct * 128 + n0) * 8); }
            sum += __uint_as_float(g[0]);
          }
        }
        ra[256 + n0] = sum;
      }
    }
    STAMP(7);
    if (c.ct < 15) LDS_BAR();
  }
  Park pk;
  const rsrc_t rw2 = __builtin_amdgcn_make_buffer_rsrc((void*)(p.w2 + cs * 160), 0, FF * D * 2 - cs * 160, 0x00020000);
  unsigned vo80[5];
  {
    const int h = c.h, e = h & 1, cb = 5 * (h >> 1) + 2 * e;
    unsigned vk[3], vv[3]; u64 mv[3], mt = 0;
#pragma unroll
    for (int i = 0; i < 3; ++i) {
      const int n = 64 * (3 * lw + i) + l, kg = n / 22, dd = n - 22 * kg;
      vk[i] = WS_QK + ((80 + cb + i) * 2048 + 64 + 64 * lw - 256) * 16 + lin - i * 1024;
      vv[i] = WS_V + (h * 256 + 4 - 32 + kg) * 320 + dd * 16 - i * 1024;
      mv[i] = __builtin_amdgcn_ballot_w64(dd < 20 && kg < 32);
      if (i == 0) mt = __builtin_amdgcn_ballot_w64(dd < 20 && kg < 28);
    }
    auto issue = [&](int s, bool withk, u64 m0, u64 m1, u64 m2) {
      unsigned t;
      if (withk) asm volatile("s_mov_b32 m0, %[d]\n\ts_lshl_b32 %[t], %[s], 12\n\t"
                              "buffer_load_dwordx4 %[v0], %[rs], %[t] offen sc1 lds\n\tbuffer_load_dwordx4 %[v1], %[rs], %[t] offen offset:1024 sc1 lds\n\tbuffer_load_dwordx4 %[v2], %[rs], %[t] offen offset:2048 sc1 lds"
                              : [t] "=&s"(t) : [d] "s"(lds0 + KRING + (s & 1) * 12288 + lw * 3072), [s] "s"(s + 1), [rs] "s"(c.rs), [v0] "v"(vk[0]), [v1] "v"(vk[1]), [v2] "v"(vk[2]) : "memory", "scc");
      asm volatile("s_mov_b32 m0, %[d]\n\ts_mul_i32 %[t], %[s], 10240\n\t"
                   "s_mov_b64 exec, %[m0]\n\tbuffer_load_dwordx4 %[v0], %[rs], %[t] offen sc1 lds\n\t"
                   "s_mov_b64 exec, %[m1]\n\ts_cbranch_execz 1f\n\tbuffer_load_dwordx4 %[v1], %[rs], %[t] offen offset:1024 sc1 lds\n\t"
                   "s_mov_b64 exec, %[m2]\n\ts_cbranch_execz 1f\n\tbuffer_load_dwordx4 %[v2], %[rs], %[t] offen offset:2048 sc1 lds\n1:\n\ts_mov_b64 exec, -1"
                   : [t] "=&s"(t) : [d] "s"(lds0 + VRING + (s & 1) * 11264 + lw * 3072), [s] "s"(s + 1), [rs] "s"(c.rs), [v0] "v"(vv[0]), [v1] "v"(vv[1]), [v2] "v"(vv[2]),
                     [m0] "s"(m0), [m1] "s"(m1), [m2] "s"(m2) : "memory");
    };
    auto bad = [&](int s, u64 m0, u64 m1, u64 m2) {
      u64 any;
      asm volatile("ds_read_b128 v[76:79], %[k]\n\tds_read_b128 v[80:83], %[k] offset:1024\n\tds_read_b128 v[84:87], %[k] offset:2048\n\t"
                   "ds_read_b128 v[88:91], %[v]\n\tds_read_b128 v[92:95], %[v] offset:1024\n\tds_read_b128 v[96:99], %[v] offset:2048\n\t"
                   WL(4) "v_max3_u32 v76, v76, v77, v78\n\tv_max3_u32 v80, v79, v80, v81\n\tv_max3_u32 v76, v76, v82, v83\n\t"
                   WL(3) "v_max3_u32 v80, v80, v84, v85\n\tv_max3_u32 v76, v76, v86, v87\n\t"
                   WL(0) "s_mov_b64 exec, %[m0]\n\tv_max3_u32 v80, v80, v88, v89\n\tv_max3_u32 v76, v76, v90, v91\n\t"
                   "s_mov_b64 exec, %[m1]\n\tv_max3_u32 v80, v80, v92, v93\n\tv_max3_u32 v76, v76, v94, v95\n\t"
                   "s_mov_b64 exec, %[m2]\n\tv_max3_u32 v80, v80, v96, v97\n\tv_max3_u32 v76, v76, v98, v99\n\t"
                   "s_mov_b64 exec, -1\n\tv_max_u32 v76, v76, v80\n\tv_cmp_eq_u32 %[any], -1, v76\n\ts_nop 0"
                   : [any] "=s"(any) : [k] "v"(lds0 + KRING + (s & 1) * 12288 + lw * 3072 + lin), [v] "v"(lds0 + VRING + (s & 1) * 11264 + lw * 3072 + lin), [m0] "s"(m0), [m1] "s"(m1), [m2] "s"(m2)
                   : "memory", CL8(76, 77, 78, 79, 80, 81, 82, 83), CL8(84, 85, 86, 87, 88, 89, 90, 91), CL8(92, 93, 94, 95, 96, 97, 98, 99));
      return any != 0;
    };
#pragma unroll
    for (int i = 0; i < 5; ++i) { const int n = 64 * i + l, rr = n / 10; vo80[i] = swap23(32 * lw + rr) * 1280 + (n - 10 * rr) * 16 - (i & 3) * 1024; }
    const rsrc_t rwo = __builtin_amdgcn_make_buffer_rsrc((void*)(p.wo + cs * 160), 0, D * D * 2 - cs * 160, 0x00020000);
    const unsigned xw = (b >> 3) * 4 + lw, vt60 = (l < 60 ? l : 0) * 256;
    u32x4 psn = {POISON, POISON, POISON, POISON};
    asm volatile("" : "+v"(psn));
    const int b1 = b < 192 ? b : 0, rg1 = b1 / 24, cg1 = b1 - 24 * rg1;
    unsigned w1o = (t4 >> 3) * 3072 + (l & 7) * 16, cs2o = WS_CS2 + (rg1 * 1536 + cg1 * 64 + 16 * lw + l) * 4;
    const char* bw = (l & 32 ? p.w2 : p.w1) + ((b * 4 + lw) * 15 + umin(l & 31, 14)) * 128;
    asm volatile("" : "+v"(w1o), "+v"(cs2o), "+v"(bw), "+v"(vo80[0]), "+v"(vo80[1]), "+v"(vo80[2]), "+v"(vo80[3]), "+v"(vo80[4]));
    LDS_BAR();
    __builtin_amdgcn_s_sleep(KDELAY);
#pragma unroll 1
    for (unsigned n = 0; n < SPIN_CAP; ++n) {
      issue(0, true, mv[0], mv[1], mv[2]);
      DRAIN();
      if (!bad(0, mv[0], mv[1], mv[2])) break;
      __builtin_amdgcn_s_sleep(RDELAY);
    }
    STAMP(9);
    LDS_BAR();
    __builtin_amdgcn_sched_barrier(0);
    __builtin_amdgcn_s_setprio(3);
#pragma unroll 1
    for (int s = 1; s < 5; ++s) {
      if (s > 1) LDS_BAR();
      STAMPV(20 + s);
      issue(s, true, mv[0], mv[1], mv[2]);
#pragma unroll
      for (int k = 0; k < 3; ++k) {
        const int q = 12 * lw + 3 * (s - 1) + k;
        asm volatile("buffer_store_dwordx4 %0, %1, %2, %3 offen sc1" :: "v"(psn), "v"(lin), "s"(c.rs),
                     "s"(q < 10 ? WS_O + rt * 81920 + cs * 10240 + q * 1024 : q < 20 ? WS_H + rt * 81920 + cs * 10240 + (q - 10) * 1024 : q < 44 ? WS_F + rt * 196608 + cs * 24576 + (q - 20) * 1024 : WS_RS2 + (rt * 8 + cs) * 512) : "memory");
      }
      if (s == 1) {
        wait_vm<3>();
#pragma unroll 1
        for (unsigned n = 0; bad(1, mv[0], mv[1], mv[2]) && n < SPIN_CAP; ++n) { issue(1, true, mv[0], mv[1], mv[2]); DRAIN(); }
        if (lw == 0) {
          poll256(p.ws, WS_F1 + l * 8, c.tag);
          if (b == 0 && l == 0) stflag(p.ws, WS_EPOCH, c.tag);
        }
      } else {
#pragma unroll 1
        for (int k = 2 * s - 4; k < (s < 4 ? 2 * s - 2 : 5); ++k)
        { const int kr = k + (rt & 3) < 5 ? k + (rt & 3) : k + (rt & 3) - 5; W80_DMA(rwo, lds0 + L_WO + kr * 20480 + lw * 5120, kr * 163840); }
        if (s == 4) { touch64(bw, lds0 + L_TOUCH); wait_vm<9>(); }
        wait_vm<13>();
        if (s == 3) {
          const char* src = p.w1 + (rg1 * 80 * 3072 + cg1 * 128);
          glds(src, w1o, lds0 + L_W1B + lw * 1024); glds(src + 32 * 3072, w1o, lds0 + L_W1B + 4096 + lw * 1024);
          if (lw < 2) glds(src + 64 * 3072, w1o, lds0 + L_W1B + 8192 + lw * 1024);
        }
      }
      STAMPV(9 + s);
    }
    u32x4 w1m[3] = {*(LAS u32x4*)(c.lds + L_W1B + t4 * 16), *(LAS u32x4*)(c.lds + L_W1B + 4096 + t4 * 16), {}};
    if (lw < 2) w1m[2] = *(LAS u32x4*)(c.lds + L_W1B + 8192 + t4 * 16);
    colsum_step1(c.lds + L_PART + 1024, l, lw, w1m);
    unsigned so, st;
#define PARK2(m, k0, c0, k1, c1) asm volatile(PKL(k0, c0) PKL(k1, c1) : PARK_REGS(m, pk), [so] "=&s"(so), [t] "=&s"(st) : W80_OPS(rw2), [r5] "s"(c.rot5 * 163840) : "memory", "scc")
    LDS_BAR(); STAMP(25);
    {
      const float cs2 = colsum_step2(c.lds + L_PART + 1024, l, lw);
      if (l < 16 && b < 192) st32_d(c.rs, cs2o, __float_as_uint(cs2));
    }
    issue(5, true, mv[0], mv[1], mv[2]);
    PARK2("=&", 0, 0, 1, 1);
    wait_bar<10>(); STAMP(26);
    publish<10>(c, L_CNTP, 4, WS_FP + b * 8);
    issue(6, true, mv[0], mv[1], mv[2]);
    PARK2("+", 2, 2, 3, 3);
    wait_bar<10>(); STAMP(27);
    issue(7, lw != 3, lw == 3 ? mt : mv[0], lw == 3 ? 0 : mv[1], mv[2]);
    PARK2("+", 4, 4, 5, 8);
    wait_bar<10>(); STAMP(28);
    if (lw == 0) poll256(p.ws, WS_FP + l * 8, c.tag);
    PARK2("+", 6, 9, 7, 10);
    if (lw == 1) {
      const int i = l & 7, k = l >> 3;
      touch64(k == 0 ? p.ws + (WS_O + c.qb * 327680 + i * 40960) : k == 1 ? p.ws + (WS_O + rt * 81920 + i * 10240) : k == 2 ? p.ws + (WS_H + rt * 81920 + i * 10240) : k == 3 ? p.ws + (WS_F + rt * 196608 + i * 24576)
              : k == 4 ? p.out + (rt * 81920 + i * 10240) : k == 5 ? p.x + (rt * 81920 + i * 10240) : p.ws + (WS_RS2 + rt * 4096 + i * 512), lds0 + L_TOUCH);
    }
    touch(p.w1 + xw * 15360, vt60, lds0 + L_TOUCH);
    touch(p.w1 + (xw * 15360 + 128), vt60, lds0 + L_TOUCH);
  }
  {
    unsigned vo[6];
#pragma unroll
    for (int i = 0; i < 3; ++i) {
      const int n = 64 * i + l, rr = n / 24, pos = n - 24 * rr;
      vo[i] = ((rr & 3) + 8 * (rr >> 2)) * 3072 + ((((pos >> 1) ^ ((rr >> 1) & 3)) << 1) | (pos & 1)) * 16 - i * 1024;
      vo[3 + i] = vo[i] + 4 * 3072;
    }
    const rsrc_t rw1 = __builtin_amdgcn_make_buffer_rsrc((void*)(p.w1 + cs * 384 + lw * 16 * 3072), 0, D * FF * 2 - cs * 384 - lw * 16 * 3072, 0x00020000);
    const unsigned dw = lds0 + lw * 6144, da = lds0 + L4_A + lw * 2048, va = WS_H + rt * 81920 + lw * 2048 + lin;
    const unsigned so = WS_RS2 + (rt * 8 + (l >> 4)) * 512 + (16 * lw + (l & 15)) * 8;
    unsigned t0, t1r; u32x2 s0, s1;
    LDS_BAR();
    STAMP(17);
    LDS_BAR();
    asm volatile(LW3(0, 0) LW3(0, 1) LW3(1, 0) LW3(1, 1) : [t0] "=&s"(t0) : W1_OPS : "memory");
    if (lw < 3) {
      float t = 0.f;
#pragma unroll
      for (int i = 0; i < 8; ++i) t += __uint_as_float(ld32_d(c.rs, WS_CS2 + (i * 1536 + cs * 192 + t4) * 4));
      *(LAS float*)(c.lds + L_CSUM + t4 * 4) = t;
    }
    asm volatile(PKL(8, 11) : PARK_REGS("+", pk), [so] "=&s"(t0), [t] "=&s"(t1r) : W80_OPS(rw2), [r5] "s"(c.rot5 * 163840) : "memory", "scc");
    LDS_BAR(); LDS_BAR(); LDS_BAR(); LDS_BAR();
    STAMP(18);
    LDS_BAR();
    asm volatile(
        LW3(2, 0) LW3(2, 1) LW3(3, 0) LW3(3, 1)
        "s_mov_b32 %[t1], 0\n"
        "1:\n\ts_sleep " STR(ADELAY) "\n\t" LA(0) LA(1) LA(2) LA(3)
        "s_waitcnt vmcnt(6)\n\t" CKR(0) CKT
        "s_cbranch_vccz 2f\n\ts_waitcnt vmcnt(0)\n\ts_add_u32 %[t1], %[t1], 1\n\ts_cmp_lt_u32 %[t1], 0x40000\n\ts_cbranch_scc1 1b\n"
        "2:\n\ts_barrier\n\t"
        IVL(1, 4, LW3(4, 0) LW3(4, 1), 63) "s_barrier\n\t"
        IVL(2, 8, LA(4) LW3(5, 0), 63) "s_barrier\n\t"
        IVL(3, 11, LA(5) LW3(5, 1), 63) "s_barrier\n\t"
        IVL(4, 8, LA(6) LW3(6, 0) LW3(6, 1), 63) "s_barrier\n\t"
        IVL(5, 11, LA(7) LW3(7, 0), 13) "s_barrier\n\t"
        IVL(6, 11, LA(8) LW3(7, 1) LW3(8, 0), 13) "s_barrier\n\t"
        IVL(7, 11, LA(9) LW3(8, 1), 8) "s_barrier\n\t"
        IVL(8, 11, "buffer_load_dwordx2 %[s0], %[so], %[rs], 0 offen sc1\n\tbuffer_load_dwordx2 %[s1], %[so], %[rs], 0 offen offset:2048 sc1\n\t" LW3(9, 0) LW3(9, 1), 8) "s_barrier\n\t"
        IVL(9, 11, "", 6)
        : [t0] "=&s"(t0), [t1] "=&s"(t1r), [s0] "=&v"(s0), [s1] "=&v"(s1)
        : W1_OPS, [rs] "s"(c.rs), [da] "s"(da), [va] "v"(va), [la] "v"(da + lin), [so] "v"(so) : "memory", "scc", "vcc", "v60", "v61", "v62", "v63", "v64", "v65", "v66", "v67");
    STAMP(29);
#pragma unroll 1
    for (unsigned n = 0; __any(umax(umax(s0[0], s0[1]), umax(s1[0], s1[1])) == POISON) && n < SPIN_CAP; ++n) { FENCE(); s0 = ld64_d(c.rs, so); s1 = ld64_d(c.rs, so + 2048); }
    float t1 = __uint_as_float(s0[0]) + __uint_as_float(s1[0]), t2 = __uint_as_float(s0[1]) + __uint_as_float(s1[1]);
    t1 = swap16_add(t1, t1); t2 = swap16_add(t2, t2); t1 = swap32_add(t1, t1); t2 = swap32_add(t2, t2);
    const float mean = t1 * (1.f / D), var = fmaxf(t2 * (1.f / D) - mean * mean, 0.f), a = __builtin_amdgcn_rsqf(var + LN_EPS);
    if (l < 32) *(LAS float*)(c.lds + L_RA + (l >> 4) * 256 + (16 * lw + (l & 15)) * 4) = l < 16 ? a : -mean * a;
    wait_bar<0>();
  }
  {
    const unsigned wa = lds0 + lw * 5120 + lin;
    {
      const int row = (5 + c.rot5) * 128 + ((b >> 6) * 4 + lw) * 24 + umin(l >> 1, 23);
      touch(p.w2 + cs * 160, (row < FF ? row : row - FF) * 1280 + (l & 1) * 156, lds0 + L_TOUCH);
    }
    LDS_BAR();
    STAMP(30);
    PUT(0, 0); PUT(1, 1); PUT(2, 2);
    LDS_BAR();
#pragma unroll 1
    for (int k = 5; k < 8; ++k) W80_DMA(rw2, lds0 + k * 20480 + lw * 5120, (k + c.rot5 < 12 ? k + c.rot5 : k + c.rot5 - 12) * 163840);
    PUT(3, 3); PUT(4, 4);
    STAMP(31);
    LDS_BAR();
    wait_bar<0>(); PUT(5, 0); PUT(6, 1); PUT(7, 2); PUT(8, 3);
    LDS_BAR(); LDS_BAR(); LDS_BAR();
  }
}

__global__ void __launch_bounds__(512, 2) block_kernel(Params p) {
  extern __shared__ __attribute__((aligned(16))) char smem[];
  Ctx c;
  c.st_lo = c.st_hi = 0;
  STAMP(3 + 20);
  c.p = p; c.lds = (lptr)smem; c.lds0 = (unsigned)(size_t)c.lds; c.l = threadIdx.x & 63; c.w = rfl(threadIdx.x >> 6); c.b = blockIdx.x;
  const int xcd = c.b & 7, j = c.b >> 3;
  const int t1 = 30 * (xcd & 1) + j;
  c.panel = 4 * (xcd >> 1) + (t1 & 3); c.ct = j < 30 ? t1 >> 2 : 15;
  c.h = c.rt = 4 * xcd + (j >> 3); c.qb = c.cs = j & 7;
  c.xr0 = 512 * (xcd >> 1) + 8 * (32 * (xcd & 1) + j); c.wi = j < 30 ? (xcd >> 1) * 30 + j : 0;
  c.rot5 = (3 * c.cs) >> 1; asm volatile("" : "+s"(c.rot5));
  if (__builtin_expect(c.w >= 4, 1)) loader_waves(c);
  else { STAMP(0); compute_waves(c); }
}

extern "C" void kernel_launch(void* const* d_in, const int* in_sizes, int n_in, void* d_out, int out_size, void* d_ws, size_t ws_size, hipStream_t stream) {
  Params p;
  p.x = (const char*)d_in[0]; p.wqkv = (const char*)d_in[1]; p.wo = (const char*)d_in[2]; p.w1 = (const char*)d_in[4]; p.w2 = (const char*)d_in[5];
  p.out = (char*)d_out; p.ws = (char*)d_ws;
  (void)hipFuncSetAttribute((const void*)block_kernel, hipFuncAttributeMaxDynamicSharedMemorySize, LDS_BYTES);
  block_kernel<<<256, 512, LDS_BYTES, stream>>>(p);
}
```

```cpp
#include <hip/hip_runtime.h>
#include <hip/hip_bf16.h>

#define DI __device__ __forceinline__
#define LAS __attribute__((address_space(3)))
#define GAS __attribute__((address_space(1)))
typedef unsigned long long u64;
typedef __attribute__((ext_vector_type(8))) short s16x8;
typedef __attribute__((ext_vector_type(4))) short s16x4;
typedef __attribute__((ext_vector_type(8))) __bf16 bf16x8;
typedef __attribute__((ext_vector_type(16))) __bf16 bf16x16;
typedef __attribute__((ext_vector_type(2))) __bf16 bf16x2;
typedef __attribute__((ext_vector_type(2))) float f32x2;
typedef __attribute__((ext_vector_type(4))) float f32x4;
typedef __attribute__((ext_vector_type(8))) float f32x8;
typedef __attribute__((ext_vector_type(16))) float f32x16;
typedef __attribute__((ext_vector_type(32))) float f32x32;
typedef __attribute__((ext_vector_type(4))) unsigned u32x4;
typedef __attribute__((ext_vector_type(2))) unsigned u32x2;
typedef LAS char* lptr;
typedef __amdgpu_buffer_rsrc_t rsrc_t;

#define V_BY_HEAD
#define SDELAY 10
#define CPD 30
#define WOT 60
#define QDELAY 20
#define KDELAY 36
#define PDELAY 2
#define ADELAY 6
#define RDELAY 6
#define PSLEEP 2
constexpr int S = 2048, D = 640, FF = 1536;
constexpr float LN_EPS = 1e-5f;
constexpr float QSCALE = 0.22360679774997896f * 1.4426950408889634f;

constexpr unsigned WS_EPOCH = 0, WS_F0 = 4096, WS_F1 = 6144, WS_FP = 8192;
constexpr unsigned WS_RS1 = 65536;
constexpr unsigned WS_CS1 = WS_RS1 + 2048 * 16;
constexpr unsigned WS_CS2 = WS_CS1 + 8 * 1920 * 8;
constexpr unsigned WS_QK = WS_CS2 + 8 * 1536 * 4;
constexpr unsigned WS_V = WS_QK + 160 * 2048 * 16;
constexpr unsigned WS_RS2 = WS_V + 256 * 640 * 16;
constexpr unsigned WS_O = WS_RS2 + 32 * 8 * 512;
constexpr unsigned WS_H = WS_O + S * D * 2;
constexpr unsigned WS_F = WS_H + S * D * 2;
constexpr unsigned WS_END = WS_F + S * FF * 2;
constexpr int LDS_BYTES = 163840;
constexpr unsigned SPIN_CAP = 1u << 18;
constexpr unsigned POISON = 0xFFFFFFFFu;
constexpr int L1_CG = 131072, L1_RG = L1_CG + 8192, L1_FIN = L1_RG + 2048;
constexpr int L1_XS = 131072, L1_WS = L1_XS + 10240;
constexpr int KRING = 0, VRING = 24576, L_W1B = 47104, L_WO = 57344;
constexpr int L4_A = 122880, L_EX = 98304;
constexpr int L_CNT0 = 159744, L_CNT1 = L_CNT0 + 4, L_CNTP = L_CNT0 + 8, L_CSUM = L_CNT0 + 256, L_RA = L_CSUM + 768, L_TOUCH = L_RA + 512;

#define TS(i) ""
#define TS_CLOB
#define TS_PUT(i, k)
#define STAMP(k)
#define STAMPV(k)
struct Params { const char* x; const char* wqkv; const char* wo; const char* w1; const char* w2; char* out; char* ws; };
struct Ctx { Params p; lptr lds; unsigned lds0; rsrc_t rs; u64 tag; int l, w, b, panel, ct, h, qb, rt, cs, rot5, xr0, wi; mutable unsigned st_lo, st_hi; };

DI unsigned rfl(unsigned v) { return __builtin_amdgcn_readfirstlane(v); }
DI unsigned cvtpk(float lo, float hi) { f32x2 v = {lo, hi}; return __builtin_bit_cast(unsigned, __builtin_convertvector(v, bf16x2)); }
typedef __attribute__((ext_vector_type(2))) short s16x2;
DI unsigned relu2(unsigned v) { return __builtin_bit_cast(unsigned, __builtin_elementwise_max(__builtin_bit_cast(s16x2, v), s16x2{0, 0})); }
DI float bflo(unsigned v) { return __uint_as_float(v << 16); }
DI float bfhi(unsigned v) { return __uint_as_float(v & 0xffff0000u); }
DI void glds(const char* sbase, unsigned voff, unsigned lds) {
  asm volatile("s_mov_b32 m0, %2\n\ts_nop 0\n\tglobal_load_lds_dwordx4 %0, %1" :: "v"(voff), "s"(sbase), "s"(lds) : "memory");
}
DI void glds_d(const char* sbase, unsigned voff, unsigned lds) {
  asm volatile("s_mov_b32 m0, %2\n\ts_nop 0\n\tglobal_load_lds_dwordx4 %0, %1 sc1" :: "v"(voff), "s"(sbase), "s"(lds) : "memory");
}
DI void glds_m(const char* sbase, unsigned voff, unsigned lds, u64 mask) {
  asm volatile("s_mov_b32 m0, %2\n\ts_mov_b64 exec, %3\n\tglobal_load_lds_dwordx4 %0, %1 sc1\n\ts_mov_b64 exec, -1" :: "v"(voff), "s"(sbase), "s"(lds), "s"(mask) : "memory");
}
DI void touch(const char* sbase, unsigned voff, unsigned lds) {
  asm volatile("s_mov_b32 m0, %2\n\ts_nop 0\n\tglobal_load_lds_dword %0, %1" :: "v"(voff), "s"(sbase), "s"(lds) : "memory");
}
DI void touch64(const char* a, unsigned lds) { asm volatile("s_mov_b32 m0, %1\n\ts_nop 0\n\tglobal_load_lds_dword %0, off" :: "v"(a), "s"(lds) : "memory"); }
template <int N> DI void wait_vm() { asm volatile("s_waitcnt vmcnt(%0)" :: "i"(N) : "memory"); }
template <int N> DI void wait_bar() { asm volatile("s_waitcnt vmcnt(%0) lgkmcnt(0)\n\ts_barrier" :: "i"(N) : "memory"); }
#define LDS_BAR() asm volatile("s_waitcnt lgkmcnt(0)\n\ts_barrier" ::: "memory")
#define DRAIN() asm volatile("s_waitcnt vmcnt(0)" ::: "memory")
#define STR_(x) #x
#define STR(x) STR_(x)
#define VR(b, n) "v[" STR(b) ":" STR(b) "+" STR(n) "-1]"
#define WL(n) "s_waitcnt lgkmcnt(" #n ")\n\t"
#define CL8(a, b, c, d, e, f, g, h) "v" #a, "v" #b, "v" #c, "v" #d, "v" #e, "v" #f, "v" #g, "v" #h
DI s16x4 ldtr(lptr p) { return __builtin_amdgcn_ds_read_tr16_b64_v4i16((LAS s16x4*)p); }
DI s16x8 ldtr2(lptr p, int d) { return __builtin_shufflevector(ldtr(p), ldtr(p + d), 0, 1, 2, 3, 4, 5, 6, 7); }
DI f32x16 mfma32(s16x8 a, s16x8 b, f32x16 c) { return __builtin_amdgcn_mfma_f32_32x32x16_bf16(__builtin_bit_cast(bf16x8, a), __builtin_bit_cast(bf16x8, b), c, 0, 0, 0); }
DI f32x4 mfma16(s16x8 a, s16x8 b, f32x4 c) { return __builtin_amdgcn_mfma_f32_16x16x32_bf16(__builtin_bit_cast(bf16x8, a), __builtin_bit_cast(bf16x8, b), c, 0, 0, 0); }
DI float dot2(unsigned a, unsigned b, float c) { return __builtin_amdgcn_fdot2_f32_bf16(__builtin_bit_cast(bf16x2, a), __builtin_bit_cast(bf16x2, b), c, false); }
constexpr unsigned ONES2 = 0x3f803f80u;
DI void st128_d(rsrc_t rs, unsigned off, u32x4 v) { __builtin_amdgcn_raw_buffer_store_b128(v, rs, off, 0, 16); }
DI void st64_d(rsrc_t rs, unsigned off, u32x2 v) { __builtin_amdgcn_raw_buffer_store_b64(v, rs, off, 0, 16); }
DI void st32_d(rsrc_t rs, unsigned off, unsigned v) { __builtin_amdgcn_raw_buffer_store_b32(v, rs, off, 0, 16); }
DI u32x4 ld128_d(rsrc_t rs, unsigned off) { return __builtin_amdgcn_raw_buffer_load_b128(rs, off, 0, 16); }
DI u32x2 ld64_d(rsrc_t rs, unsigned off) { return __builtin_amdgcn_raw_buffer_load_b64(rs, off, 0, 16); }
DI unsigned ld32_d(rsrc_t rs, unsigned off) { return __builtin_amdgcn_raw_buffer_load_b32(rs, off, 0, 16); }
DI u64 ldflag(const char* ws, unsigned off) { return __hip_atomic_load((GAS u64*)(ws + off), __ATOMIC_RELAXED, __HIP_MEMORY_SCOPE_AGENT); }
DI void stflag(char* ws, unsigned off, u64 v) { __hip_atomic_store((GAS u64*)(ws + off), v, __ATOMIC_RELAXED, __HIP_MEMORY_SCOPE_AGENT); }
DI void read_tag(Ctx& c) {
  const char* ws = c.p.ws; asm volatile("" : "+s"(ws));
  c.rs = __builtin_amdgcn_make_buffer_rsrc(c.p.ws, 0, WS_END, 0x00020000);
  c.tag = *(const __attribute__((address_space(4))) u64*)(ws + WS_EPOCH) + 1;
}
DI void poll256(const char* ws, unsigned off, u64 tag) {
#pragma unroll 1
  for (unsigned n = 0; n < SPIN_CAP; ++n) {
    const u64 a = ldflag(ws, off), b = ldflag(ws, off + 512), c = ldflag(ws, off + 1024), d = ldflag(ws, off + 1536);
    if (__all(a == tag && b == tag && c == tag && d == tag)) break;
    __builtin_amdgcn_s_sleep(4);
  }
}
template <int YOUNGER> DI void publish(const Ctx& c, int cnt_off, unsigned n, unsigned flag_off) {
  wait_vm<YOUNGER>();
  if (c.l == 0) {
    const unsigned old = __hip_atomic_fetch_add((LAS unsigned*)(c.lds + cnt_off), 1u, __ATOMIC_RELAXED, __HIP_MEMORY_SCOPE_WORKGROUP);
    if (old == n - 1) stflag(c.p.ws, flag_off, c.tag);
  }
}
template <int CTRL> DI float dpp_add(float v) { return v + __int_as_float(__builtin_amdgcn_update_dpp(0, __float_as_int(v), CTRL, 0xf, 0xf, true)); }
DI float swap16_add(float a, float b) { const auto t = __builtin_amdgcn_permlane16_swap(__float_as_uint(a), __float_as_uint(b), false, false); return __uint_as_float(t[0]) + __uint_as_float(t[1]); }
DI float swap32_add(float a, float b) { const auto t = __builtin_amdgcn_permlane32_swap(__float_as_uint(a), __float_as_uint(b), false, false); return __uint_as_float(t[0]) + __uint_as_float(t[1]); }
DI float swap32_max(float a) { const auto t = __builtin_amdgcn_permlane32_swap(__float_as_uint(a), __float_as_uint(a), false, false); return fmaxf(__uint_as_float(t[0]), __uint_as_float(t[1])); }
DI float upper_half(float a) { const auto t = __builtin_amdgcn_permlane32_swap(__float_as_uint(a), __float_as_uint(a), false, false); return __uint_as_float(t[1]); }
DI u32x4 swap32_pair(unsigned a0, unsigned a1, unsigned b0, unsigned b1) {
  const auto s0 = __builtin_amdgcn_permlane32_swap(a0, b0, false, false), s1 = __builtin_amdgcn_permlane32_swap(a1, b1, false, false);
  return u32x4{s0[0], s1[0], s0[1], s1[1]};
}
DI u32x4 swap16_pair(unsigned a0, unsigned a1, unsigned b0, unsigned b1) {
  const auto s0 = __builtin_amdgcn_permlane16_swap(a0, b0, false, false), s1 = __builtin_amdgcn_permlane16_swap(a1, b1, false, false);
  return u32x4{s0[0], s1[0], s0[1], s1[1]};
}
DI float colsum(lptr blk, int l, int w4) {
  lptr src = blk + (8 * (l >> 4) + ((l & 15) >> 2)) * 128 + w4 * 32 + (l & 3) * 8;
  const short one = 0x3f80, last = l < 32 ? one : 0;
  const s16x8 b0 = ldtr2(src, 512), b1 = ldtr2(src + 4096, 512), b2 = ldtr2(src + (l < 32 ? 8192 : 0), 512);
  f32x4 acc = mfma16(s16x8{one, one, one, one, one, one, one, one}, b0, f32x4{});
  acc = mfma16(s16x8{one, one, one, one, one, one, one, one}, b1, acc);
  return mfma16(s16x8{last, last, last, last, last, last, last, last}, b2, acc)[0];
}
DI unsigned np(unsigned v) { return v < POISON - 1 ? v : POISON - 1; }
DI u32x4 np(u32x4 v) { return u32x4{np(v[0]), np(v[1]), np(v[2]), np(v[3])}; }
DI unsigned umax(unsigned a, unsigned b) { return a > b ? a : b; }
DI unsigned umin(unsigned a, unsigned b) { return a < b ? a : b; }
DI unsigned umax4(u32x4 v) { return umax(umax(v[0], v[1]), umax(v[2], v[3])); }
#define FENCE() asm volatile("" ::: "memory")
DI f32x16 lo16(const f32x32& s) { return __builtin_shufflevector(s, s, 0, 1, 2, 3, 4, 5, 6, 7, 8, 9, 10, 11, 12, 13, 14, 15); }
DI f32x16 hi16(const f32x32& s) { return __builtin_shufflevector(s, s, 16, 17, 18, 19, 20, 21, 22, 23, 24, 25, 26, 27, 28, 29, 30, 31); }
DI int swap23(int k) { return (k & ~12) | ((k & 4) << 1) | ((k & 8) >> 1); }

DI void row_stats(const Ctx& c, const u32x4 (&xv)[3], int xrow) {
  const int r = c.l & 31;
  float s1 = 0.f, s2 = 0.f;
#pragma unroll
  for (int i = 0; i < 3; ++i)
#pragma unroll
    for (int k = 0; k < 4; ++k) { const unsigned v = (i < 2 || r < 16) ? xv[i][k] : 0u; s1 = dot2(v, ONES2, s1); s2 = dot2(v, v, s2); }
  s1 = dpp_add<0x128>(dpp_add<0x141>(dpp_add<0x4E>(dpp_add<0xB1>(s1)))); s2 = dpp_add<0x128>(dpp_add<0x141>(dpp_add<0x4E>(dpp_add<0xB1>(s2))));
  s1 = swap16_add(s1, s1); s2 = swap16_add(s2, s2);
  const float mean = s1 * (1.f / D), var = fmaxf(s2 * (1.f / D) - mean * mean, 0.f), a = __builtin_amdgcn_rsqf(var + LN_EPS);
  const unsigned tag = (unsigned)c.tag;
  if (r == 0) st128_d(c.rs, WS_RS1 + xrow * 16, u32x4{__float_as_uint(a), tag, __float_as_uint(-mean * a), tag});
}
DI void spare_poison(const Ctx& c) {
  u32x4 psn = {POISON, POISON, POISON, POISON};
  asm volatile("" : "+v"(psn));
  const unsigned q0 = ((c.b - 240) * 8 + c.w) * 17;
#pragma unroll 1
  for (unsigned q = q0; q < umin(q0 + 17, 2064); ++q) {
    const unsigned v = q - 720;
    asm volatile("buffer_store_dwordx4 %0, %1, %2, %3 offen sc1" :: "v"(psn), "v"(c.l * 16), "s"(c.rs),
                 "s"(q < 720 ? WS_QK + (80 + q / 9) * 32768 + q % 9 * 1024 : q < 1424 ? WS_V + v / 22 * 81920 + v % 22 * 1024 : WS_QK + (q - 1424) * 1024) : "memory");
  }
}
#define Q_X 96
#define Q_Y 128
#define QMK(S, m, n) "v_mfma_f32_16x16x32_bf16 " VR(32 + m * 16 + n * 4, 4) ", " VR(S + 16 + n * 4, 4) ", " VR(S + m * 4, 4) ", " VR(32 + m * 16 + n * 4, 4) "\n\t"
#define QMV(S, m, n) "v_mfma_f32_16x16x32_bf16 " VR(32 + m * 16 + n * 4, 4) ", " VR(S + m * 4, 4) ", " VR(S + 16 + n * 4, 4) ", " VR(32 + m * 16 + n * 4, 4) "\n\t"
#define QRA(S, m, ks) "ds_read_b128 " VR(S + m * 4, 4) ", v[160+" STR(ks) "] offset:" STR(m * 2048) "\n\t"
#define QRB(S, n, h, ks) "ds_read_b64_tr_b16 " VR(S + 16 + n * 4 + h * 2, 2) ", v[162+" STR(n) "] offset:" STR(ks * 8192 + h * 1024) "\n\t"
#define QAD(i) "v_add_u32 v[160+" #i "], %[dl], v[160+" #i "]\n\t"
#define P1_LOOP(MM) \
  QRA(Q_Y, 0, 0) QRB(Q_Y, 0, 0, 0) QRB(Q_Y, 0, 1, 0) QRB(Q_Y, 1, 0, 0) QRB(Q_Y, 1, 1, 0) QRA(Q_Y, 1, 0) \
  QRB(Q_Y, 2, 0, 0) QRB(Q_Y, 2, 1, 0) QRB(Q_Y, 3, 0, 0) QRB(Q_Y, 3, 1, 0) QRA(Q_Y, 2, 0) QRA(Q_Y, 3, 0) \
  "s_branch 2f\n" \
  "1:\n\t" \
  MM(Q_X, 1, 0) QRA(Q_Y, 0, 0) MM(Q_X, 1, 1) QRB(Q_Y, 0, 0, 0) MM(Q_X, 0, 2) QRB(Q_Y, 0, 1, 0) MM(Q_X, 1, 2) QRB(Q_Y, 1, 0, 0) \
  MM(Q_X, 0, 3) QRB(Q_Y, 1, 1, 0) MM(Q_X, 1, 3) QRA(Q_Y, 1, 0) MM(Q_X, 2, 0) QRB(Q_Y, 2, 0, 0) MM(Q_X, 2, 1) QRB(Q_Y, 2, 1, 0) \
  MM(Q_X, 2, 2) QRB(Q_Y, 3, 0, 0) MM(Q_X, 2, 3) QRB(Q_Y, 3, 1, 0) MM(Q_X, 3, 0) QRA(Q_Y, 2, 0) MM(Q_X, 3, 1) QRA(Q_Y, 3, 0) MM(Q_X, 3, 2) MM(Q_X, 3, 3) \
  "2:\n\t" \
  WL(9) MM(Q_Y, 0, 0) QRA(Q_X, 0, 1) WL(8) MM(Q_Y, 0, 1) QRB(Q_X, 0, 0, 1) WL(8) MM(Q_Y, 1, 0) QRB(Q_X, 0, 1, 1) MM(Q_Y, 1, 1) QRB(Q_X, 1, 0, 1) \
  WL(8) MM(Q_Y, 0, 2) QRB(Q_X, 1, 1, 1) MM(Q_Y, 1, 2) QRA(Q_X, 1, 1) WL(8) MM(Q_Y, 0, 3) QRB(Q_X, 2, 0, 1) MM(Q_Y, 1, 3) QRB(Q_X, 2, 1, 1) \
  WL(9) MM(Q_Y, 2, 0) QRB(Q_X, 3, 0, 1) MM(Q_Y, 2, 1) QRB(Q_X, 3, 1, 1) MM(Q_Y, 2, 2) QRA(Q_X, 2, 1) MM(Q_Y, 2, 3) QRA(Q_X, 3, 1) \
  WL(12) MM(Q_Y, 3, 0) "s_add_u32 %[st], %[st], 1\n\ts_and_b32 %[dl], %[st], 3\n\ts_cselect_b32 %[dl], 32768, %[wr]\n\t" \
  MM(Q_Y, 3, 1) QAD(0) QAD(1) MM(Q_Y, 3, 2) QAD(2) QAD(3) MM(Q_Y, 3, 3) QAD(4) QAD(5) \
  WL(7) MM(Q_X, 0, 0) "s_cmp_eq_u32 %[st], 12\n\t" WL(0) MM(Q_X, 0, 1) "s_cbranch_scc1 3f\n\t" \
  "s_barrier\n\ts_cmp_lg_u32 %[st], %[fk]\n\ts_cbranch_scc1 1b\n\t" \
  "s_mov_b64 exec, 1\n\tbuffer_store_dwordx2 v[168:169], v166, %[rs], 0 offen sc1\n\ts_mov_b64 exec, -1\n\ts_branch 1b\n" \
  "3:\n\t" \
  MM(Q_X, 1, 0) MM(Q_X, 1, 1) MM(Q_X, 0, 2) MM(Q_X, 1, 2) MM(Q_X, 0, 3) MM(Q_X, 1, 3) MM(Q_X, 2, 0) MM(Q_X, 2, 1) \
  MM(Q_X, 2, 2) MM(Q_X, 2, 3) MM(Q_X, 3, 0) MM(Q_X, 3, 1) MM(Q_X, 3, 2) MM(Q_X, 3, 3) \
  "s_nop 7"
#define P1_OPERANDS \
  : "+{v[32:63]}"(ac0), "+{v[64:95]}"(ac1), "+{v160}"(aoff[0]), "+{v161}"(aoff[1]), "+{v162}"(boff[0]), "+{v163}"(boff[1]), "+{v164}"(boff[2]), "+{v165}"(boff[3]), \
    [st] "+s"(st), [dl] "=&s"(dl) \
  : [wr] "s"(-98304), [fk] "s"(w == 0 ? 5 : 0), [rs] "s"(c.rs), "{v166}"(WS_F0 + c.b * 8), "{v[168:169]}"(c.tag) \
  : "memory", "scc", CL8(96, 97, 98, 99, 100, 101, 102, 103), CL8(104, 105, 106, 107, 108, 109, 110, 111), CL8(112, 113, 114, 115, 116, 117, 118, 119), CL8(120, 121, 122, 123, 124, 125, 126, 127), \
    CL8(128, 129, 130, 131, 132, 133, 134, 135), CL8(136, 137, 138, 139, 140, 141, 142, 143), CL8(144, 145, 146, 147, 148, 149, 150, 151), CL8(152, 153, 154, 155, 156, 157, 158, 159)
template <bool VT>
DI void p1_compute(const Ctx& c) {
  const int l = c.l, w = c.w, wm = w >> 1, wn = w & 1, t = l & 15, g = l >> 4, q = t >> 2, pp = l & 3;
  unsigned aoff[2], boff[4];
#pragma unroll
  for (int i = 0; i < 2; ++i) aoff[i] = c.lds0 + 65536 + (wm * 64 + t) * 128 + (((4 * i + g) ^ (t >> 1)) << 4);
#pragma unroll
  for (int n = 0; n < 4; ++n) boff[n] = c.lds0 + 65536 + 16384 + (8 * g + q) * 256 + (((wn * 8 + 2 * n + (pp >> 1)) ^ ((q << 2) | ((g & 1) << 1))) << 4) + 8 * (pp & 1);
  f32x32 ac0 = {}, ac1 = {};
  unsigned st = 2, dl;
  if (VT) asm volatile(P1_LOOP(QMV) P1_OPERANDS); else asm volatile(P1_LOOP(QMK) P1_OPERANDS);
  LAS float* ra = (LAS float*)(c.lds + L1_FIN); LAS float* rb = ra + 128; LAS float* cs = ra + 256;
  unsigned base[2];
#pragma unroll
  for (int i = 0; i < 2; ++i) {
    const int f = (c.ct - 10) * 128 + wn * 64 + (2 * i + (g & 1)) * 16 + t, h = f / 20, d = f - 20 * h, dd = (h & 1) ? (d < 4 ? d + 16 : d - 4) : d;
    base[i] = VT ? WS_V + (h * 256 + c.panel * 16 + wm * 8 + (g >> 1)) * 320 + dd * 16 : WS_QK + ((c.ct * 16 + wn * 8 + (g >> 1)) * 2048 + c.panel * 128 + wm * 64 + (2 * i + (g & 1)) * 16 + t) * 16;
  }
  asm volatile("" : "+v"(base[0]), "+v"(base[1]));
  LDS_BAR();
  STAMP(3);
  float s0[4], s1[4]; f32x4 u4[4], v4[4];
#pragma unroll
  for (int i = 0; i < 4; ++i) {
    s0[i] = VT ? cs[wn * 64 + 16 * i + t] : ra[wm * 64 + 16 * i + t]; s1[i] = VT ? 0.f : rb[wm * 64 + 16 * i + t];
    u4[i] = *(LAS f32x4*)(VT ? ra + wm * 64 + 16 * i + 4 * g : cs + wn * 64 + 16 * i + 4 * g);
    v4[i] = VT ? *(LAS f32x4*)(rb + wm * 64 + 16 * i + 4 * g) : f32x4{};
  }
  __builtin_amdgcn_sched_barrier(0);
#pragma unroll
  for (int o = 0; o < 4; ++o)
#pragma unroll
    for (int ip = 0; ip < 2; ++ip) {
      unsigned pk[2][2];
#pragma unroll
      for (int j = 0; j < 2; ++j) {
        const int i2 = 2 * ip + j, m = VT ? o : i2, n = VT ? i2 : o; float y[4];
#pragma unroll
        for (int i = 0; i < 4; ++i) {
          const float a = m < 2 ? ac0[m * 16 + n * 4 + i] : ac1[(m - 2) * 16 + n * 4 + i];
          y[i] = VT ? fmaf(a, u4[o][i], v4[o][i] * s0[i2]) : fmaf(a, s0[i2], s1[i2] * u4[o][i]);
        }
        pk[j][0] = cvtpk(y[0], y[1]); pk[j][1] = cvtpk(y[2], y[3]);
      }
      st128_d(c.rs, base[ip] + o * (VT ? 2 * 320 : 2 * 32768), np(swap16_pair(pk[0][0], pk[0][1], pk[1][0], pk[1][1])));
    }
}

#define EX(b, i) "v_exp_f32 v[" STR(b) "+" STR(i) "], v[" STR(b) "+" STR(i) "]\n\t"
#define EX3(b, i) EX(b, i) EX(b, i + 1) EX(b, i + 2)
#define EX4(b, i) EX3(b, i) EX(b, i + 3)
#define R_SA 32
#define R_SB 64
#define R_OA 96
#define R_OB 112
#define R_NA 128
#define R_NB 144
#define R_QA 160
#define R_QB 164
#define R_QR 168
#define R_KR 170
#define R_K 172
#define R_VA 176
#define R_VB 184
#define R_IDX 192
#define R_KAD 193
#define R_KRAD 194
#define R_VAD 195
#define PV(o, vreg, sreg) "v_smfmac_f32_32x32x32_bf16 " VR(o, 16) ", " VR(vreg, 4) ", " VR(sreg, 8) ", v" STR(R_IDX) "\n\t"
#define QK16(dst, q, nm) "v_mfma_f32_32x32x16_bf16 " VR(dst, 16) ", " VR(R_K, 4) ", " VR(q, 4) ", " VR(nm, 16) "\n\t"
#define STEP(C, N, VC, VN, AB, i, MID, KRLOAD) \
  QK16(N, R_QA, R_NA) \
  MID \
  EX4(C, 0) \
  PV(R_OB, VN + 4, N + 24) \
  "ds_read_b128 " VR(VN, 4) ", v" STR(R_VAD) " offset:" STR(i * 1408) "\n\t" \
  "ds_read_b128 " VR(VN + 4, 4) ", v" STR(R_VAD) " offset:" STR(i * 1408 + 704) "\n\t" \
  EX3(C, 4) \
  QK16(N + 16, R_QB, R_NB) \
  "ds_read_b128 " VR(R_K, 4) ", v" STR(R_KAD) " offset:" STR((i / 2) * 3072 + (i & 1) * 512) "\n\t" \
  EX4(C, 7) \
  PV(R_OA, VC, C) \
  "s_waitcnt lgkmcnt(3)\n\t" \
  EX4(C, 11) \
  "v_mfma_f32_32x32x4_2b_bf16 " VR(N, 32) ", " VR(R_KR, 2) ", " VR(R_QR, 2) ", " VR(N, 32) " cbsz:1 abid:" STR(AB) "\n\t" \
  EX4(C, 15) EX4(C, 19) \
  PV(R_OA, VC + 4, C + 8) \
  "s_waitcnt lgkmcnt(0)\n\t" \
  KRLOAD \
  EX4(C, 23) \
  PV(R_OB, VC, C + 16) \
  EX4(C, 27) EX(C, 31)
#define KRL(i) "ds_read_b64 " VR(R_KR, 2) ", v" STR(R_KRAD) " offset:" STR((i / 2) * 3072) "\n\t"
#define STAGE_TOP \
  "s_barrier\n\t" \
  "v_add_u32 v" STR(R_KAD) ", %[dk], v" STR(R_KAD) "\n\tv_add_u32 v" STR(R_KRAD) ", %[dk], v" STR(R_KRAD) "\n\tv_add_u32 v" STR(R_VAD) ", %[dv], v" STR(R_VAD) "\n\t" \
  "s_sub_u32 %[dk], 0, %[dk]\n\ts_sub_u32 %[dv], 0, %[dv]\n\t"

DI f32x32 cat16(const f32x16& a, const f32x16& b) {
  return __builtin_shufflevector(a, b, 0, 1, 2, 3, 4, 5, 6, 7, 8, 9, 10, 11, 12, 13, 14, 15, 16, 17, 18, 19, 20, 21, 22, 23, 24, 25, 26, 27, 28, 29, 30, 31);
}
DI float max16(const f32x16& s) { float m = s[0];
#pragma unroll
  for (int i = 1; i < 16; ++i) m = fmaxf(m, s[i]);
  return m; }
DI f32x16 splat16(float v) { f32x16 s;
#pragma unroll
  for (int i = 0; i < 16; ++i) s[i] = v;
  return s; }
DI bool out_of_range(float sum) { return !(sum > 0.f && sum < 1e30f); }

DI void p2_compute(const Ctx& c, u32x2 (&res)[5]) {
  const int l = c.l, w = c.w, r = l & 31, hh = l >> 5, h = c.h;
  const int e = h & 1, cb = 5 * (h >> 1) + 2 * e, q0 = c.qb * 256 + w * 64;
  unsigned kad = c.lds0 + KRING + 12288 + (e + hh) * 1024 + r * 16, krad = c.lds0 + KRING + 12288 + (e ? 0 : 2048) + l * 16 + 8 * e;
  unsigned vad = c.lds0 + VRING + 11264 + hh * 352 + (r < 21 ? r : 21) * 16;
  LDS_BAR();
  __builtin_amdgcn_s_sleep(QDELAY);
  const unsigned ko = WS_QK + ((80 + cb + e + hh) * 2048 + r) * 16, kro = WS_QK + ((80 + cb + (e ? 0 : 2)) * 2048 + r) * 16 + 8 * e;
  const unsigned vg = WS_V + (h * 256 + hh) * 320 + (r < 20 ? r : 0) * 16, pad = r == 20 ? ONES2 : 0u;
  s16x8 qa, qb, kf; s16x4 qr, kr; u32x4 k0, t0, t1;
#pragma unroll 1
  for (unsigned n = 0; n < SPIN_CAP; ++n) {
    FENCE();
    const u32x4 ta = ld128_d(c.rs, WS_QK + ((cb + e + hh) * 2048 + q0 + r) * 16), tb = ld128_d(c.rs, WS_QK + ((cb + e + hh) * 2048 + q0 + 32 + r) * 16);
    const u32x2 tr = ld64_d(c.rs, WS_QK + ((e ? cb : cb + 2) * 2048 + q0 + l) * 16 + 8 * e), tk = ld64_d(c.rs, kro + hh * 512);
    k0 = ld128_d(c.rs, ko); const u32x4 k1 = ld128_d(c.rs, ko + 512);
    t0 = ld128_d(c.rs, vg); t1 = ld128_d(c.rs, vg + 640);
    if (n == 0) publish<8>(c, L_CNT1, 4, WS_F1 + c.b * 8);
    qa = __builtin_bit_cast(s16x8, ta); qb = __builtin_bit_cast(s16x8, tb); qr = __builtin_bit_cast(s16x4, tr);
    kf = __builtin_bit_cast(s16x8, k1); kr = __builtin_bit_cast(s16x4, tk);
    if (!__any(umax(umax(umax(umax4(ta), umax4(tb)), umax(umax4(k0), umax4(k1))), umax(umax(umax4(t0), umax4(t1)), umax(umax(tr[0], tr[1]), umax(tk[0], tk[1])))) == POISON)) break;
    __builtin_amdgcn_s_sleep(RDELAY);
  }
  STAMP(5);
  f32x32 sa = {}, sb = {};
  sa = __builtin_amdgcn_mfma_f32_32x32x4bf16_1k(kr, qr, sa, 1, 0, 0);
  f32x16 s0 = mfma32(__builtin_bit_cast(s16x8, k0), qa, lo16(sa)), s1 = mfma32(__builtin_bit_cast(s16x8, k0), qb, hi16(sa));
  f32x8 va = {}, vb = {};
#pragma unroll
  for (int i = 0; i < 4; ++i) { va[i] = __uint_as_float(r < 20 ? t0[i] : pad); va[4 + i] = __uint_as_float(r < 20 ? t1[i] : pad); }
  f32x16 na = splat16(-swap32_max(max16(s0))), nb = splat16(-swap32_max(max16(s1))), oa = {}, ob = {};
  sa = cat16(s0 + na, s1 + nb);
  unsigned idx = 0xDDDDDDDDu, cnt = 8, dk = -12288, dv = -11264;
  {
    const char* xg = c.p.x + (size_t)((c.rt * 64 + 32 * (w & 1) + 16 * (w >> 1) + (l & 15)) * 1280 + c.cs * 160 + (l >> 4) * 8);
#pragma unroll
    for (int nt = 0; nt < 5; ++nt) res[nt] = *(const GAS u32x2*)(xg + nt * 32);
  }
  STAMP(7);
  asm volatile(
      "s_waitcnt lgkmcnt(0)\n\ts_nop 4\n"
      "1:\n\t"
      STEP(R_SA, R_SB, R_VA, R_VB, 1, 0, STAGE_TOP, KRL(0))
      STEP(R_SB, R_SA, R_VB, R_VA, 0, 1, "", "")
      STEP(R_SA, R_SB, R_VA, R_VB, 1, 2, "", KRL(2))
      STEP(R_SB, R_SA, R_VB, R_VA, 0, 3, "", "")
      STEP(R_SA, R_SB, R_VA, R_VB, 1, 4, "", KRL(4))
      STEP(R_SB, R_SA, R_VB, R_VA, 0, 5, "", "")
      STEP(R_SA, R_SB, R_VA, R_VB, 1, 6, "", KRL(6))
      STEP(R_SB, R_SA, R_VB, R_VA, 0, 7, "", "")
      "s_sub_u32 %[cnt], %[cnt], 1\n\t"
      "s_cmp_lg_u32 %[cnt], 0\n\t"
      "s_cbranch_scc1 1b\n\t"
      "s_waitcnt lgkmcnt(0)\n\t"
      "s_barrier\n\t"
      PV(R_OB, R_VB + 4, R_SB + 24)
      "s_nop 15\n\t"
      : "+{v[32:63]}"(sa), "+{v[64:95]}"(sb), "+{v[96:111]}"(oa), "+{v[112:127]}"(ob), "+{v[170:171]}"(kr), "+{v[172:175]}"(kf), "+{v[176:183]}"(va), "+{v[184:191]}"(vb),
        "+{v193}"(kad), "+{v194}"(krad), "+{v195}"(vad), [cnt] "+s"(cnt), [dk] "+s"(dk), [dv] "+s"(dv)
      : "{v[128:143]}"(na), "{v[144:159]}"(nb), "{v[160:163]}"(qa), "{v[164:167]}"(qb), "{v[168:169]}"(qr), "{v192}"(idx)
      : "memory", "scc");
  STAMP(8);
  float la = upper_half(oa[8]), lb = upper_half(ob[8]);
  if (__any(out_of_range(la) || out_of_range(lb))) {
    float ma = -INFINITY, mb = -INFINITY;
#pragma unroll 1
    for (int pass = 0; pass < 2; ++pass) {
      na = splat16(pass ? -ma : 0.f); nb = splat16(pass ? -mb : 0.f); oa = splat16(0.f); ob = splat16(0.f);
#pragma unroll 1
      for (int t = 0; t < 64; ++t) {
        const s16x8 k16 = __builtin_bit_cast(s16x8, ld128_d(c.rs, ko + t * 512));
        f32x32 s = cat16(mfma32(k16, qa, na), mfma32(k16, qb, nb));
        s = __builtin_amdgcn_mfma_f32_32x32x4bf16_1k(__builtin_bit_cast(s16x4, ld64_d(c.rs, kro + t * 512)), qr, s, 0, 0, 0);
        if (pass == 0) { ma = fmaxf(ma, max16(lo16(s))); mb = fmaxf(mb, max16(hi16(s))); }
        else {
#pragma unroll
          for (int g = 0; g < 2; ++g) {
            u32x4 vf = ld128_d(c.rs, vg + (4 * t + 2 * g) * 320);
            if (r >= 20) vf = u32x4{pad, pad, pad, pad};
            f32x8 pa, pb;
#pragma unroll
            for (int i = 0; i < 8; ++i) { pa[i] = __builtin_amdgcn_exp2f(s[8 * g + i]); pb[i] = __builtin_amdgcn_exp2f(s[16 + 8 * g + i]); }
            oa = __builtin_amdgcn_smfmac_f32_32x32x32_bf16(__builtin_bit_cast(bf16x8, vf), __builtin_bit_cast(bf16x16, pa), oa, 0xDDDD, 0, 0);
            ob = __builtin_amdgcn_smfmac_f32_32x32x32_bf16(__builtin_bit_cast(bf16x8, vf), __builtin_bit_cast(bf16x16, pb), ob, 0xDDDD, 0, 0);
          }
        }
      }
      if (pass == 0) { ma = swap32_max(ma); mb = swap32_max(mb); }
    }
    la = upper_half(oa[8]); lb = upper_half(ob[8]);
  }
#pragma unroll
  for (int t = 0; t < 2; ++t) {
    const f32x16& o = t ? ob : oa;
    const float rl = __builtin_amdgcn_rcpf(t ? lb : la);
    unsigned pk[6];
#pragma unroll
    for (int i = 0; i < 6; ++i) pk[i] = cvtpk(o[2 * i] * rl, o[2 * i + 1] * rl);
    const unsigned og = WS_O + (q0 >> 6) * 81920 + (32 * t + r) * 16;
    st128_d(c.rs, og + (cb + e + hh) * 1024, np(swap32_pair(pk[0], pk[1], pk[2], pk[3])));
    if (hh == 0) st64_d(c.rs, og + (e ? cb : cb + 2) * 1024 + 8 * e, u32x2{np(pk[4]), np(pk[5])});
  }
}

#define G_ACC 32
#define G_B0 72
#define G_B1 92
#define G_A 112
#define G_VB 176
#define G_VBO 177
#define G_VA0 178
#define G_VA1 180
#define G_CHK 182
#define GMM(m, nt, B, j) "v_mfma_f32_16x16x32_bf16 " VR(G_ACC + m * 20 + nt * 4, 4) ", " VR(B + nt * 4, 4) ", " VR(G_A + j * 8 + m * 4, 4) ", " VR(G_ACC + m * 20 + nt * 4, 4) "\n\t"
#define GRB(B, nt, ks) "ds_read_b64_tr_b16 " VR(B + nt * 4, 2) ", v" STR(G_VB) " offset:" STR(ks * 5120 + nt * 32) "\n\t" \
                       "ds_read_b64_tr_b16 " VR(B + nt * 4 + 2, 2) ", v" STR(G_VB) " offset:" STR(ks * 5120 + nt * 32 + 1280) "\n\t"
#define GCK(j, i) "v_max3_u32 v" STR(G_CHK) ", v[" STR(G_A + j * 8 + i * 2) "], v[" STR(G_A + j * 8 + i * 2 + 1) "], v" STR(G_CHK) "\n\t"
#define GLD1(j, m, va) "buffer_load_dwordx4 " VR(G_A + (j) * 8 + m * 4, 4) ", v[" STR(va + m) "], %[rs], %[so] offen sc1\n\t"
#define GAD0 "s_add_u32 %[so], %[so], 16384\n\t"
#define GAD1 "s_cmp_eq_u32 %[so], %[sw]\n\t"
#define GAD2 "s_cselect_b32 %[so], 0, %[so]\n\t"
#define GLD2(j) GLD1(j, 0, G_VA0) GLD1(j, 1, G_VA0) GLD1(j + 1, 0, G_VA1) GLD1(j + 1, 1, G_VA1)
#define GLD2A(j) GLD2(j) GAD0 GAD1 GAD2
#define GNX0 "s_add_u32 %[sl], %[sl], 20480\n\t"
#define GNX1 "s_cmp_eq_u32 %[sl], %[lw]\n\t"
#define GNX2 "s_cselect_b32 %[sl], %[lr], %[sl]\n\t"
#define GADR "v_add_u32 v" STR(G_VB) ", %[sl], v" STR(G_VBO) "\n\t"
#define GBODY(j, BC, BN, ks, VM, W0, PRE, S0, S1, S2, L0, L1) \
  "s_waitcnt vmcnt(" STR(VM) ") lgkmcnt(" STR(W0) ")\n\t" PRE \
  GMM(0, 0, BC, j) GRB(BN, 0, ks) GMM(1, 0, BC, j) GRB(BN, 1, ks) GMM(0, 1, BC, j) GRB(BN, 2, ks) GMM(1, 1, BC, j) GRB(BN, 3, ks) \
  GMM(0, 2, BC, j) GRB(BN, 4, ks) GMM(1, 2, BC, j) S0 WL(10) GMM(0, 3, BC, j) GCK(j, 0) S1 GMM(1, 3, BC, j) GCK(j, 1) S2 \
  GMM(0, 4, BC, j) GCK(j, 2) L0 GMM(1, 4, BC, j) GCK(j, 3) L1
#define GEVEN(j) GBODY(j, G_B0, G_B1, 1, 14, 4, "", GAD0, GAD1, GAD2, GLD1(j, 0, G_VA0), GLD1(j, 1, G_VA0))
#define GODD(j) GBODY(j, G_B1, G_B0, 0, 14, 4, GADR, GNX0, GNX1, GNX2, GLD1(j, 0, G_VA1), GLD1(j, 1, G_VA1))
#define GODDB(j) GBODY(j, G_B1, G_B0, 0, 14, 0, "s_barrier\n\t" GADR, GNX0, GNX1, GNX2, GLD1(j, 0, G_VA1), GLD1(j, 1, G_VA1))
#define TEVEN(j) GBODY(j, G_B0, G_B1, 1, 14 - 2 * j, 4, "", "", "", "", "", "")
#define TODD(j) GBODY(j, G_B1, G_B0, 0, 14 - 2 * j, 4, GADR, GNX0, GNX1, GNX2, "", "")

DI void g80_compute(const Ctx& c, int last, u32x2 (&res)[5]) {
  int l = c.l; asm volatile("" : "+v"(l));
  const int w = c.w, wm = w & 1, wk = w >> 1, g = l >> 4, q = (l & 15) >> 2, pp = l & 3;
  const unsigned tile = last ? WS_F + c.rt * 196608 : WS_O + c.rt * 81920;
  __builtin_amdgcn_s_barrier();
  const int row = 32 * wm + 16 * wk + (l & 15);
  f32x32 acc0 = {}; f32x8 acc1;
#pragma unroll
  for (int i = 0; i < 20; ++i) { const float v = (i & 1) ? bfhi(res[i >> 2][(i >> 1) & 1]) : bflo(res[i >> 2][(i >> 1) & 1]); if (i < 12) acc0[20 + i] = v; else acc1[i - 12] = v; }
  asm volatile("" : "+{v[32:63]}"(acc0), "+{v[64:71]}"(acc1));
  __builtin_amdgcn_s_sleep(PDELAY);
  {
    const int hd = l & 31, e = hd & 1, cb = 5 * (hd >> 1) + 2 * e;
    const unsigned po = tile + (last ? ((hd >> 2) * 24 + (hd & 3) * 6 + 5) * 1024 + 63 * 16 : (e ? cb : cb + 2) * 1024 + 63 * 16 + 8 * e);
#pragma unroll 1
    for (unsigned n = 0; n < SPIN_CAP; ++n) { FENCE(); if (!__any(ld32_d(c.rs, po) == POISON)) break; __builtin_amdgcn_s_sleep(PSLEEP); }
  }
  if (last) STAMP(16); else STAMP(10);
  const unsigned va0 = tile + (8 * wk + g) * 1024 + row * 16;
  const unsigned r0 = last ? c.rot5 : (5 * c.cs) >> 3;
  unsigned chk = 0, so = r0 * 16384, sl = last ? 0 : L_WO + r0 * 20480, cnt = last ? 2 : 1;
  asm volatile(
      "s_cmp_eq_u32 %[cnt], 1\n\ts_cbranch_scc0 4f\n\t"
      GLD2A(6)
      "4:\n\t"
      GLD2A(0) GLD2A(2) TS(0)
      "s_barrier\n\t" TS(1)
      GADR
      GRB(G_B0, 0, 0) GRB(G_B0, 1, 0) GRB(G_B0, 2, 0) GRB(G_B0, 3, 0) GRB(G_B0, 4, 0)
      GNX0 GNX1 GNX2
      GLD2(4)
      "s_cmp_eq_u32 %[cnt], 1\n\ts_cbranch_scc1 2f\n\t"
      GAD0 GAD1 GAD2 GLD2(6)
      "1:\n\t"
      GEVEN(0) GODD(1) GEVEN(2) GODD(3) GEVEN(4) GODD(5)
      "2:\n\t"
      GEVEN(6)
      GODDB(7)
      "s_sub_u32 %[cnt], %[cnt], 1\n\ts_cmp_lg_u32 %[cnt], 0\n\ts_cbranch_scc1 1b\n\t"
      TEVEN(0) TODD(1) TEVEN(2) TODD(3) TEVEN(4) TODD(5) TEVEN(6)
      "s_waitcnt vmcnt(0) lgkmcnt(0)\n\ts_barrier\n\t"
      GMM(0, 0, G_B1, 7) GMM(1, 0, G_B1, 7) GCK(7, 0) GMM(0, 1, G_B1, 7) GCK(7, 1) GMM(1, 1, G_B1, 7) GCK(7, 2) GMM(0, 2, G_B1, 7) GCK(7, 3)
      GMM(1, 2, G_B1, 7) GMM(0, 3, G_B1, 7) GMM(1, 3, G_B1, 7) GMM(0, 4, G_B1, 7) GMM(1, 4, G_B1, 7)
      "s_nop 15\n\t" TS(5)
      : "+{v[32:63]}"(acc0), "+{v[64:71]}"(acc1), "+{v182}"(chk), [so] "+s"(so), [sl] "+s"(sl), [cnt] "+s"(cnt)
      : "{v177}"(c.lds0 + (64 * wk + 16 * (g >> 1) + 4 * (g & 1) + q) * 160 + pp * 8), "{v178}"(va0 ^ 256), "{v179}"(va0), "{v180}"((va0 ^ 256) + 4096), "{v181}"(va0 + 4096), [rs] "s"(c.rs),
        [sw] "s"(last ? 196608 : 81920), [lw] "s"(last ? 163840 : L_WO + 102400), [lr] "s"(last ? 0 : L_WO)
      : "memory", "scc", "v176", CL8(160, 161, 162, 163, 164, 165, 166, 167), CL8(168, 169, 170, 171, 172, 173, 174, 175),
        "v72", "v73", "v74", "v75", "v76", "v77", "v78", "v79", "v80", "v81", "v82", "v83", "v84", "v85", "v86", "v87", "v88", "v89", "v90", "v91", "v92", "v93", "v94", "v95",
        "v96", "v97", "v98", "v99", "v100", "v101", "v102", "v103", "v104", "v105", "v106", "v107", "v108", "v109", "v110", "v111", "v112", "v113", "v114", "v115", "v116", "v117", "v118", "v119",
        "v120", "v121", "v122", "v123", "v124", "v125", "v126", "v127", "v128", "v129", "v130", "v131", "v132", "v133", "v134", "v135", "v136", "v137", "v138", "v139", "v140", "v141", "v142", "v143",
        "v144", "v145", "v146", "v147", "v148", "v149", "v150", "v151", "v152", "v153", "v154", "v155", "v156", "v157", "v158", "v159" TS_CLOB);
  if (last) { TS_PUT(0, 25); TS_PUT(1, 26); TS_PUT(2, 27); TS_PUT(3, 28); TS_PUT(4, 29); TS_PUT(5, 30); } else { TS_PUT(0, 19); TS_PUT(1, 20); TS_PUT(4, 21); TS_PUT(5, 22); }
  f32x4 acc[2][5];
#pragma unroll
  for (int i = 0; i < 40; ++i) acc[i / 20][(i % 20) / 4][i % 4] = i < 32 ? acc0[i] : acc1[i - 32];
  if (__any(chk == POISON)) {
    const char* wg = (last ? c.p.w2 : c.p.wo) + c.cs * 160 + (l & 15) * 2;
#pragma unroll
    for (int nt = 0; nt < 5; ++nt) { acc[0][nt] = f32x4{}; acc[1][nt] = f32x4{bflo(res[nt][0]), bfhi(res[nt][0]), bflo(res[nt][1]), bfhi(res[nt][1])}; }
#pragma unroll 1
    for (int st = 0; st < (last ? 24 : 10); ++st) {
      u32x4 a0, a1;
#pragma unroll 1
      for (unsigned n = 0; n < SPIN_CAP; ++n) {
        FENCE();
        a0 = ld128_d(c.rs, (va0 ^ 256) + (st >> 1) * 16384 + (st & 1) * 4096); a1 = ld128_d(c.rs, va0 + (st >> 1) * 16384 + (st & 1) * 4096);
        if (!__any(umax(umax4(a0), umax4(a1)) == POISON)) break;
      }
      const char* wr = wg + (size_t)((st >> 1) * 128 + 64 * wk + (st & 1) * 32 + 8 * g) * 1280;
#pragma unroll
      for (int nt = 0; nt < 5; ++nt) {
        s16x8 bf;
#pragma unroll
        for (int j = 0; j < 8; ++j) bf[j] = *(const GAS short*)(wr + j * 1280 + nt * 32);
        acc[0][nt] = mfma16(bf, __builtin_bit_cast(s16x8, a0), acc[0][nt]); acc[1][nt] = mfma16(bf, __builtin_bit_cast(s16x8, a1), acc[1][nt]);
      }
    }
  }
  lptr ex = c.lds + L_EX + l * 16;
#pragma unroll
  for (int nt = 0; nt < 5; ++nt) *(LAS f32x4*)(ex + w * 5120 + nt * 1024) = acc[0][nt];
  const rsrc_t ro = __builtin_amdgcn_make_buffer_rsrc(last ? c.p.out : c.p.ws + WS_H, 0, S * D * 2, 0x00020000);
  const unsigned ob = c.rt * 81920 + (last ? c.cs * 160 + row * 1280 : c.cs * 10240 + row * 16), oc = last ? 16 : 1024;
  unsigned so3[3];
#pragma unroll
  for (int i = 0; i < 3; ++i) so3[i] = ob + (4 * i + (i < 2 ? 2 * (g & 1) : 0) + (g >> 1)) * oc;
  asm volatile("" : "+v"(so3[0]), "+v"(so3[1]), "+v"(so3[2]));
  LDS_BAR();
  if (last) STAMP(17); else STAMP(11);
  f32x4 o4[5];
#pragma unroll
  for (int nt = 0; nt < 5; ++nt) o4[nt] = *(LAS f32x4*)(ex + (w ^ 2) * 5120 + nt * 1024);
  __builtin_amdgcn_sched_barrier(0);
#pragma unroll
  for (int nt = 0; nt < 5; ++nt) { const f32x4 u = acc[1][nt] + o4[nt]; res[nt] = u32x2{np(cvtpk(u[0], u[1])), np(cvtpk(u[2], u[3]))}; }
  u32x4 pc[3];
#pragma unroll
  for (int i = 0; i < 3; ++i) { const int n1 = i < 2 ? 2 * i + 1 : 4; pc[i] = swap16_pair(res[2 * i][0], res[2 * i][1], res[n1][0], res[n1][1]); }
  if (last) {
#pragma unroll
    for (int i = 0; i < 3; ++i) __builtin_amdgcn_raw_buffer_store_b128(pc[i], ro, so3[i], 0, 0);
  } else {
#pragma unroll
    for (int i = 0; i < 3; ++i) st128_d(ro, so3[i], pc[i]);
    STAMP(12);
    __builtin_amdgcn_s_barrier();
    float s1 = 0.f, s2 = 0.f;
#pragma unroll
    for (int nt = 0; nt < 5; ++nt) { s1 = dot2(res[nt][0], ONES2, dot2(res[nt][1], ONES2, s1)); s2 = dot2(res[nt][0], res[nt][0], dot2(res[nt][1], res[nt][1], s2)); }
    s1 = swap16_add(s1, s1); s2 = swap16_add(s2, s2); s1 = swap32_add(s1, s1); s2 = swap32_add(s2, s2);
    if (l < 16) st64_d(c.rs, WS_RS2 + (c.rt * 8 + c.cs) * 512 + row * 8, u32x2{np(__float_as_uint(s1)), np(__float_as_uint(s2))});
  }
}

#define F_X 80
#define F_Y 108
#define FMM(S, nt, m) "v_mfma_f32_16x16x32_bf16 " VR(32 + nt * 16 + m * 4, 4) ", " VR(S + nt * 4, 4) ", " VR(S + 12 + m * 4, 4) ", " VR(32 + nt * 16 + m * 4, 4) "\n\t"
#define FRA(S, m, ks) "ds_read_b128 " VR(S + 12 + m * 4, 4) ", v140 offset:" STR(ks * 4096 + m * 256) "\n\t"
#define FRB(S, nt, h, ks) "ds_read_b64_tr_b16 " VR(S + nt * 4 + h * 2, 2) ", v[141+" STR(nt) "] offset:" STR(ks * 12288 + h * 3072) "\n\t"
#define FRF(m) "ds_read2st64_b32 v[144+2*" #m ":145+2*" #m "], v152 offset0:0 offset1:1\n\tv_add_u32 v152, 64, v152\n\t"
DI void p4_compute(const Ctx& c) {
  int l = c.l; asm volatile("" : "+v"(l));
  const int w = c.w, g = l >> 4, q = (l & 15) >> 2, pp = l & 3;
  unsigned boff[3];
#pragma unroll
  for (int nt = 0; nt < 3; ++nt) boff[nt] = c.lds0 + (16 * (g >> 1) + 4 * (g & 1) + q) * 384 + (((3 * w + nt) ^ (2 * (g & 1) + (q >> 1))) * 32) + pp * 8;
  f32x32 ac0 = {}; f32x16 ac1 = {}; f32x8 ab;
  unsigned ch = 0, sa, sb = 0, rap = c.lds0 + L_RA + (l & 15) * 4;
  f32x4 c4[3];
#pragma unroll
  for (int nt = 0; nt < 3; ++nt) c4[nt] = *(LAS f32x4*)(c.lds + L_CSUM + (w * 48 + nt * 16 + 4 * g) * 4);
  asm volatile("" : "+v"(c4[0]), "+v"(c4[1]), "+v"(c4[2]));
  asm volatile(
      "v_mov_b32 v140, v136\n\tv_mov_b32 v141, v137\n\tv_mov_b32 v142, v138\n\tv_mov_b32 v143, v139\n\t"
      "s_barrier\n\t" TS(0)
      FRB(F_Y, 0, 0, 0) FRB(F_Y, 0, 1, 0) FRA(F_Y, 0, 0) FRA(F_Y, 1, 0) FRA(F_Y, 2, 0) FRA(F_Y, 3, 0) FRB(F_Y, 1, 0, 0) FRB(F_Y, 1, 1, 0) FRB(F_Y, 2, 0, 0) FRB(F_Y, 2, 1, 0)
      "s_branch 2f\n"
      "1:\n\t"
      FMM(F_X, 0, 2) FRB(F_Y, 0, 0, 0) FMM(F_X, 0, 3) FRB(F_Y, 0, 1, 0) FMM(F_X, 1, 0) FRA(F_Y, 0, 0) FMM(F_X, 1, 1) FRA(F_Y, 1, 0) FMM(F_X, 1, 2) FRA(F_Y, 2, 0)
      FMM(F_X, 1, 3) FRA(F_Y, 3, 0) FMM(F_X, 2, 0) FRB(F_Y, 1, 0, 0) FMM(F_X, 2, 1) FRB(F_Y, 1, 1, 0) FMM(F_X, 2, 2) FRB(F_Y, 2, 0, 0) FMM(F_X, 2, 3) FRB(F_Y, 2, 1, 0)
      "2:\n\t"
      WL(7) FMM(F_Y, 0, 0) FRB(F_X, 0, 0, 1) WL(7) FMM(F_Y, 0, 1) FRB(F_X, 0, 1, 1) WL(7) FMM(F_Y, 0, 2) FRA(F_X, 0, 1) WL(7) FMM(F_Y, 0, 3) FRA(F_X, 1, 1)
      WL(6) FMM(F_Y, 1, 0) FRA(F_X, 2, 1) "s_add_u32 %[ch], %[ch], 1\n\t" FMM(F_Y, 1, 1) FRA(F_X, 3, 1) "s_add_u32 %[sb], %[sb], 24576\n\t"
      FMM(F_Y, 1, 2) FRB(F_X, 1, 0, 1) "s_cmp_eq_u32 %[ch], 5\n\t" FMM(F_Y, 1, 3) FRB(F_X, 1, 1, 1)
      WL(8) FMM(F_Y, 2, 0) FRB(F_X, 2, 0, 1) "s_cselect_b32 %[sb], 0, %[sb]\n\t" FMM(F_Y, 2, 1) FRB(F_X, 2, 1, 1) "s_and_b32 %[sa], %[ch], 3\n\t"
      FMM(F_Y, 2, 2) "s_lshl_b32 %[sa], %[sa], 13\n\tv_add_u32 v141, %[sb], v137\n\t" FMM(F_Y, 2, 3) "v_add_u32 v142, %[sb], v138\n\t"
      WL(7) FMM(F_X, 0, 0) "v_add_u32 v143, %[sb], v139\n\t" WL(6) FMM(F_X, 0, 1) "v_add_u32 v140, %[sa], v136\n\ts_cmp_eq_u32 %[ch], 10\n\t"
      WL(0) "s_barrier\n\t"
      "s_cbranch_scc0 1b\n\t"
      FMM(F_X, 0, 2) FRF(0) FMM(F_X, 0, 3) FRF(1) FMM(F_X, 1, 0) FRF(2) FMM(F_X, 1, 1) FRF(3) FMM(F_X, 1, 2) FMM(F_X, 1, 3) FMM(F_X, 2, 0) FMM(F_X, 2, 1) FMM(F_X, 2, 2) FMM(F_X, 2, 3)
      WL(0) "s_nop 7"
      : "+{v[32:63]}"(ac0), "+{v[64:79]}"(ac1), "={v[144:151]}"(ab), "+{v152}"(rap), [ch] "+s"(ch), [sa] "=&s"(sa), [sb] "+s"(sb)
      : "{v136}"(c.lds0 + L4_A + g * 1024 + (l & 15) * 16), "{v137}"(boff[0]), "{v138}"(boff[1]), "{v139}"(boff[2])
      : "memory", "scc", "v140", "v141", "v142", "v143", CL8(80, 81, 82, 83, 84, 85, 86, 87), CL8(88, 89, 90, 91, 92, 93, 94, 95), CL8(96, 97, 98, 99, 100, 101, 102, 103), CL8(104, 105, 106, 107, 108, 109, 110, 111),
        CL8(112, 113, 114, 115, 116, 117, 118, 119), CL8(120, 121, 122, 123, 124, 125, 126, 127), CL8(128, 129, 130, 131, 132, 133, 134, 135) TS_CLOB);
  TS_PUT(0, 13);
  f32x4 acc[4][3];
#pragma unroll
  for (int i = 0; i < 48; ++i) acc[(i >> 2) & 3][i >> 4][i & 3] = i < 32 ? ac0[i] : ac1[i - 32];
  STAMP(14);
#pragma unroll
  for (int nt = 0; nt < 3; ++nt)
#pragma unroll
    for (int mp = 0; mp < 2; ++mp) {
      unsigned pk[2][2];
#pragma unroll
      for (int j = 0; j < 2; ++j) {
        const int m = 2 * mp + j; float y[4];
#pragma unroll
        for (int i = 0; i < 4; ++i) y[i] = fmaf(acc[m][nt][i], ab[2 * m], ab[2 * m + 1] * c4[nt][i]);
        pk[j][0] = relu2(cvtpk(y[0], y[1])); pk[j][1] = relu2(cvtpk(y[2], y[3]));
      }
      st128_d(c.rs, WS_F + c.rt * 196608 + (c.cs * 24 + w * 6 + nt * 2 + (g >> 1)) * 1024 + (32 * mp + (g & 1) * 16 + (l & 15)) * 16, swap16_pair(pk[0][0], pk[0][1], pk[1][0], pk[1][1]));
    }
}

DI void compute_waves(Ctx& c) {
  const Params& p = c.p;
  read_tag(c);
  const int l = c.l, w = c.w, b = c.b, r = l & 31, hh = l >> 5;
  if (c.ct == 15) { __builtin_amdgcn_s_sleep(CPD); spare_poison(c); }
  {
    const int xrow = c.xr0 + 2 * w + hh, rg = c.wi & 7, cg = 15 * (b & 1) + (c.wi >> 3);
    LDS_BAR();
    lptr xs = c.lds + L1_XS + (2 * w + hh) * 1280 + r * 16;
    const u32x4 xv[3] = {*(LAS u32x4*)xs, *(LAS u32x4*)(xs + 512), *(LAS u32x4*)(xs + 1024)};
    const float v = colsum(c.lds + L1_WS, l, w);
    row_stats(c, xv, xrow);
    if (l < 16 && c.ct < 15) st64_d(c.rs, WS_CS1 + (rg * 1920 + cg * 64 + 16 * w + l) * 8, u32x2{__float_as_uint(v), (unsigned)c.tag});
    LDS_BAR();
    STAMP(1);
    if (c.ct < 10) p1_compute<false>(c);
    else if (c.ct < 15) p1_compute<true>(c);
    else publish<0>(c, L_CNT0 + 12, 8, WS_F0 + c.b * 8);
  }
  STAMP(4);
  u32x2 res[5];
  p2_compute(c, res);
  STAMP(9);
  int last = 0;
#pragma unroll 1
  for (;;) {
    asm volatile("" : "+s"(last));
    g80_compute(c, last, res);
    if (last) break;
    p4_compute(c);
    STAMP(15);
    asm volatile(".Lblk_end1:");
    last = 1;
  }
  STAMP(18);
  asm volatile(".Lblk_end2:");
}

struct Park { f32x32 a, b, c, d, e; f32x16 f; f32x4 g; };
#define PARK_REGS(m, pk) m "{v[76:107]}"(pk.a), m "{v[108:139]}"(pk.b), m "{v[140:171]}"(pk.c), m "{v[172:203]}"(pk.d), m "{v[204:235]}"(pk.e), m "{v[236:251]}"(pk.f), m "{v[252:255]}"(pk.g)
#define PKL1(k, j, o) "buffer_load_dwordx4 " VR(76 + k * 20 + j * 4, 4) ", %[v" #j "], %[rs], %[so] offen offset:" STR(o) "\n\t"
#define PKL(k, ch) "s_add_u32 %[so], %[r5], " STR(ch * 163840 - 1966080) "\n\ts_cselect_b32 %[so], %[so], " STR(ch * 163840) "\n\ts_cselect_b32 %[t], 0, %[r5]\n\ts_add_u32 %[so], %[so], %[t]\n\t" PKL1(k, 0, 0) PKL1(k, 1, 1024) PKL1(k, 2, 2048) PKL1(k, 3, 3072) PKL1(k, 4, 0)
#define W80_OPS(r) [rs] "s"(r), [v0] "v"(vo80[0]), [v1] "v"(vo80[1]), [v2] "v"(vo80[2]), [v3] "v"(vo80[3]), [v4] "v"(vo80[4])
#define W80_DMA(r, dst, off) asm volatile("s_mov_b32 m0, %[d]\n\ts_nop 0\n\t" \
    "buffer_load_dwordx4 %[v0], %[rs], %[so] offen lds\n\tbuffer_load_dwordx4 %[v1], %[rs], %[so] offen offset:1024 lds\n\t" \
    "buffer_load_dwordx4 %[v2], %[rs], %[so] offen offset:2048 lds\n\tbuffer_load_dwordx4 %[v3], %[rs], %[so] offen offset:3072 lds\n\t" \
    "s_add_u32 m0, %[d], 4096\n\ts_nop 0\n\tbuffer_load_dwordx4 %[v4], %[rs], %[so] offen lds" :: [d] "s"(dst), [so] "s"(off), W80_OPS(r) : "memory", "scc")
#define PKS1(k, j) "ds_write_b128 %[va], " VR(76 + k * 20 + j * 4, 4) " offset:" STR(j * 1024) "\n\t"
#define PUT(k, slot) asm volatile(PKS1(k, 0) PKS1(k, 1) PKS1(k, 2) PKS1(k, 3) PKS1(k, 4) :: [va] "v"(wa + slot * 20480), PARK_REGS("", pk) : "memory")
#define P1_STATE [sa] "+s"(sa), [sb] "+s"(sb), [md] "+s"(md)
#define P1_CONST [rx] "s"(rx), [rw] "s"(rw), [l0] "s"(lds0 + lw * 4096), [a0] "v"(va[0]), [a1] "v"(va[1]), [a2] "v"(va[2]), [a3] "v"(va[3]), [b0] "v"(vb[0]), [b1x] "v"(vb[1]), [b2] "v"(vb[2]), [b3] "v"(vb[3])
#define P1_LD(v, r, s, o) "buffer_load_dwordx4 %[" v "], %[" r "], %[" s "] offen offset:" #o " lds\n\t"
#define P1_STAGE \
  "s_mov_b32 m0, %[md]\n\ts_add_u32 %[md], %[md], 16384\n\t" \
  P1_LD("a0", "rx", "sa", 0) P1_LD("a1", "rx", "sa", 1024) P1_LD("a2", "rx", "sa", 2048) P1_LD("a3", "rx", "sa", 3072) \
  "s_mov_b32 m0, %[md]\n\ts_add_u32 %[sa], %[sa], 128\n\t" \
  P1_LD("b0", "rw", "sb", 0) P1_LD("b1x", "rw", "sb", 1024) P1_LD("b2", "rw", "sb", 2048) P1_LD("b3", "rw", "sb", 3072) \
  "s_add_u32 %[sb], %[sb], 245760\n\ts_cmp_eq_u32 %[sa], 1280\n\ts_cselect_b32 %[sa], 0, %[sa]\n\ts_cselect_b32 %[sb], 0, %[sb]\n\t" \
  "s_sub_u32 %[md], %[md], %[l0]\n\ts_add_u32 %[md], %[md], 16384\n\ts_and_b32 %[md], %[md], 0x18000\n\ts_add_u32 %[md], %[md], %[l0]\n\t"
#define W1_OPS [rw] "s"(rw1), [dw] "s"(dw), [v0] "v"(vo[0]), [v1] "v"(vo[1]), [v2] "v"(vo[2]), [v3] "v"(vo[3]), [v4] "v"(vo[4]), [v5] "v"(vo[5])
#define LW1(v, o) "buffer_load_dwordx4 %[" v "], %[rw], %[t0] offen offset:" STR(o) " lds\n\t"
#define LW3_0 LW1("v0", 0) LW1("v1", 1024) LW1("v2", 2048)
#define LW3_1 LW1("v3", 0) LW1("v4", 1024) LW1("v5", 2048)
#define LW3(ch, u) "s_add_u32 m0, %[dw], " STR((ch - 5 * (ch / 5)) * 24576 + u * 3072) "\n\ts_mov_b32 %[t0], " STR(ch * 196608) "\n\t" LW3_##u
#define LA(ch) "s_add_u32 m0, %[da], " STR((ch & 3) * 8192) "\n\ts_mov_b32 %[t0], " STR(ch * 8192) "\n\t" \
  "buffer_load_dwordx4 %[va], %[rs], %[t0] offen sc1 lds\n\tbuffer_load_dwordx4 %[va], %[rs], %[t0] offen offset:1024 sc1 lds\n\t"
#define CKR(ch) "ds_read_b128 v[60:63], %[la] offset:" STR((ch & 3) * 8192) "\n\tds_read_b128 v[64:67], %[la] offset:" STR((ch & 3) * 8192 + 1024) "\n\t"
#define CKT "s_waitcnt lgkmcnt(0)\n\tv_max3_u32 v60, v60, v61, v62\n\tv_max3_u32 v64, v63, v64, v65\n\tv_max3_u32 v60, v60, v66, v67\n\tv_max_u32 v60, v60, v64\n\tv_cmp_eq_u32 vcc, -1, v60\n\t"
#define FIX(ch) "s_cbranch_vccz 2f\n\ts_mov_b32 %[t1], 0\n1:\n\ts_sleep 2\n\t" LA(ch) "s_waitcnt vmcnt(0)\n\t" CKR(ch) CKT \
  "s_cbranch_vccz 2f\n\ts_add_u32 %[t1], %[t1], 1\n\ts_cmp_lt_u32 %[t1], 0x40000\n\ts_cbranch_scc1 1b\n2:\n\t"
#define IVL(ch, VA, FETCH, VW) "s_waitcnt vmcnt(" STR(VA) ")\n\t" CKR(ch) FETCH CKT FIX(ch) "s_waitcnt vmcnt(" STR(VW) ")\n\t"

DI void loader_waves(Ctx& c) {
  const Params& p = c.p;
  const int l = c.l, lw = c.w - 4, b = c.b, t4 = lw * 64 + l, cs = c.cs, rt = c.rt;
  const unsigned lds0 = c.lds0, lin = l * 16;
  {
    {
      unsigned vo = lw < 2 ? lin : (l >> 3) * 3840 + (l & 7) * 16;
      unsigned so = lw < 2 ? c.xr0 * 1280 + lw * 5120 : ((c.wi & 7) * 80 + (lw - 2) * 40) * 3840 + (15 * (b & 1) + (c.wi >> 3)) * 128, st = lw < 2 ? 1024 : 8 * 3840;
      int isx = lw;
      asm volatile("" : "+s"(isx), "+v"(vo), "+s"(so), "+s"(st));
      __builtin_amdgcn_sched_barrier(0);
      STAMP(0);
      const char* src = (isx < 2 ? p.x : p.wqkv) + so;
#pragma unroll
      for (int i = 0; i < 5; ++i) glds(src + i * st, vo, lds0 + L1_XS + lw * 5120 + i * 1024);
      __builtin_amdgcn_sched_barrier(0);
    }
    const int rot = (5 * ((c.ct + 2 * c.panel) & 7)) >> 2;
    unsigned va[4], vb[4], sa = rot * 128, sb = rot * 245760, md = lds0 + 65536 + lw * 4096;
#pragma unroll
    for (int i = 0; i < 4; ++i) {
      va[i] = (32 * lw + 8 * i + (l >> 3)) * 1280 + (((l & 7) ^ ((4 * i + (l >> 4)) & 7)) << 4) - i * 1024;
      vb[i] = (16 * lw + 4 * i + (l >> 4)) * 3840 + (((l & 15) ^ (((l >> 4) << 2) | (i & 2))) << 4) - i * 1024;
    }
    const rsrc_t rx = __builtin_amdgcn_make_buffer_rsrc((void*)(p.x + c.panel * 163840), 0, 163840, 0x00020000);
    const rsrc_t rw = __builtin_amdgcn_make_buffer_rsrc((void*)(p.wqkv + c.ct * 256), 0, 640 * 3840 - c.ct * 256, 0x00020000);
    if (c.ct < 15) asm volatile("s_sleep " STR(SDELAY) "\n\t" P1_STAGE "s_waitcnt vmcnt(8)\n\ts_barrier\n\t" P1_STAGE : P1_STATE : P1_CONST : "memory", "scc");
    else { read_tag(c); __builtin_amdgcn_s_sleep(CPD); spare_poison(c); wait_bar<0>(); }
    read_tag(c);
    STAMP(2);
    {
      const char* pc; unsigned len;
      asm volatile("s_getpc_b64 %0\n.Lblk_pc:\n\ts_mov_b32 %1, .Lblk_end1-.Lblk_pc\n\ts_max_i32 %1, %1, .Lblk_end2-.Lblk_pc" : "=s"(pc), "=s"(len) :: "scc");
      const unsigned off = (((b >> 3) * 4 + lw) * 5 + umin(l, 4)) * 128;
      touch(pc, off < len ? off : 0u, lds0 + L_TOUCH);
    }
    if (c.ct < 15) {
      unsigned n = 0;
      asm volatile("s_waitcnt vmcnt(9)\n\ts_barrier\n\t" P1_STAGE
                   "buffer_store_dwordx4 %[ps], %[p0], %[rs], 0 offen sc1\n\tbuffer_store_dwordx4 %[ps], %[p0], %[rs], 0 offen offset:1024 sc1\n\t"
                   "s_waitcnt vmcnt(10)\n\ts_barrier\n\t" P1_STAGE "s_waitcnt vmcnt(10)\n"
                   "1:\n\ts_barrier\n\t" P1_STAGE
                   "s_add_u32 %[n], %[n], 1\n\ts_cmp_lt_u32 %[n], 6\n\ts_waitcnt vmcnt(8)\n\ts_cbranch_scc1 1b"
                   : P1_STATE, [n] "+s"(n)
                   : P1_CONST, [p0] "v"(WS_QK + (640 + 8 * b + 2 * lw) * 1024 + lin), [rs] "s"(c.rs), [ps] "v"(u32x4{POISON, POISON, POISON, POISON}) : "memory", "scc");
      STAMP(5);
      glds_d(p.ws + WS_CS1 + (2 * lw * 1920 + c.ct * 128) * 8, lin, lds0 + L1_CG + lw * 2048);
      glds_d(p.ws + WS_CS1 + ((2 * lw + 1) * 1920 + c.ct * 128) * 8, lin, lds0 + L1_CG + lw * 2048 + 1024);
      glds_m(p.ws + WS_RS1 + (c.panel * 128 + 32 * lw) * 16, lin, lds0 + L1_RG + lw * 512, 0xFFFFFFFFull);
    }
    if (lw == 0 && l < 4) *(LAS unsigned*)(c.lds + L_CNT0 + l * 4) = 0u;
    wait_bar<11>();
    if (c.ct == 15) { publish<0>(c, L_CNT0 + 12, 8, WS_F0 + c.b * 8); __builtin_amdgcn_s_sleep(WOT); }
    {
      const unsigned pv = (l & 1) ? 0u : ONES2;
      if (l < 32) *(LAS u32x4*)(c.lds + VRING + (16 * lw + (l >> 1)) * 352 + 320 + (l & 1) * 16) = u32x4{pv, pv, pv, pv};
    }
    if (lw == 3) poll256(p.ws, WS_F0 + l * 8, c.tag);
    if (c.ct < 15) wait_bar<0>();
    STAMP(6);
    if (c.ct < 15) {
      LAS float* ra = (LAS float*)(c.lds + L1_FIN);
      const unsigned tag = (unsigned)c.tag;
      if (lw < 2) {
        u32x4 g = *(LAS u32x4*)(c.lds + L1_RG + t4 * 16);
#pragma unroll 1
        for (unsigned n = 0; (g[1] != tag || g[3] != tag) && n < SPIN_CAP; ++n) { FENCE(); g = ld128_d(c.rs, WS_RS1 + (c.panel * 128 + t4) * 16); }
        const float sc = c.ct < 5 ? QSCALE : 1.f;
        ra[t4] = __uint_as_float(g[0]) * sc; ra[128 + t4] = __uint_as_float(g[2]) * sc;
      } else {
        const int n0 = t4 - 128; float sum = 0.f; bool ok = true;
#pragma unroll
        for (int i = 0; i < 8; ++i) { const u32x2 g = *(LAS u32x2*)(c.lds + L1_CG + i * 1024 + n0 * 8); sum += __uint_as_float(g[0]); ok = ok && g[1] == tag; }
        if (__any(!ok)) {
          sum = 0.f;
#pragma unroll 1
          for (int i = 0; i < 8; ++i) {
            u32x2 g = {0u, ~tag};
#pragma unroll 1
            for (unsigned n = 0; g[1] != tag && n < SPIN_CAP; ++n) { FENCE(); g = ld64_d(c.rs, WS_CS1 + (i * 1920 + c.ct * 128 + n0) * 8); }
            sum += __uint_as_float(g[0]);
          }
        }
        ra[256 + n0] = sum;
      }
    }
    STAMP(7);
    if (c.ct < 15) LDS_BAR();
  }
  Park pk;
  const rsrc_t rw2 = __builtin_amdgcn_make_buffer_rsrc((void*)(p.w2 + cs * 160), 0, FF * D * 2 - cs * 160, 0x00020000);
  unsigned vo80[5];
  {
    const int h = c.h, e = h & 1, cb = 5 * (h >> 1) + 2 * e;
    unsigned vk[3], vv[3]; u64 mv[3], mt = 0;
#pragma unroll
    for (int i = 0; i < 3; ++i) {
      const int n = 64 * (3 * lw + i) + l, kg = n / 22, dd = n - 22 * kg;
      vk[i] = WS_QK + ((80 + cb + i) * 2048 + 64 + 64 * lw - 256) * 16 + lin - i * 1024;
      vv[i] = WS_V + (h * 256 + 4 - 32 + kg) * 320 + dd * 16 - i * 1024;
      mv[i] = __builtin_amdgcn_ballot_w64(dd < 20 && kg < 32);
      if (i == 0) mt = __builtin_amdgcn_ballot_w64(dd < 20 && kg < 28);
    }
    auto issue = [&](int s, bool withk, u64 m0, u64 m1, u64 m2) {
      unsigned t;
      if (withk) asm volatile("s_mov_b32 m0, %[d]\n\ts_lshl_b32 %[t], %[s], 12\n\t"
                              "buffer_load_dwordx4 %[v0], %[rs], %[t] offen sc1 lds\n\tbuffer_load_dwordx4 %[v1], %[rs], %[t] offen offset:1024 sc1 lds\n\tbuffer_load_dwordx4 %[v2], %[rs], %[t] offen offset:2048 sc1 lds"
                              : [t] "=&s"(t) : [d] "s"(lds0 + KRING + (s & 1) * 12288 + lw * 3072), [s] "s"(s + 1), [rs] "s"(c.rs), [v0] "v"(vk[0]), [v1] "v"(vk[1]), [v2] "v"(vk[2]) : "memory", "scc");
      asm volatile("s_mov_b32 m0, %[d]\n\ts_mul_i32 %[t], %[s], 10240\n\t"
                   "s_mov_b64 exec, %[m0]\n\tbuffer_load_dwordx4 %[v0], %[rs], %[t] offen sc1 lds\n\t"
                   "s_mov_b64 exec, %[m1]\n\ts_cbranch_execz 1f\n\tbuffer_load_dwordx4 %[v1], %[rs], %[t] offen offset:1024 sc1 lds\n\t"
                   "s_mov_b64 exec, %[m2]\n\ts_cbranch_execz 1f\n\tbuffer_load_dwordx4 %[v2], %[rs], %[t] offen offset:2048 sc1 lds\n1:\n\ts_mov_b64 exec, -1"
                   : [t] "=&s"(t) : [d] "s"(lds0 + VRING + (s & 1) * 11264 + lw * 3072), [s] "s"(s + 1), [rs] "s"(c.rs), [v0] "v"(vv[0]), [v1] "v"(vv[1]), [v2] "v"(vv[2]),
                     [m0] "s"(m0), [m1] "s"(m1), [m2] "s"(m2) : "memory");
    };
    auto bad = [&](int s, u64 m0, u64 m1, u64 m2) {
      u64 any;
      asm volatile("ds_read_b128 v[76:79], %[k]\n\tds_read_b128 v[80:83], %[k] offset:1024\n\tds_read_b128 v[84:87], %[k] offset:2048\n\t"
                   "ds_read_b128 v[88:91], %[v]\n\tds_read_b128 v[92:95], %[v] offset:1024\n\tds_read_b128 v[96:99], %[v] offset:2048\n\t"
                   WL(4) "v_max3_u32 v76, v76, v77, v78\n\tv_max3_u32 v80, v79, v80, v81\n\tv_max3_u32 v76, v76, v82, v83\n\t"
                   WL(3) "v_max3_u32 v80, v80, v84, v85\n\tv_max3_u32 v76, v76, v86, v87\n\t"
                   WL(0) "s_mov_b64 exec, %[m0]\n\tv_max3_u32 v80, v80, v88, v89\n\tv_max3_u32 v76, v76, v90, v91\n\t"
                   "s_mov_b64 exec, %[m1]\n\tv_max3_u32 v80, v80, v92, v93\n\tv_max3_u32 v76, v76, v94, v95\n\t"
                   "s_mov_b64 exec, %[m2]\n\tv_max3_u32 v80, v80, v96, v97\n\tv_max3_u32 v76, v76, v98, v99\n\t"
                   "s_mov_b64 exec, -1\n\tv_max_u32 v76, v76, v80\n\tv_cmp_eq_u32 %[any], -1, v76\n\ts_nop 0"
                   : [any] "=s"(any) : [k] "v"(lds0 + KRING + (s & 1) * 12288 + lw * 3072 + lin), [v] "v"(lds0 + VRING + (s & 1) * 11264 + lw * 3072 + lin), [m0] "s"(m0), [m1] "s"(m1), [m2] "s"(m2)
                   : "memory", CL8(76, 77, 78, 79, 80, 81, 82, 83), CL8(84, 85, 86, 87, 88, 89, 90, 91), CL8(92, 93, 94, 95, 96, 97, 98, 99));
      return any != 0;
    };
#pragma unroll
    for (int i = 0; i < 5; ++i) { const int n = 64 * i + l, rr = n / 10; vo80[i] = swap23(32 * lw + rr) * 1280 + (n - 10 * rr) * 16 - (i & 3) * 1024; }
    const rsrc_t rwo = __builtin_amdgcn_make_buffer_rsrc((void*)(p.wo + cs * 160), 0, D * D * 2 - cs * 160, 0x00020000);
    const unsigned xw = (b >> 3) * 4 + lw, vt60 = (l < 60 ? l : 0) * 256;
    u32x4 psn = {POISON, POISON, POISON, POISON};
    asm volatile("" : "+v"(psn));
    const int b1 = b < 192 ? b : 0, rg1 = b1 / 24, cg1 = b1 - 24 * rg1;
    unsigned w1o = (t4 >> 3) * 3072 + (l & 7) * 16, cs2o = WS_CS2 + (rg1 * 1536 + cg1 * 64 + 16 * lw + l) * 4;
    const char* bw = (l & 32 ? p.w2 : p.w1) + ((b * 4 + lw) * 15 + umin(l & 31, 14)) * 128;
    asm volatile("" : "+v"(w1o), "+v"(cs2o), "+v"(bw), "+v"(vo80[0]), "+v"(vo80[1]), "+v"(vo80[2]), "+v"(vo80[3]), "+v"(vo80[4]));
    LDS_BAR();
    __builtin_amdgcn_s_sleep(KDELAY);
#pragma unroll 1
    for (unsigned n = 0; n < SPIN_CAP; ++n) {
      issue(0, true, mv[0], mv[1], mv[2]);
      DRAIN();
      if (!bad(0, mv[0], mv[1], mv[2])) break;
      __builtin_amdgcn_s_sleep(RDELAY);
    }
    STAMP(9);
    LDS_BAR();
    __builtin_amdgcn_sched_barrier(0);
    __builtin_amdgcn_s_setprio(3);
#pragma unroll 1
    for (int s = 1; s < 5; ++s) {
      if (s > 1) LDS_BAR();
      STAMPV(20 + s);
      issue(s, true, mv[0], mv[1], mv[2]);
#pragma unroll
      for (int k = 0; k < 3; ++k) {
        const int q = 12 * lw + 3 * (s - 1) + k;
        asm volatile("buffer_store_dwordx4 %0, %1, %2, %3 offen sc1" :: "v"(psn), "v"(lin), "s"(c.rs),
                     "s"(q < 10 ? WS_O + rt * 81920 + cs * 10240 + q * 1024 : q < 20 ? WS_H + rt * 81920 + cs * 10240 + (q - 10) * 1024 : q < 44 ? WS_F + rt * 196608 + cs * 24576 + (q - 20) * 1024 : WS_RS2 + (rt * 8 + cs) * 512) : "memory");
      }
      if (s == 1) {
        wait_vm<3>();
#pragma unroll 1
        for (unsigned n = 0; bad(1, mv[0], mv[1], mv[2]) && n < SPIN_CAP; ++n) { issue(1, true, mv[0], mv[1], mv[2]); DRAIN(); }
        if (lw == 0) {
          poll256(p.ws, WS_F1 + l * 8, c.tag);
          if (b == 0 && l == 0) stflag(p.ws, WS_EPOCH, c.tag);
        }
      } else {
#pragma unroll 1
        for (int k = 2 * s - 4; k < (s < 4 ? 2 * s - 2 : 5); ++k)
        { const int kr = k + (rt & 3) < 5 ? k + (rt & 3) : k + (rt & 3) - 5; W80_DMA(rwo, lds0 + L_WO + kr * 20480 + lw * 5120, kr * 163840); }
        if (s == 4) { touch64(bw, lds0 + L_TOUCH); wait_vm<9>(); }
        wait_vm<13>();
        if (s == 3) {
          const char* src = p.w1 + (rg1 * 80 * 3072 + cg1 * 128);
          glds(src, w1o, lds0 + L_W1B + lw * 1024); glds(src + 32 * 3072, w1o, lds0 + L_W1B + 4096 + lw * 1024);
          if (lw < 2) glds(src + 64 * 3072, w1o, lds0 + L_W1B + 8192 + lw * 1024);
        }
      }
      STAMPV(9 + s);
    }
    unsigned so, st;
#define PARK2(m, k0, c0, k1, c1) asm volatile(PKL(k0, c0) PKL(k1, c1) : PARK_REGS(m, pk), [so] "=&s"(so), [t] "=&s"(st) : W80_OPS(rw2), [r5] "s"(c.rot5 * 163840) : "memory", "scc")
    LDS_BAR(); STAMP(25);
    issue(5, true, mv[0], mv[1], mv[2]);
    {
      const float cs2 = colsum(c.lds + L_W1B, l, lw);
      if (l < 16 && b < 192) st32_d(c.rs, cs2o, __float_as_uint(cs2));
    }
    PARK2("=&", 0, 0, 1, 1);
    wait_bar<10>(); STAMP(26);
    publish<10>(c, L_CNTP, 4, WS_FP + b * 8);
    issue(6, true, mv[0], mv[1], mv[2]);
    PARK2("+", 2, 2, 3, 3);
    wait_bar<10>(); STAMP(27);
    issue(7, lw != 3, lw == 3 ? mt : mv[0], lw == 3 ? 0 : mv[1], mv[2]);
    PARK2("+", 4, 4, 5, 8);
    wait_bar<10>(); STAMP(28);
    if (lw == 0) poll256(p.ws, WS_FP + l * 8, c.tag);
    PARK2("+", 6, 9, 7, 10);
    if (lw == 1) {
      const int i = l & 7, k = l >> 3;
      touch64(k == 0 ? p.ws + (WS_O + c.qb * 327680 + i * 40960) : k == 1 ? p.ws + (WS_O + rt * 81920 + i * 10240) : k == 2 ? p.ws + (WS_H + rt * 81920 + i * 10240) : k == 3 ? p.ws + (WS_F + rt * 196608 + i * 24576)
              : k == 4 ? p.out + (rt * 81920 + i * 10240) : k == 5 ? p.x + (rt * 81920 + i * 10240) : p.ws + (WS_RS2 + rt * 4096 + i * 512), lds0 + L_TOUCH);
    }
    touch(p.w1 + xw * 15360, vt60, lds0 + L_TOUCH);
    touch(p.w1 + (xw * 15360 + 128), vt60, lds0 + L_TOUCH);
  }
  {
    unsigned vo[6];
#pragma unroll
    for (int i = 0; i < 3; ++i) {
      const int n = 64 * i + l, rr = n / 24, pos = n - 24 * rr;
      vo[i] = ((rr & 3) + 8 * (rr >> 2)) * 3072 + ((((pos >> 1) ^ ((rr >> 1) & 3)) << 1) | (pos & 1)) * 16 - i * 1024;
      vo[3 + i] = vo[i] + 4 * 3072;
    }
    const rsrc_t rw1 = __builtin_amdgcn_make_buffer_rsrc((void*)(p.w1 + cs * 384 + lw * 16 * 3072), 0, D * FF * 2 - cs * 384 - lw * 16 * 3072, 0x00020000);
    const unsigned dw = lds0 + lw * 6144, da = lds0 + L4_A + lw * 2048, va = WS_H + rt * 81920 + lw * 2048 + lin;
    const unsigned so = WS_RS2 + (rt * 8 + (l >> 4)) * 512 + (16 * lw + (l & 15)) * 8;
    unsigned t0, t1r; u32x2 s0, s1;
    LDS_BAR();
    __builtin_amdgcn_s_setprio(0);
    STAMP(17);
    LDS_BAR();
    asm volatile(LW3(0, 0) LW3(0, 1) LW3(1, 0) LW3(1, 1) : [t0] "=&s"(t0) : W1_OPS : "memory");
    if (lw < 3) {
      float t = 0.f;
#pragma unroll
      for (int i = 0; i < 8; ++i) t += __uint_as_float(ld32_d(c.rs, WS_CS2 + (i * 1536 + cs * 192 + t4) * 4));
      *(LAS float*)(c.lds + L_CSUM + t4 * 4) = t;
    }
    asm volatile(PKL(8, 11) : PARK_REGS("+", pk), [so] "=&s"(t0), [t] "=&s"(t1r) : W80_OPS(rw2), [r5] "s"(c.rot5 * 163840) : "memory", "scc");
    LDS_BAR(); LDS_BAR(); LDS_BAR(); LDS_BAR();
    STAMP(18);
    LDS_BAR();
    asm volatile(
        LW3(2, 0) LW3(2, 1) LW3(3, 0) LW3(3, 1)
        "s_mov_b32 %[t1], 0\n"
        "1:\n\ts_sleep " STR(ADELAY) "\n\t" LA(0) LA(1) LA(2) LA(3)
        "s_waitcnt vmcnt(6)\n\t" CKR(0) CKT
        "s_cbranch_vccz 2f\n\ts_waitcnt vmcnt(0)\n\ts_add_u32 %[t1], %[t1], 1\n\ts_cmp_lt_u32 %[t1], 0x40000\n\ts_cbranch_scc1 1b\n"
        "2:\n\ts_barrier\n\t"
        IVL(1, 4, LW3(4, 0) LW3(4, 1), 63) "s_barrier\n\t"
        IVL(2, 8, LA(4) LW3(5, 0), 63) "s_barrier\n\t"
        IVL(3, 11, LA(5) LW3(5, 1), 63) "s_barrier\n\t"
        IVL(4, 8, LA(6) LW3(6, 0) LW3(6, 1), 63) "s_barrier\n\t"
        IVL(5, 11, LA(7) LW3(7, 0), 13) "s_barrier\n\t"
        IVL(6, 11, LA(8) LW3(7, 1) LW3(8, 0), 13) "s_barrier\n\t"
        IVL(7, 11, LA(9) LW3(8, 1), 8) "s_barrier\n\t"
        IVL(8, 11, "buffer_load_dwordx2 %[s0], %[so], %[rs], 0 offen sc1\n\tbuffer_load_dwordx2 %[s1], %[so], %[rs], 0 offen offset:2048 sc1\n\t" LW3(9, 0) LW3(9, 1), 8) "s_barrier\n\t"
        IVL(9, 11, "", 6)
        : [t0] "=&s"(t0), [t1] "=&s"(t1r), [s0] "=&v"(s0), [s1] "=&v"(s1)
        : W1_OPS, [rs] "s"(c.rs), [da] "s"(da), [va] "v"(va), [la] "v"(da + lin), [so] "v"(so) : "memory", "scc", "vcc", "v60", "v61", "v62", "v63", "v64", "v65", "v66", "v67");
    STAMP(29);
#pragma unroll 1
    for (unsigned n = 0; __any(umax(umax(s0[0], s0[1]), umax(s1[0], s1[1])) == POISON) && n < SPIN_CAP; ++n) { FENCE(); s0 = ld64_d(c.rs, so); s1 = ld64_d(c.rs, so + 2048); }
    float t1 = __uint_as_float(s0[0]) + __uint_as_float(s1[0]), t2 = __uint_as_float(s0[1]) + __uint_as_float(s1[1]);
    t1 = swap16_add(t1, t1); t2 = swap16_add(t2, t2); t1 = swap32_add(t1, t1); t2 = swap32_add(t2, t2);
    const float mean = t1 * (1.f / D), var = fmaxf(t2 * (1.f / D) - mean * mean, 0.f), a = __builtin_amdgcn_rsqf(var + LN_EPS);
    if (l < 32) *(LAS float*)(c.lds + L_RA + (l >> 4) * 256 + (16 * lw + (l & 15)) * 4) = l < 16 ? a : -mean * a;
    wait_bar<0>();
  }
  {
    const unsigned wa = lds0 + lw * 5120 + lin;
    {
      const int row = (5 + c.rot5) * 128 + ((b >> 6) * 4 + lw) * 24 + umin(l >> 1, 23);
      touch(p.w2 + cs * 160, (row < FF ? row : row - FF) * 1280 + (l & 1) * 156, lds0 + L_TOUCH);
    }
    LDS_BAR();
    STAMP(30);
    PUT(0, 0); PUT(1, 1); PUT(2, 2);
    LDS_BAR();
#pragma unroll 1
    for (int k = 5; k < 8; ++k) W80_DMA(rw2, lds0 + k * 20480 + lw * 5120, (k + c.rot5 < 12 ? k + c.rot5 : k + c.rot5 - 12) * 163840);
    PUT(3, 3); PUT(4, 4);
    STAMP(31);
    LDS_BAR();
    wait_bar<0>(); PUT(5, 0); PUT(6, 1); PUT(7, 2); PUT(8, 3);
    LDS_BAR(); LDS_BAR(); LDS_BAR();
  }
}

__global__ void __launch_bounds__(512, 2) block_kernel(Params p) {
  extern __shared__ __attribute__((aligned(16))) char smem[];
  Ctx c;
  c.st_lo = c.st_hi = 0;
  STAMP(3 + 20);
  c.p = p; c.lds = (lptr)smem; c.lds0 = (unsigned)(size_t)c.lds; c.l = threadIdx.x & 63; c.w = rfl(threadIdx.x >> 6); c.b = blockIdx.x;
  const int xcd = c.b & 7, j = c.b >> 3;
  const int t1 = 30 * (xcd & 1) + j;
  c.panel = 4 * (xcd >> 1) + (t1 & 3); c.ct = j < 30 ? t1 >> 2 : 15;
  c.h = c.rt = 4 * xcd + (j >> 3); c.qb = c.cs = j & 7;
  c.xr0 = 512 * (xcd >> 1) + 8 * (32 * (xcd & 1) + j); c.wi = j < 30 ? (xcd >> 1) * 30 + j : 0;
  c.rot5 = (3 * c.cs) >> 1; asm volatile("" : "+s"(c.rot5));
  if (__builtin_expect(c.w >= 4, 1)) loader_waves(c);
  else { STAMP(0); compute_waves(c); }
}

extern "C" void kernel_launch(void* const* d_in, const int* in_sizes, int n_in, void* d_out, int out_size, void* d_ws, size_t ws_size, hipStream_t stream) {
  Params p;
  p.x = (const char*)d_in[0]; p.wqkv = (const char*)d_in[1]; p.wo = (const char*)d_in[2]; p.w1 = (const char*)d_in[4]; p.w2 = (const char*)d_in[5];
  p.out = (char*)d_out; p.ws = (char*)d_ws;
  (void)hipFuncSetAttribute((const void*)block_kernel, hipFuncAttributeMaxDynamicSharedMemorySize, LDS_BYTES);
  block_kernel<<<256, 512, LDS_BYTES, stream>>>(p);
}
```

```cpp
#include <hip/hip_runtime.h>
#include <hip/hip_bf16.h>

#define DI __device__ __forceinline__
#define LAS __attribute__((address_space(3)))
#define GAS __attribute__((address_space(1)))
typedef unsigned long long u64;
typedef __attribute__((ext_vector_type(8))) short s16x8;
typedef __attribute__((ext_vector_type(4))) short s16x4;
typedef __attribute__((ext_vector_type(8))) __bf16 bf16x8;
typedef __attribute__((ext_vector_type(16))) __bf16 bf16x16;
typedef __attribute__((ext_vector_type(2))) __bf16 bf16x2;
typedef __attribute__((ext_vector_type(2))) float f32x2;
typedef __attribute__((ext_vector_type(4))) float f32x4;
typedef __attribute__((ext_vector_type(8))) float f32x8;
typedef __attribute__((ext_vector_type(16))) float f32x16;
typedef __attribute__((ext_vector_type(32))) float f32x32;
typedef __attribute__((ext_vector_type(4))) unsigned u32x4;
typedef __attribute__((ext_vector_type(2))) unsigned u32x2;
typedef LAS char* lptr;
typedef __amdgpu_buffer_rsrc_t rsrc_t;

#define V_BY_HEAD
#define SDELAY 10
#define CPD 30
#define WOT 60
#define QDELAY 20
#define KDELAY 36
#define PDELAY 2
#define ADELAY 6
#define RDELAY 6
#define PSLEEP 2
constexpr int S = 2048, D = 640, FF = 1536;
constexpr float LN_EPS = 1e-5f;
constexpr float QSCALE = 0.22360679774997896f * 1.4426950408889634f;

constexpr unsigned WS_EPOCH = 0, WS_F0 = 4096, WS_F1 = 6144, WS_FP = 8192;
constexpr unsigned WS_RS1 = 65536;
constexpr unsigned WS_CS1 = WS_RS1 + 2048 * 16;
constexpr unsigned WS_CS2 = WS_CS1 + 8 * 1920 * 8;
constexpr unsigned WS_QK = WS_CS2 + 8 * 1536 * 4;
constexpr unsigned WS_V = WS_QK + 160 * 2048 * 16;
constexpr unsigned WS_RS2 = WS_V + 256 * 640 * 16;
constexpr unsigned WS_O = WS_RS2 + 32 * 8 * 512;
constexpr unsigned WS_H = WS_O + S * D * 2;
constexpr unsigned WS_F = WS_H + S * D * 2;
constexpr unsigned WS_END = WS_F + S * FF * 2;
constexpr int LDS_BYTES = 163840;
constexpr unsigned SPIN_CAP = 1u << 18;
constexpr unsigned POISON = 0xFFFFFFFFu;
constexpr int L1_CG = 131072, L1_RG = L1_CG + 8192, L1_FIN = L1_RG + 2048;
constexpr int L1_XS = 131072, L1_WS = L1_XS + 10240;
constexpr int KRING = 0, VRING = 24576, L_W1B = 47104, L_WO = 57344;
constexpr int L4_A = 122880, L_EX = 98304;
constexpr int L_CNT0 = 159744, L_CNT1 = L_CNT0 + 4, L_CNTP = L_CNT0 + 8, L_CSUM = L_CNT0 + 256, L_RA = L_CSUM + 768, L_TOUCH = L_RA + 512;

#define TS(i) ""
#define TS_CLOB
#define TS_PUT(i, k)
#define STAMP(k)
#define STAMPV(k)
struct Params { const char* x; const char* wqkv; const char* wo; const char* w1; const char* w2; char* out; char* ws; };
struct Ctx { Params p; lptr lds; unsigned lds0; rsrc_t rs; u64 tag; int l, w, b, panel, ct, h, qb, rt, cs, xr0, wi; mutable unsigned st_lo, st_hi; };

DI unsigned rfl(unsigned v) { return __builtin_amdgcn_readfirstlane(v); }
DI unsigned cvtpk(float lo, float hi) { f32x2 v = {lo, hi}; return __builtin_bit_cast(unsigned, __builtin_convertvector(v, bf16x2)); }
typedef __attribute__((ext_vector_type(2))) short s16x2;
DI unsigned relu2(unsigned v) { return __builtin_bit_cast(unsigned, __builtin_elementwise_max(__builtin_bit_cast(s16x2, v), s16x2{0, 0})); }
DI float bflo(unsigned v) { return __uint_as_float(v << 16); }
DI float bfhi(unsigned v) { return __uint_as_float(v & 0xffff0000u); }
DI void glds(const char* sbase, unsigned voff, unsigned lds) {
  asm volatile("s_mov_b32 m0, %2\n\ts_nop 0\n\tglobal_load_lds_dwordx4 %0, %1" :: "v"(voff), "s"(sbase), "s"(lds) : "memory");
}
DI void glds_d(const char* sbase, unsigned voff, unsigned lds) {
  asm volatile("s_mov_b32 m0, %2\n\ts_nop 0\n\tglobal_load_lds_dwordx4 %0, %1 sc1" :: "v"(voff), "s"(sbase), "s"(lds) : "memory");
}
DI void glds_m(const char* sbase, unsigned voff, unsigned lds, u64 mask) {
  asm volatile("s_mov_b32 m0, %2\n\ts_mov_b64 exec, %3\n\tglobal_load_lds_dwordx4 %0, %1 sc1\n\ts_mov_b64 exec, -1" :: "v"(voff), "s"(sbase), "s"(lds), "s"(mask) : "memory");
}
DI void touch(const char* sbase, unsigned voff, unsigned lds) {
  asm volatile("s_mov_b32 m0, %2\n\ts_nop 0\n\tglobal_load_lds_dword %0, %1" :: "v"(voff), "s"(sbase), "s"(lds) : "memory");
}
DI void touch64(const char* a, unsigned lds) { asm volatile("s_mov_b32 m0, %1\n\ts_nop 0\n\tglobal_load_lds_dword %0, off" :: "v"(a), "s"(lds) : "memory"); }
template <int N> DI void wait_vm() { asm volatile("s_waitcnt vmcnt(%0)" :: "i"(N) : "memory"); }
template <int N> DI void wait_bar() { asm volatile("s_waitcnt vmcnt(%0) lgkmcnt(0)\n\ts_barrier" :: "i"(N) : "memory"); }
#define LDS_BAR() asm volatile("s_waitcnt lgkmcnt(0)\n\ts_barrier" ::: "memory")
#define DRAIN() asm volatile("s_waitcnt vmcnt(0)" ::: "memory")
#define STR_(x) #x
#define STR(x) STR_(x)
#define VR(b, n) "v[" STR(b) ":" STR(b) "+" STR(n) "-1]"
#define WL(n) "s_waitcnt lgkmcnt(" #n ")\n\t"
#define CL8(a, b, c, d, e, f, g, h) "v" #a, "v" #b, "v" #c, "v" #d, "v" #e, "v" #f, "v" #g, "v" #h
DI s16x4 ldtr(lptr p) { return __builtin_amdgcn_ds_read_tr16_b64_v4i16((LAS s16x4*)p); }
DI s16x8 ldtr2(lptr p, int d) { return __builtin_shufflevector(ldtr(p), ldtr(p + d), 0, 1, 2, 3, 4, 5, 6, 7); }
DI f32x16 mfma32(s16x8 a, s16x8 b, f32x16 c) { return __builtin_amdgcn_mfma_f32_32x32x16_bf16(__builtin_bit_cast(bf16x8, a), __builtin_bit_cast(bf16x8, b), c, 0, 0, 0); }
DI f32x4 mfma16(s16x8 a, s16x8 b, f32x4 c) { return __builtin_amdgcn_mfma_f32_16x16x32_bf16(__builtin_bit_cast(bf16x8, a), __builtin_bit_cast(bf16x8, b), c, 0, 0, 0); }
DI float dot2(unsigned a, unsigned b, float c) { return __builtin_amdgcn_fdot2_f32_bf16(__builtin_bit_cast(bf16x2, a), __builtin_bit_cast(bf16x2, b), c, false); }
constexpr unsigned ONES2 = 0x3f803f80u;
DI void st128_d(rsrc_t rs, unsigned off, u32x4 v) { __builtin_amdgcn_raw_buffer_store_b128(v, rs, off, 0, 16); }
DI void st64_d(rsrc_t rs, unsigned off, u32x2 v) { __builtin_amdgcn_raw_buffer_store_b64(v, rs, off, 0, 16); }
DI void st32_d(rsrc_t rs, unsigned off, unsigned v) { __builtin_amdgcn_raw_buffer_store_b32(v, rs, off, 0, 16); }
DI u32x4 ld128_d(rsrc_t rs, unsigned off) { return __builtin_amdgcn_raw_buffer_load_b128(rs, off, 0, 16); }
DI u32x2 ld64_d(rsrc_t rs, unsigned off) { return __builtin_amdgcn_raw_buffer_load_b64(rs, off, 0, 16); }
DI unsigned ld32_d(rsrc_t rs, unsigned off) { return __builtin_amdgcn_raw_buffer_load_b32(rs, off, 0, 16); }
DI u64 ldflag(const char* ws, unsigned off) { return __hip_atomic_load((GAS u64*)(ws + off), __ATOMIC_RELAXED, __HIP_MEMORY_SCOPE_AGENT); }
DI void stflag(char* ws, unsigned off, u64 v) { __hip_atomic_store((GAS u64*)(ws + off), v, __ATOMIC_RELAXED, __HIP_MEMORY_SCOPE_AGENT); }
DI void read_tag(Ctx& c) {
  const char* ws = c.p.ws; asm volatile("" : "+s"(ws));
  c.rs = __builtin_amdgcn_make_buffer_rsrc(c.p.ws, 0, WS_END, 0x00020000);
  c.tag = *(const __attribute__((address_space(4))) u64*)(ws + WS_EPOCH) + 1;
}
DI void poll256(const char* ws, unsigned off, u64 tag) {
#pragma unroll 1
  for (unsigned n = 0; n < SPIN_CAP; ++n) {
    const u64 a = ldflag(ws, off), b = ldflag(ws, off + 512), c = ldflag(ws, off + 1024), d = ldflag(ws, off + 1536);
    if (__all(a == tag && b == tag && c == tag && d == tag)) break;
    __builtin_amdgcn_s_sleep(4);
  }
}
template <int YOUNGER> DI void publish(const Ctx& c, int cnt_off, unsigned n, unsigned flag_off) {
  wait_vm<YOUNGER>();
  if (c.l == 0) {
    const unsigned old = __hip_atomic_fetch_add((LAS unsigned*)(c.lds + cnt_off), 1u, __ATOMIC_RELAXED, __HIP_MEMORY_SCOPE_WORKGROUP);
    if (old == n - 1) stflag(c.p.ws, flag_off, c.tag);
  }
}
template <int CTRL> DI float dpp_add(float v) { return v + __int_as_float(__builtin_amdgcn_update_dpp(0, __float_as_int(v), CTRL, 0xf, 0xf, true)); }
DI float swap16_add(float a, float b) { const auto t = __builtin_amdgcn_permlane16_swap(__float_as_uint(a), __float_as_uint(b), false, false); return __uint_as_float(t[0]) + __uint_as_float(t[1]); }
DI float swap32_add(float a, float b) { const auto t = __builtin_amdgcn_permlane32_swap(__float_as_uint(a), __float_as_uint(b), false, false); return __uint_as_float(t[0]) + __uint_as_float(t[1]); }
DI float swap32_max(float a) { const auto t = __builtin_amdgcn_permlane32_swap(__float_as_uint(a), __float_as_uint(a), false, false); return fmaxf(__uint_as_float(t[0]), __uint_as_float(t[1])); }
DI float upper_half(float a) { const auto t = __builtin_amdgcn_permlane32_swap(__float_as_uint(a), __float_as_uint(a), false, false); return __uint_as_float(t[1]); }
DI u32x4 swap32_pair(unsigned a0, unsigned a1, unsigned b0, unsigned b1) {
  const auto s0 = __builtin_amdgcn_permlane32_swap(a0, b0, false, false), s1 = __builtin_amdgcn_permlane32_swap(a1, b1, false, false);
  return u32x4{s0[0], s1[0], s0[1], s1[1]};
}
DI u32x4 swap16_pair(unsigned a0, unsigned a1, unsigned b0, unsigned b1) {
  const auto s0 = __builtin_amdgcn_permlane16_swap(a0, b0, false, false), s1 = __builtin_amdgcn_permlane16_swap(a1, b1, false, false);
  return u32x4{s0[0], s1[0], s0[1], s1[1]};
}
DI float colsum(lptr blk, int l, int w4) {
  lptr src = blk + (8 * (l >> 4) + ((l & 15) >> 2)) * 128 + w4 * 32 + (l & 3) * 8;
  const short one = 0x3f80, last = l < 32 ? one : 0;
  const s16x8 b0 = ldtr2(src, 512), b1 = ldtr2(src + 4096, 512), b2 = ldtr2(src + (l < 32 ? 8192 : 0), 512);
  f32x4 acc = mfma16(s16x8{one, one, one, one, one, one, one, one}, b0, f32x4{});
  acc = mfma16(s16x8{one, one, one, one, one, one, one, one}, b1, acc);
  return mfma16(s16x8{last, last, last, last, last, last, last, last}, b2, acc)[0];
}
DI unsigned np(unsigned v) { return v < POISON - 1 ? v : POISON - 1; }
DI u32x4 np(u32x4 v) { return u32x4{np(v[0]), np(v[1]), np(v[2]), np(v[3])}; }
DI unsigned umax(unsigned a, unsigned b) { return a > b ? a : b; }
DI unsigned umin(unsigned a, unsigned b) { return a < b ? a : b; }
DI unsigned umax4(u32x4 v) { return umax(umax(v[0], v[1]), umax(v[2], v[3])); }
#define FENCE() asm volatile("" ::: "memory")
DI f32x16 lo16(const f32x32& s) { return __builtin_shufflevector(s, s, 0, 1, 2, 3, 4, 5, 6, 7, 8, 9, 10, 11, 12, 13, 14, 15); }
DI f32x16 hi16(const f32x32& s) { return __builtin_shufflevector(s, s, 16, 17, 18, 19, 20, 21, 22, 23, 24, 25, 26, 27, 28, 29, 30, 31); }
DI int swap23(int k) { return (k & ~12) | ((k & 4) << 1) | ((k & 8) >> 1); }

DI void row_stats(const Ctx& c, const u32x4 (&xv)[3], int xrow) {
  const int r = c.l & 31;
  float s1 = 0.f, s2 = 0.f;
#pragma unroll
  for (int i = 0; i < 3; ++i)
#pragma unroll
    for (int k = 0; k < 4; ++k) { const unsigned v = (i < 2 || r < 16) ? xv[i][k] : 0u; s1 = dot2(v, ONES2, s1); s2 = dot2(v, v, s2); }
  s1 = dpp_add<0x128>(dpp_add<0x141>(dpp_add<0x4E>(dpp_add<0xB1>(s1)))); s2 = dpp_add<0x128>(dpp_add<0x141>(dpp_add<0x4E>(dpp_add<0xB1>(s2))));
  s1 = swap16_add(s1, s1); s2 = swap16_add(s2, s2);
  const float mean = s1 * (1.f / D), var = fmaxf(s2 * (1.f / D) - mean * mean, 0.f), a = __builtin_amdgcn_rsqf(var + LN_EPS);
  const unsigned tag = (unsigned)c.tag;
  if (r == 0) st128_d(c.rs, WS_RS1 + xrow * 16, u32x4{__float_as_uint(a), tag, __float_as_uint(-mean * a), tag});
}
DI void spare_poison(const Ctx& c) {
  u32x4 psn = {POISON, POISON, POISON, POISON};
  asm volatile("" : "+v"(psn));
  const unsigned q0 = ((c.b - 240) * 8 + c.w) * 17;
#pragma unroll 1
  for (unsigned q = q0; q < umin(q0 + 17, 2064); ++q) {
    const unsigned v = q - 720;
    asm volatile("buffer_store_dwordx4 %0, %1, %2, %3 offen sc1" :: "v"(psn), "v"(c.l * 16), "s"(c.rs),
                 "s"(q < 720 ? WS_QK + (80 + q / 9) * 32768 + q % 9 * 1024 : q < 1424 ? WS_V + v / 22 * 81920 + v % 22 * 1024 : WS_QK + (q - 1424) * 1024) : "memory");
  }
}
#define Q_X 96
#define Q_Y 128
#define QMK(S, m, n) "v_mfma_f32_16x16x32_bf16 " VR(32 + m * 16 + n * 4, 4) ", " VR(S + 16 + n * 4, 4) ", " VR(S + m * 4, 4) ", " VR(32 + m * 16 + n * 4, 4) "\n\t"
#define QMV(S, m, n) "v_mfma_f32_16x16x32_bf16 " VR(32 + m * 16 + n * 4, 4) ", " VR(S + m * 4, 4) ", " VR(S + 16 + n * 4, 4) ", " VR(32 + m * 16 + n * 4, 4) "\n\t"
#define QRA(S, m, ks) "ds_read_b128 " VR(S + m * 4, 4) ", v[160+" STR(ks) "] offset:" STR(m * 2048) "\n\t"
#define QRB(S, n, h, ks) "ds_read_b64_tr_b16 " VR(S + 16 + n * 4 + h * 2, 2) ", v[162+" STR(n) "] offset:" STR(ks * 8192 + h * 1024) "\n\t"
#define QAD(i) "v_add_u32 v[160+" #i "], %[dl], v[160+" #i "]\n\t"
#define P1_LOOP(MM) \
  QRA(Q_Y, 0, 0) QRB(Q_Y, 0, 0, 0) QRB(Q_Y, 0, 1, 0) QRB(Q_Y, 1, 0, 0) QRB(Q_Y, 1, 1, 0) QRA(Q_Y, 1, 0) \
  QRB(Q_Y, 2, 0, 0) QRB(Q_Y, 2, 1, 0) QRB(Q_Y, 3, 0, 0) QRB(Q_Y, 3, 1, 0) QRA(Q_Y, 2, 0) QRA(Q_Y, 3, 0) \
  "s_branch 2f\n" \
  "1:\n\t" \
  MM(Q_X, 1, 0) QRA(Q_Y, 0, 0) MM(Q_X, 1, 1) QRB(Q_Y, 0, 0, 0) MM(Q_X, 0, 2) QRB(Q_Y, 0, 1, 0) MM(Q_X, 1, 2) QRB(Q_Y, 1, 0, 0) \
  MM(Q_X, 0, 3) QRB(Q_Y, 1, 1, 0) MM(Q_X, 1, 3) QRA(Q_Y, 1, 0) MM(Q_X, 2, 0) QRB(Q_Y, 2, 0, 0) MM(Q_X, 2, 1) QRB(Q_Y, 2, 1, 0) \
  MM(Q_X, 2, 2) QRB(Q_Y, 3, 0, 0) MM(Q_X, 2, 3) QRB(Q_Y, 3, 1, 0) MM(Q_X, 3, 0) QRA(Q_Y, 2, 0) MM(Q_X, 3, 1) QRA(Q_Y, 3, 0) MM(Q_X, 3, 2) MM(Q_X, 3, 3) \
  "2:\n\t" \
  WL(9) MM(Q_Y, 0, 0) QRA(Q_X, 0, 1) WL(8) MM(Q_Y, 0, 1) QRB(Q_X, 0, 0, 1) WL(8) MM(Q_Y, 1, 0) QRB(Q_X, 0, 1, 1) MM(Q_Y, 1, 1) QRB(Q_X, 1, 0, 1) \
  WL(8) MM(Q_Y, 0, 2) QRB(Q_X, 1, 1, 1) MM(Q_Y, 1, 2) QRA(Q_X, 1, 1) WL(8) MM(Q_Y, 0, 3) QRB(Q_X, 2, 0, 1) MM(Q_Y, 1, 3) QRB(Q_X, 2, 1, 1) \
  WL(9) MM(Q_Y, 2, 0) QRB(Q_X, 3, 0, 1) MM(Q_Y, 2, 1) QRB(Q_X, 3, 1, 1) MM(Q_Y, 2, 2) QRA(Q_X, 2, 1) MM(Q_Y, 2, 3) QRA(Q_X, 3, 1) \
  WL(12) MM(Q_Y, 3, 0) "s_add_u32 %[st], %[st], 1\n\ts_and_b32 %[dl], %[st], 3\n\ts_cselect_b32 %[dl], 32768, %[wr]\n\t" \
  MM(Q_Y, 3, 1) QAD(0) QAD(1) MM(Q_Y, 3, 2) QAD(2) QAD(3) MM(Q_Y, 3, 3) QAD(4) QAD(5) \
  WL(7) MM(Q_X, 0, 0) "s_cmp_eq_u32 %[st], 12\n\t" WL(0) MM(Q_X, 0, 1) "s_cbranch_scc1 3f\n\t" \
  "s_barrier\n\ts_cmp_lg_u32 %[st], %[fk]\n\ts_cbranch_scc1 1b\n\t" \
  "s_mov_b64 exec, 1\n\tbuffer_store_dwordx2 v[168:169], v166, %[rs], 0 offen sc1\n\ts_mov_b64 exec, -1\n\ts_branch 1b\n" \
  "3:\n\t" \
  MM(Q_X, 1, 0) MM(Q_X, 1, 1) MM(Q_X, 0, 2) MM(Q_X, 1, 2) MM(Q_X, 0, 3) MM(Q_X, 1, 3) MM(Q_X, 2, 0) MM(Q_X, 2, 1) \
  MM(Q_X, 2, 2) MM(Q_X, 2, 3) MM(Q_X, 3, 0) MM(Q_X, 3, 1) MM(Q_X, 3, 2) MM(Q_X, 3, 3) \
  "s_nop 7"
#define P1_OPERANDS \
  : "+{v[32:63]}"(ac0), "+{v[64:95]}"(ac1), "+{v160}"(aoff[0]), "+{v161}"(aoff[1]), "+{v162}"(boff[0]), "+{v163}"(boff[1]), "+{v164}"(boff[2]), "+{v165}"(boff[3]), \
    [st] "+&s"(st), [dl] "=&s"(dl) \
  : [wr] "s"(-98304), [fk] "s"(w == 0 ? 5 : 0), [rs] "s"(c.rs), "{v166}"(WS_F0 + c.b * 8), "{v[168:169]}"(c.tag) \
  : "memory", "scc", CL8(96, 97, 98, 99, 100, 101, 102, 103), CL8(104, 105, 106, 107, 108, 109, 110, 111), CL8(112, 113, 114, 115, 116, 117, 118, 119), CL8(120, 121, 122, 123, 124, 125, 126, 127), \
    CL8(128, 129, 130, 131, 132, 133, 134, 135), CL8(136, 137, 138, 139, 140, 141, 142, 143), CL8(144, 145, 146, 147, 148, 149, 150, 151), CL8(152, 153, 154, 155, 156, 157, 158, 159)
template <bool VT>
DI void p1_compute(const Ctx& c) {
  const int l = c.l, w = c.w, wm = w >> 1, wn = w & 1, t = l & 15, g = l >> 4, q = t >> 2, pp = l & 3;
  unsigned aoff[2], boff[4];
#pragma unroll
  for (int i = 0; i < 2; ++i) aoff[i] = c.lds0 + 65536 + (wm * 64 + t) * 128 + (((4 * i + g) ^ (t >> 1)) << 4);
#pragma unroll
  for (int n = 0; n < 4; ++n) boff[n] = c.lds0 + 65536 + 16384 + (8 * g + q) * 256 + (((wn * 8 + 2 * n + (pp >> 1)) ^ ((q << 2) | ((g & 1) << 1))) << 4) + 8 * (pp & 1);
  f32x32 ac0 = {}, ac1 = {};
  unsigned st = 2, dl;
  if (VT) asm volatile(P1_LOOP(QMV) P1_OPERANDS); else asm volatile(P1_LOOP(QMK) P1_OPERANDS);
  LAS float* ra = (LAS float*)(c.lds + L1_FIN); LAS float* rb = ra + 128; LAS float* cs = ra + 256;
  unsigned base[2];
#pragma unroll
  for (int i = 0; i < 2; ++i) {
    const int f = (c.ct - 10) * 128 + wn * 64 + (2 * i + (g & 1)) * 16 + t, h = f / 20, d = f - 20 * h, dd = (h & 1) ? (d < 4 ? d + 16 : d - 4) : d;
    base[i] = VT ? WS_V + (h * 256 + c.panel * 16 + wm * 8 + (g >> 1)) * 320 + dd * 16 : WS_QK + ((c.ct * 16 + wn * 8 + (g >> 1)) * 2048 + c.panel * 128 + wm * 64 + (2 * i + (g & 1)) * 16 + t) * 16;
  }
  asm volatile("" : "+v"(base[0]), "+v"(base[1]));
  LDS_BAR();
  STAMP(3);
  float s0[4], s1[4]; f32x4 u4[4], v4[4];
#pragma unroll
  for (int i = 0; i < 4; ++i) {
    s0[i] = VT ? cs[wn * 64 + 16 * i + t] : ra[wm * 64 + 16 * i + t]; s1[i] = VT ? 0.f : rb[wm * 64 + 16 * i + t];
    u4[i] = *(LAS f32x4*)(VT ? ra + wm * 64 + 16 * i + 4 * g : cs + wn * 64 + 16 * i + 4 * g);
    v4[i] = VT ? *(LAS f32x4*)(rb + wm * 64 + 16 * i + 4 * g) : f32x4{};
  }
  __builtin_amdgcn_sched_barrier(0);
#pragma unroll
  for (int o = 0; o < 4; ++o)
#pragma unroll
    for (int ip = 0; ip < 2; ++ip) {
      unsigned pk[2][2];
#pragma unroll
      for (int j = 0; j < 2; ++j) {
        const int i2 = 2 * ip + j, m = VT ? o : i2, n = VT ? i2 : o; float y[4];
#pragma unroll
        for (int i = 0; i < 4; ++i) {
          const float a = m < 2 ? ac0[m * 16 + n * 4 + i] : ac1[(m - 2) * 16 + n * 4 + i];
          y[i] = VT ? fmaf(a, u4[o][i], v4[o][i] * s0[i2]) : fmaf(a, s0[i2], s1[i2] * u4[o][i]);
        }
        pk[j][0] = cvtpk(y[0], y[1]); pk[j][1] = cvtpk(y[2], y[3]);
      }
      st128_d(c.rs, base[ip] + o * (VT ? 2 * 320 : 2 * 32768), np(swap16_pair(pk[0][0], pk[0][1], pk[1][0], pk[1][1])));
    }
}

#define EX(b, i) "v_exp_f32 v[" STR(b) "+" STR(i) "], v[" STR(b) "+" STR(i) "]\n\t"
#define EX3(b, i) EX(b, i) EX(b, i + 1) EX(b, i + 2)
#define EX4(b, i) EX3(b, i) EX(b, i + 3)
#define R_SA 32
#define R_SB 64
#define R_OA 96
#define R_OB 112
#define R_NA 128
#define R_NB 144
#define R_QA 160
#define R_QB 164
#define R_QR 168
#define R_KR 170
#define R_K 172
#define R_VA 176
#define R_VB 184
#define R_IDX 192
#define R_KAD 193
#define R_KRAD 194
#define R_VAD 195
#define PV(o, vreg, sreg) "v_smfmac_f32_32x32x32_bf16 " VR(o, 16) ", " VR(vreg, 4) ", " VR(sreg, 8) ", v" STR(R_IDX) "\n\t"
#define QK16(dst, q, nm) "v_mfma_f32_32x32x16_bf16 " VR(dst, 16) ", " VR(R_K, 4) ", " VR(q, 4) ", " VR(nm, 16) "\n\t"
#define STEP(C, N, VC, VN, AB, i, MID, KRLOAD) \
  QK16(N, R_QA, R_NA) \
  MID \
  EX4(C, 0) \
  PV(R_OB, VN + 4, N + 24) \
  "ds_read_b128 " VR(VN, 4) ", v" STR(R_VAD) " offset:" STR(i * 1408) "\n\t" \
  "ds_read_b128 " VR(VN + 4, 4) ", v" STR(R_VAD) " offset:" STR(i * 1408 + 704) "\n\t" \
  EX3(C, 4) \
  QK16(N + 16, R_QB, R_NB) \
  "ds_read_b128 " VR(R_K, 4) ", v" STR(R_KAD) " offset:" STR((i / 2) * 3072 + (i & 1) * 512) "\n\t" \
  EX4(C, 7) \
  PV(R_OA, VC, C) \
  "s_waitcnt lgkmcnt(3)\n\t" \
  EX4(C, 11) \
  "v_mfma_f32_32x32x4_2b_bf16 " VR(N, 32) ", " VR(R_KR, 2) ", " VR(R_QR, 2) ", " VR(N, 32) " cbsz:1 abid:" STR(AB) "\n\t" \
  EX4(C, 15) EX4(C, 19) \
  PV(R_OA, VC + 4, C + 8) \
  "s_waitcnt lgkmcnt(0)\n\t" \
  KRLOAD \
  EX4(C, 23) \
  PV(R_OB, VC, C + 16) \
  EX4(C, 27) EX(C, 31)
#define KRL(i) "ds_read_b64 " VR(R_KR, 2) ", v" STR(R_KRAD) " offset:" STR((i / 2) * 3072) "\n\t"
#define STAGE_TOP \
  "s_barrier\n\t" \
  "v_add_u32 v" STR(R_KAD) ", %[dk], v" STR(R_KAD) "\n\tv_add_u32 v" STR(R_KRAD) ", %[dk], v" STR(R_KRAD) "\n\tv_add_u32 v" STR(R_VAD) ", %[dv], v" STR(R_VAD) "\n\t" \
  "s_sub_u32 %[dk], 0, %[dk]\n\ts_sub_u32 %[dv], 0, %[dv]\n\t"

DI f32x32 cat16(const f32x16& a, const f32x16& b) {
  return __builtin_shufflevector(a, b, 0, 1, 2, 3, 4, 5, 6, 7, 8, 9, 10, 11, 12, 13, 14, 15, 16, 17, 18, 19, 20, 21, 22, 23, 24, 25, 26, 27, 28, 29, 30, 31);
}
DI float max16(const f32x16& s) { float m = s[0];
#pragma unroll
  for (int i = 1; i < 16; ++i) m = fmaxf(m, s[i]);
  return m; }
DI f32x16 splat16(float v) { f32x16 s;
#pragma unroll
  for (int i = 0; i < 16; ++i) s[i] = v;
  return s; }
DI bool out_of_range(float sum) { return !(sum > 0.f && sum < 1e30f); }

DI void p2_compute(const Ctx& c, u32x2 (&res)[5]) {
  const int l = c.l, w = c.w, r = l & 31, hh = l >> 5, h = c.h;
  const int e = h & 1, cb = 5 * (h >> 1) + 2 * e, q0 = c.qb * 256 + w * 64;
  unsigned kad = c.lds0 + KRING + 12288 + (e + hh) * 1024 + r * 16, krad = c.lds0 + KRING + 12288 + (e ? 0 : 2048) + l * 16 + 8 * e;
  unsigned vad = c.lds0 + VRING + 11264 + hh * 352 + (r < 21 ? r : 21) * 16;
  LDS_BAR();
  __builtin_amdgcn_s_sleep(QDELAY);
  const unsigned ko = WS_QK + ((80 + cb + e + hh) * 2048 + r) * 16, kro = WS_QK + ((80 + cb + (e ? 0 : 2)) * 2048 + r) * 16 + 8 * e;
  const unsigned vg = WS_V + (h * 256 + hh) * 320 + (r < 20 ? r : 0) * 16, pad = r == 20 ? ONES2 : 0u;
  s16x8 qa, qb, kf; s16x4 qr, kr; u32x4 k0, t0, t1;
#pragma unroll 1
  for (unsigned n = 0; n < SPIN_CAP; ++n) {
    FENCE();
    const u32x4 ta = ld128_d(c.rs, WS_QK + ((cb + e + hh) * 2048 + q0 + r) * 16), tb = ld128_d(c.rs, WS_QK + ((cb + e + hh) * 2048 + q0 + 32 + r) * 16);
    const u32x2 tr = ld64_d(c.rs, WS_QK + ((e ? cb : cb + 2) * 2048 + q0 + l) * 16 + 8 * e), tk = ld64_d(c.rs, kro + hh * 512);
    k0 = ld128_d(c.rs, ko); const u32x4 k1 = ld128_d(c.rs, ko + 512);
    t0 = ld128_d(c.rs, vg); t1 = ld128_d(c.rs, vg + 640);
    if (n == 0) publish<8>(c, L_CNT1, 4, WS_F1 + c.b * 8);
    qa = __builtin_bit_cast(s16x8, ta); qb = __builtin_bit_cast(s16x8, tb); qr = __builtin_bit_cast(s16x4, tr);
    kf = __builtin_bit_cast(s16x8, k1); kr = __builtin_bit_cast(s16x4, tk);
    if (!__any(umax(umax(umax(umax4(ta), umax4(tb)), umax(umax4(k0), umax4(k1))), umax(umax(umax4(t0), umax4(t1)), umax(umax(tr[0], tr[1]), umax(tk[0], tk[1])))) == POISON)) break;
    __builtin_amdgcn_s_sleep(RDELAY);
  }
  STAMP(5);
  f32x32 sa = {}, sb = {};
  sa = __builtin_amdgcn_mfma_f32_32x32x4bf16_1k(kr, qr, sa, 1, 0, 0);
  f32x16 s0 = mfma32(__builtin_bit_cast(s16x8, k0), qa, lo16(sa)), s1 = mfma32(__builtin_bit_cast(s16x8, k0), qb, hi16(sa));
  f32x8 va = {}, vb = {};
#pragma unroll
  for (int i = 0; i < 4; ++i) { va[i] = __uint_as_float(r < 20 ? t0[i] : pad); va[4 + i] = __uint_as_float(r < 20 ? t1[i] : pad); }
  f32x16 na = splat16(-swap32_max(max16(s0))), nb = splat16(-swap32_max(max16(s1))), oa = {}, ob = {};
  sa = cat16(s0 + na, s1 + nb);
  unsigned idx = 0xDDDDDDDDu, cnt = 8, dk = -12288, dv = -11264;
  {
    const char* xg = c.p.x + (size_t)((c.rt * 64 + 32 * (w & 1) + 16 * (w >> 1) + (l & 15)) * 1280 + c.cs * 160 + (l >> 4) * 8);
#pragma unroll
    for (int nt = 0; nt < 5; ++nt) res[nt] = *(const GAS u32x2*)(xg + nt * 32);
  }
  STAMP(7);
  asm volatile(
      "s_waitcnt lgkmcnt(0)\n\ts_nop 4\n"
      "1:\n\t"
      STEP(R_SA, R_SB, R_VA, R_VB, 1, 0, STAGE_TOP, KRL(0))
      STEP(R_SB, R_SA, R_VB, R_VA, 0, 1, "", "")
      STEP(R_SA, R_SB, R_VA, R_VB, 1, 2, "", KRL(2))
      STEP(R_SB, R_SA, R_VB, R_VA, 0, 3, "", "")
      STEP(R_SA, R_SB, R_VA, R_VB, 1, 4, "", KRL(4))
      STEP(R_SB, R_SA, R_VB, R_VA, 0, 5, "", "")
      STEP(R_SA, R_SB, R_VA, R_VB, 1, 6, "", KRL(6))
      STEP(R_SB, R_SA, R_VB, R_VA, 0, 7, "", "")
      "s_sub_u32 %[cnt], %[cnt], 1\n\t"
      "s_cmp_lg_u32 %[cnt], 0\n\t"
      "s_cbranch_scc1 1b\n\t"
      "s_waitcnt lgkmcnt(0)\n\t"
      "s_barrier\n\t"
      PV(R_OB, R_VB + 4, R_SB + 24)
      "s_nop 15\n\t"
      : "+{v[32:63]}"(sa), "+{v[64:95]}"(sb), "+{v[96:111]}"(oa), "+{v[112:127]}"(ob), "+{v[170:171]}"(kr), "+{v[172:175]}"(kf), "+{v[176:183]}"(va), "+{v[184:191]}"(vb),
        "+{v193}"(kad), "+{v194}"(krad), "+{v195}"(vad), [cnt] "+s"(cnt), [dk] "+s"(dk), [dv] "+s"(dv)
      : "{v[128:143]}"(na), "{v[144:159]}"(nb), "{v[160:163]}"(qa), "{v[164:167]}"(qb), "{v[168:169]}"(qr), "{v192}"(idx)
      : "memory", "scc");
  STAMP(8);
  float la = upper_half(oa[8]), lb = upper_half(ob[8]);
  if (__any(out_of_range(la) || out_of_range(lb))) {
    float ma = -INFINITY, mb = -INFINITY;
#pragma unroll 1
    for (int pass = 0; pass < 2; ++pass) {
      na = splat16(pass ? -ma : 0.f); nb = splat16(pass ? -mb : 0.f); oa = splat16(0.f); ob = splat16(0.f);
#pragma unroll 1
      for (int t = 0; t < 64; ++t) {
        const s16x8 k16 = __builtin_bit_cast(s16x8, ld128_d(c.rs, ko + t * 512));
        f32x32 s = cat16(mfma32(k16, qa, na), mfma32(k16, qb, nb));
        s = __builtin_amdgcn_mfma_f32_32x32x4bf16_1k(__builtin_bit_cast(s16x4, ld64_d(c.rs, kro + t * 512)), qr, s, 0, 0, 0);
        if (pass == 0) { ma = fmaxf(ma, max16(lo16(s))); mb = fmaxf(mb, max16(hi16(s))); }
        else {
#pragma unroll
          for (int g = 0; g < 2; ++g) {
            u32x4 vf = ld128_d(c.rs, vg + (4 * t + 2 * g) * 320);
            if (r >= 20) vf = u32x4{pad, pad, pad, pad};
            f32x8 pa, pb;
#pragma unroll
            for (int i = 0; i < 8; ++i) { pa[i] = __builtin_amdgcn_exp2f(s[8 * g + i]); pb[i] = __builtin_amdgcn_exp2f(s[16 + 8 * g + i]); }
            oa = __builtin_amdgcn_smfmac_f32_32x32x32_bf16(__builtin_bit_cast(bf16x8, vf), __builtin_bit_cast(bf16x16, pa), oa, 0xDDDD, 0, 0);
            ob = __builtin_amdgcn_smfmac_f32_32x32x32_bf16(__builtin_bit_cast(bf16x8, vf), __builtin_bit_cast(bf16x16, pb), ob, 0xDDDD, 0, 0);
          }
        }
      }
      if (pass == 0) { ma = swap32_max(ma); mb = swap32_max(mb); }
    }
    la = upper_half(oa[8]); lb = upper_half(ob[8]);
  }
#pragma unroll
  for (int t = 0; t < 2; ++t) {
    const f32x16& o = t ? ob : oa;
    const float rl = __builtin_amdgcn_rcpf(t ? lb : la);
    unsigned pk[6];
#pragma unroll
    for (int i = 0; i < 6; ++i) pk[i] = cvtpk(o[2 * i] * rl, o[2 * i + 1] * rl);
    const unsigned og = WS_O + (q0 >> 6) * 81920 + (32 * t + r) * 16;
    st128_d(c.rs, og + (cb + e + hh) * 1024, np(swap32_pair(pk[0], pk[1], pk[2], pk[3])));
    if (hh == 0) st64_d(c.rs, og + (e ? cb : cb + 2) * 1024 + 8 * e, u32x2{np(pk[4]), np(pk[5])});
  }
}

#define G_ACC 32
#define G_B0 72
#define G_B1 92
#define G_A 112
#define G_VB 176
#define G_VBO 177
#define G_VA0 178
#define G_VA1 180
#define G_CHK 182
#define GMM(m, nt, B, j) "v_mfma_f32_16x16x32_bf16 " VR(G_ACC + m * 20 + nt * 4, 4) ", " VR(B + nt * 4, 4) ", " VR(G_A + j * 8 + m * 4, 4) ", " VR(G_ACC + m * 20 + nt * 4, 4) "\n\t"
#define GRB(B, nt, ks) "ds_read_b64_tr_b16 " VR(B + nt * 4, 2) ", v" STR(G_VB) " offset:" STR(ks * 5120 + nt * 32) "\n\t" \
                       "ds_read_b64_tr_b16 " VR(B + nt * 4 + 2, 2) ", v" STR(G_VB) " offset:" STR(ks * 5120 + nt * 32 + 1280) "\n\t"
#define GCK(j, i) "v_max3_u32 v" STR(G_CHK) ", v[" STR(G_A + j * 8 + i * 2) "], v[" STR(G_A + j * 8 + i * 2 + 1) "], v" STR(G_CHK) "\n\t"
#define GLD1(j, m, va) "buffer_load_dwordx4 " VR(G_A + (j) * 8 + m * 4, 4) ", v[" STR(va + m) "], %[rs], %[so] offen sc1\n\t"
#define GAD0 "s_add_u32 %[so], %[so], 16384\n\t"
#define GLD2(j) GLD1(j, 0, G_VA0) GLD1(j, 1, G_VA0) GLD1(j + 1, 0, G_VA1) GLD1(j + 1, 1, G_VA1)
#define GLD2A(j) GLD2(j) GAD0
#define GNX0 "s_add_u32 %[sl], %[sl], 20480\n\t"
#define GNX1 "s_cmp_eq_u32 %[sl], 163840\n\t"
#define GNX2 "s_cselect_b32 %[sl], 0, %[sl]\n\t"
#define GADR "v_add_u32 v" STR(G_VB) ", %[sl], v" STR(G_VBO) "\n\t"
#define GBODY(j, BC, BN, ks, VM, W0, PRE, S0, S1, S2, L0, L1) \
  "s_waitcnt vmcnt(" STR(VM) ") lgkmcnt(" STR(W0) ")\n\t" PRE \
  GMM(0, 0, BC, j) GRB(BN, 0, ks) GMM(1, 0, BC, j) GRB(BN, 1, ks) GMM(0, 1, BC, j) GRB(BN, 2, ks) GMM(1, 1, BC, j) GRB(BN, 3, ks) \
  GMM(0, 2, BC, j) GRB(BN, 4, ks) GMM(1, 2, BC, j) S0 WL(10) GMM(0, 3, BC, j) GCK(j, 0) S1 GMM(1, 3, BC, j) GCK(j, 1) S2 \
  GMM(0, 4, BC, j) GCK(j, 2) L0 GMM(1, 4, BC, j) GCK(j, 3) L1
#define GEVEN(j) GBODY(j, G_B0, G_B1, 1, 14, 4, "", GAD0, "", "", GLD1(j, 0, G_VA0), GLD1(j, 1, G_VA0))
#define GODD(j) GBODY(j, G_B1, G_B0, 0, 14, 4, GADR, GNX0, GNX1, GNX2, GLD1(j, 0, G_VA1), GLD1(j, 1, G_VA1))
#define GODDB(j) GBODY(j, G_B1, G_B0, 0, 14, 0, "s_barrier\n\t" GADR, GNX0, GNX1, GNX2, GLD1(j, 0, G_VA1), GLD1(j, 1, G_VA1))
#define TEVEN(j) GBODY(j, G_B0, G_B1, 1, 14 - 2 * j, 4, "", "", "", "", "", "")
#define TODD(j) GBODY(j, G_B1, G_B0, 0, 14 - 2 * j, 4, GADR, GNX0, GNX1, GNX2, "", "")

DI void g80_compute(const Ctx& c, int last, u32x2 (&res)[5]) {
  int l = c.l; asm volatile("" : "+v"(l));
  const int w = c.w, wm = w & 1, wk = w >> 1, g = l >> 4, q = (l & 15) >> 2, pp = l & 3;
  const unsigned tile = last ? WS_F + c.rt * 196608 : WS_O + c.rt * 81920;
  __builtin_amdgcn_s_barrier();
  const int row = 32 * wm + 16 * wk + (l & 15);
  f32x32 acc0 = {}; f32x8 acc1;
#pragma unroll
  for (int i = 0; i < 20; ++i) { const float v = (i & 1) ? bfhi(res[i >> 2][(i >> 1) & 1]) : bflo(res[i >> 2][(i >> 1) & 1]); if (i < 12) acc0[20 + i] = v; else acc1[i - 12] = v; }
  asm volatile("" : "+{v[32:63]}"(acc0), "+{v[64:71]}"(acc1));
  __builtin_amdgcn_s_sleep(PDELAY);
  {
    const int hd = l & 31, e = hd & 1, cb = 5 * (hd >> 1) + 2 * e;
    const unsigned po = tile + (last ? ((hd >> 2) * 24 + (hd & 3) * 6 + 5) * 1024 + 63 * 16 : (e ? cb : cb + 2) * 1024 + 63 * 16 + 8 * e);
#pragma unroll 1
    for (unsigned n = 0; n < SPIN_CAP; ++n) { FENCE(); if (!__any(ld32_d(c.rs, po) == POISON)) break; __builtin_amdgcn_s_sleep(PSLEEP); }
  }
  if (last) STAMP(16); else STAMP(10);
  const unsigned va0 = tile + (8 * wk + g) * 1024 + row * 16;
  unsigned chk = 0, so = 0, sl = last ? 0 : L_WO, cnt = last ? 2 : 1;
  asm volatile(
      "s_cmp_eq_u32 %[cnt], 1\n\ts_cbranch_scc0 4f\n\t"
      GLD2A(6)
      "4:\n\t"
      GLD2A(0) GLD2A(2) TS(0)
      "s_barrier\n\t" TS(1)
      GADR
      GRB(G_B0, 0, 0) GRB(G_B0, 1, 0) GRB(G_B0, 2, 0) GRB(G_B0, 3, 0) GRB(G_B0, 4, 0)
      GNX0 GNX1 GNX2
      GLD2(4)
      "s_cmp_eq_u32 %[cnt], 1\n\ts_cbranch_scc1 2f\n\t"
      GAD0 GLD2(6)
      "1:\n\t"
      GEVEN(0) GODD(1) GEVEN(2) GODD(3) GEVEN(4) GODD(5)
      "2:\n\t"
      GEVEN(6)
      GODDB(7)
      "s_sub_u32 %[cnt], %[cnt], 1\n\ts_cmp_lg_u32 %[cnt], 0\n\ts_cbranch_scc1 1b\n\t"
      TEVEN(0) TODD(1) TEVEN(2) TODD(3) TEVEN(4) TODD(5) TEVEN(6)
      "s_waitcnt vmcnt(0) lgkmcnt(0)\n\t"
      GMM(0, 0, G_B1, 7) GMM(1, 0, G_B1, 7) GCK(7, 0) GMM(0, 1, G_B1, 7) GCK(7, 1) GMM(1, 1, G_B1, 7) GCK(7, 2) GMM(0, 2, G_B1, 7) GCK(7, 3)
      GMM(1, 2, G_B1, 7) GMM(0, 3, G_B1, 7) GMM(1, 3, G_B1, 7) GMM(0, 4, G_B1, 7) GMM(1, 4, G_B1, 7)
      "s_nop 15\n\t" TS(5)
      : "+{v[32:63]}"(acc0), "+{v[64:71]}"(acc1), "+{v182}"(chk), [so] "+&s"(so), [sl] "+&s"(sl), [cnt] "+&s"(cnt)
      : "{v177}"(c.lds0 + (64 * wk + 16 * (g >> 1) + 4 * (g & 1) + q) * 160 + pp * 8), "{v178}"(va0 ^ 256), "{v179}"(va0), "{v180}"((va0 ^ 256) + 4096), "{v181}"(va0 + 4096), [rs] "s"(c.rs)
      : "memory", "scc", "v176", CL8(160, 161, 162, 163, 164, 165, 166, 167), CL8(168, 169, 170, 171, 172, 173, 174, 175),
        "v72", "v73", "v74", "v75", "v76", "v77", "v78", "v79", "v80", "v81", "v82", "v83", "v84", "v85", "v86", "v87", "v88", "v89", "v90", "v91", "v92", "v93", "v94", "v95",
        "v96", "v97", "v98", "v99", "v100", "v101", "v102", "v103", "v104", "v105", "v106", "v107", "v108", "v109", "v110", "v111", "v112", "v113", "v114", "v115", "v116", "v117", "v118", "v119",
        "v120", "v121", "v122", "v123", "v124", "v125", "v126", "v127", "v128", "v129", "v130", "v131", "v132", "v133", "v134", "v135", "v136", "v137", "v138", "v139", "v140", "v141", "v142", "v143",
        "v144", "v145", "v146", "v147", "v148", "v149", "v150", "v151", "v152", "v153", "v154", "v155", "v156", "v157", "v158", "v159" TS_CLOB);
  if (last) { TS_PUT(0, 25); TS_PUT(1, 26); TS_PUT(2, 27); TS_PUT(3, 28); TS_PUT(4, 29); TS_PUT(5, 30); } else { TS_PUT(0, 19); TS_PUT(1, 20); TS_PUT(4, 21); TS_PUT(5, 22); }
  f32x4 acc[2][5];
#pragma unroll
  for (int i = 0; i < 40; ++i) acc[i / 20][(i % 20) / 4][i % 4] = i < 32 ? acc0[i] : acc1[i - 32];
  if (__any(chk == POISON)) {
    const char* wg = (last ? c.p.w2 : c.p.wo) + c.cs * 160 + (l & 15) * 2;
#pragma unroll
    for (int nt = 0; nt < 5; ++nt) { acc[0][nt] = f32x4{}; acc[1][nt] = f32x4{bflo(res[nt][0]), bfhi(res[nt][0]), bflo(res[nt][1]), bfhi(res[nt][1])}; }
#pragma unroll 1
    for (int st = 0; st < (last ? 24 : 10); ++st) {
      u32x4 a0, a1;
#pragma unroll 1
      for (unsigned n = 0; n < SPIN_CAP; ++n) {
        FENCE();
        a0 = ld128_d(c.rs, (va0 ^ 256) + (st >> 1) * 16384 + (st & 1) * 4096); a1 = ld128_d(c.rs, va0 + (st >> 1) * 16384 + (st & 1) * 4096);
        if (!__any(umax(umax4(a0), umax4(a1)) == POISON)) break;
      }
      const char* wr = wg + (size_t)((st >> 1) * 128 + 64 * wk + (st & 1) * 32 + 8 * g) * 1280;
#pragma unroll
      for (int nt = 0; nt < 5; ++nt) {
        s16x8 bf;
#pragma unroll
        for (int j = 0; j < 8; ++j) bf[j] = *(const GAS short*)(wr + j * 1280 + nt * 32);
        acc[0][nt] = mfma16(bf, __builtin_bit_cast(s16x8, a0), acc[0][nt]); acc[1][nt] = mfma16(bf, __builtin_bit_cast(s16x8, a1), acc[1][nt]);
      }
    }
  }
  lptr ex = c.lds + (last ? L_EX : L_WO) + l * 16;
#pragma unroll
  for (int nt = 0; nt < 5; ++nt) *(LAS f32x4*)(ex + w * 5120 + nt * 1024) = acc[0][nt];
  const rsrc_t ro = __builtin_amdgcn_make_buffer_rsrc(last ? c.p.out : c.p.ws + WS_H, 0, S * D * 2, 0x00020000);
  const unsigned ob = c.rt * 81920 + (last ? c.cs * 160 + row * 1280 : c.cs * 10240 + row * 16), oc = last ? 16 : 1024;
  unsigned so3[3];
#pragma unroll
  for (int i = 0; i < 3; ++i) so3[i] = ob + (4 * i + (i < 2 ? 2 * (g & 1) : 0) + (g >> 1)) * oc;
  asm volatile("" : "+v"(so3[0]), "+v"(so3[1]), "+v"(so3[2]));
  LDS_BAR();
  if (last) STAMP(17); else STAMP(11);
  f32x4 o4[5];
#pragma unroll
  for (int nt = 0; nt < 5; ++nt) o4[nt] = *(LAS f32x4*)(ex + (w ^ 2) * 5120 + nt * 1024);
  __builtin_amdgcn_sched_barrier(0);
#pragma unroll
  for (int nt = 0; nt < 5; ++nt) { const f32x4 u = acc[1][nt] + o4[nt]; res[nt] = u32x2{np(cvtpk(u[0], u[1])), np(cvtpk(u[2], u[3]))}; }
  u32x4 pc[3];
#pragma unroll
  for (int i = 0; i < 3; ++i) { const int n1 = i < 2 ? 2 * i + 1 : 4; pc[i] = swap16_pair(res[2 * i][0], res[2 * i][1], res[n1][0], res[n1][1]); }
  if (last) {
#pragma unroll
    for (int i = 0; i < 3; ++i) __builtin_amdgcn_raw_buffer_store_b128(pc[i], ro, so3[i], 0, 0);
  } else {
#pragma unroll
    for (int i = 0; i < 3; ++i) st128_d(ro, so3[i], pc[i]);
    STAMP(12);
    __builtin_amdgcn_s_barrier();
    float s1 = 0.f, s2 = 0.f;
#pragma unroll
    for (int nt = 0; nt < 5; ++nt) { s1 = dot2(res[nt][0], ONES2, dot2(res[nt][1], ONES2, s1)); s2 = dot2(res[nt][0], res[nt][0], dot2(res[nt][1], res[nt][1], s2)); }
    s1 = swap16_add(s1, s1); s2 = swap16_add(s2, s2); s1 = swap32_add(s1, s1); s2 = swap32_add(s2, s2);
    if (l < 16) st64_d(c.rs, WS_RS2 + (c.rt * 8 + c.cs) * 512 + row * 8, u32x2{np(__float_as_uint(s1)), np(__float_as_uint(s2))});
  }
}

#define F_X 80
#define F_Y 108
#define FMM(S, nt, m) "v_mfma_f32_16x16x32_bf16 " VR(32 + nt * 16 + m * 4, 4) ", " VR(S + nt * 4, 4) ", " VR(S + 12 + m * 4, 4) ", " VR(32 + nt * 16 + m * 4, 4) "\n\t"
#define FRA(S, m, ks) "ds_read_b128 " VR(S + 12 + m * 4, 4) ", v140 offset:" STR(ks * 4096 + m * 256) "\n\t"
#define FRB(S, nt, h, ks) "ds_read_b64_tr_b16 " VR(S + nt * 4 + h * 2, 2) ", v[141+" STR(nt) "] offset:" STR(ks * 12288 + h * 3072) "\n\t"
#define FRF(m) "ds_read2st64_b32 v[144+2*" #m ":145+2*" #m "], v152 offset0:0 offset1:1\n\tv_add_u32 v152, 64, v152\n\t"
DI void p4_compute(const Ctx& c) {
  int l = c.l; asm volatile("" : "+v"(l));
  const int w = c.w, g = l >> 4, q = (l & 15) >> 2, pp = l & 3;
  unsigned boff[3];
#pragma unroll
  for (int nt = 0; nt < 3; ++nt) boff[nt] = c.lds0 + (16 * (g >> 1) + 4 * (g & 1) + q) * 384 + (((3 * w + nt) ^ (2 * (g & 1) + (q >> 1))) * 32) + pp * 8;
  f32x32 ac0 = {}; f32x16 ac1 = {}; f32x8 ab;
  unsigned ch = 0, sa, sb = 0, rap = c.lds0 + L_RA + (l & 15) * 4;
  f32x4 c4[3];
#pragma unroll
  for (int nt = 0; nt < 3; ++nt) c4[nt] = *(LAS f32x4*)(c.lds + L_CSUM + (w * 48 + nt * 16 + 4 * g) * 4);
  asm volatile("" : "+v"(c4[0]), "+v"(c4[1]), "+v"(c4[2]));
  asm volatile(
      "v_mov_b32 v140, v136\n\tv_mov_b32 v141, v137\n\tv_mov_b32 v142, v138\n\tv_mov_b32 v143, v139\n\t"
      "s_barrier\n\t" TS(0)
      FRB(F_Y, 0, 0, 0) FRB(F_Y, 0, 1, 0) FRA(F_Y, 0, 0) FRA(F_Y, 1, 0) FRA(F_Y, 2, 0) FRA(F_Y, 3, 0) FRB(F_Y, 1, 0, 0) FRB(F_Y, 1, 1, 0) FRB(F_Y, 2, 0, 0) FRB(F_Y, 2, 1, 0)
      "s_branch 2f\n"
      "1:\n\t"
      FMM(F_X, 0, 2) FRB(F_Y, 0, 0, 0) FMM(F_X, 0, 3) FRB(F_Y, 0, 1, 0) FMM(F_X, 1, 0) FRA(F_Y, 0, 0) FMM(F_X, 1, 1) FRA(F_Y, 1, 0) FMM(F_X, 1, 2) FRA(F_Y, 2, 0)
      FMM(F_X, 1, 3) FRA(F_Y, 3, 0) FMM(F_X, 2, 0) FRB(F_Y, 1, 0, 0) FMM(F_X, 2, 1) FRB(F_Y, 1, 1, 0) FMM(F_X, 2, 2) FRB(F_Y, 2, 0, 0) FMM(F_X, 2, 3) FRB(F_Y, 2, 1, 0)
      "2:\n\t"
      WL(7) FMM(F_Y, 0, 0) FRB(F_X, 0, 0, 1) WL(7) FMM(F_Y, 0, 1) FRB(F_X, 0, 1, 1) WL(7) FMM(F_Y, 0, 2) FRA(F_X, 0, 1) WL(7) FMM(F_Y, 0, 3) FRA(F_X, 1, 1)
      WL(6) FMM(F_Y, 1, 0) FRA(F_X, 2, 1) "s_add_u32 %[ch], %[ch], 1\n\t" FMM(F_Y, 1, 1) FRA(F_X, 3, 1) "s_add_u32 %[sb], %[sb], 24576\n\t"
      FMM(F_Y, 1, 2) FRB(F_X, 1, 0, 1) "s_cmp_eq_u32 %[ch], 5\n\t" FMM(F_Y, 1, 3) FRB(F_X, 1, 1, 1)
      WL(8) FMM(F_Y, 2, 0) FRB(F_X, 2, 0, 1) "s_cselect_b32 %[sb], 0, %[sb]\n\t" FMM(F_Y, 2, 1) FRB(F_X, 2, 1, 1) "s_and_b32 %[sa], %[ch], 3\n\t"
      FMM(F_Y, 2, 2) "s_lshl_b32 %[sa], %[sa], 13\n\tv_add_u32 v141, %[sb], v137\n\t" FMM(F_Y, 2, 3) "v_add_u32 v142, %[sb], v138\n\t"
      WL(7) FMM(F_X, 0, 0) "v_add_u32 v143, %[sb], v139\n\t" WL(6) FMM(F_X, 0, 1) "v_add_u32 v140, %[sa], v136\n\ts_cmp_eq_u32 %[ch], 10\n\t"
      WL(0) "s_barrier\n\t"
      "s_cbranch_scc0 1b\n\t"
      FMM(F_X, 0, 2) FRF(0) FMM(F_X, 0, 3) FRF(1) FMM(F_X, 1, 0) FRF(2) FMM(F_X, 1, 1) FRF(3) FMM(F_X, 1, 2) FMM(F_X, 1, 3) FMM(F_X, 2, 0) FMM(F_X, 2, 1) FMM(F_X, 2, 2) FMM(F_X, 2, 3)
      WL(0) "s_nop 7"
      : "+{v[32:63]}"(ac0), "+{v[64:79]}"(ac1), "={v[144:151]}"(ab), "+{v152}"(rap), [ch] "+s"(ch), [sa] "=&s"(sa), [sb] "+s"(sb)
      : "{v136}"(c.lds0 + L4_A + g * 1024 + (l & 15) * 16), "{v137}"(boff[0]), "{v138}"(boff[1]), "{v139}"(boff[2])
      : "memory", "scc", "v140", "v141", "v142", "v143", CL8(80, 81, 82, 83, 84, 85, 86, 87), CL8(88, 89, 90, 91, 92, 93, 94, 95), CL8(96, 97, 98, 99, 100, 101, 102, 103), CL8(104, 105, 106, 107, 108, 109, 110, 111),
        CL8(112, 113, 114, 115, 116, 117, 118, 119), CL8(120, 121, 122, 123, 124, 125, 126, 127), CL8(128, 129, 130, 131, 132, 133, 134, 135) TS_CLOB);
  TS_PUT(0, 13);
  f32x4 acc[4][3];
#pragma unroll
  for (int i = 0; i < 48; ++i) acc[(i >> 2) & 3][i >> 4][i & 3] = i < 32 ? ac0[i] : ac1[i - 32];
  STAMP(14);
#pragma unroll
  for (int nt = 0; nt < 3; ++nt)
#pragma unroll
    for (int mp = 0; mp < 2; ++mp) {
      unsigned pk[2][2];
#pragma unroll
      for (int j = 0; j < 2; ++j) {
        const int m = 2 * mp + j; float y[4];
#pragma unroll
        for (int i = 0; i < 4; ++i) y[i] = fmaf(acc[m][nt][i], ab[2 * m], ab[2 * m + 1] * c4[nt][i]);
        pk[j][0] = relu2(cvtpk(y[0], y[1])); pk[j][1] = relu2(cvtpk(y[2], y[3]));
      }
      st128_d(c.rs, WS_F + c.rt * 196608 + (c.cs * 24 + w * 6 + nt * 2 + (g >> 1)) * 1024 + (32 * mp + (g & 1) * 16 + (l & 15)) * 16, swap16_pair(pk[0][0], pk[0][1], pk[1][0], pk[1][1]));
    }
}

DI void compute_waves(Ctx& c) {
  const Params& p = c.p;
  read_tag(c);
  const int l = c.l, w = c.w, b = c.b, r = l & 31, hh = l >> 5;
  if (c.ct == 15) { __builtin_amdgcn_s_sleep(CPD); spare_poison(c); }
  {
    const int xrow = c.xr0 + 2 * w + hh, rg = c.wi & 7, cg = 15 * (b & 1) + (c.wi >> 3);
    LDS_BAR();
    lptr xs = c.lds + L1_XS + (2 * w + hh) * 1280 + r * 16;
    const u32x4 xv[3] = {*(LAS u32x4*)xs, *(LAS u32x4*)(xs + 512), *(LAS u32x4*)(xs + 1024)};
    const float v = colsum(c.lds + L1_WS, l, w);
    row_stats(c, xv, xrow);
    if (l < 16 && c.ct < 15) st64_d(c.rs, WS_CS1 + (rg * 1920 + cg * 64 + 16 * w + l) * 8, u32x2{__float_as_uint(v), (unsigned)c.tag});
    LDS_BAR();
    STAMP(1);
    if (c.ct < 10) p1_compute<false>(c);
    else if (c.ct < 15) p1_compute<true>(c);
    else publish<0>(c, L_CNT0 + 12, 8, WS_F0 + c.b * 8);
  }
  STAMP(4);
  u32x2 res[5];
  p2_compute(c, res);
  STAMP(9);
  int last = 0;
#pragma unroll 1
  for (;;) {
    asm volatile("" : "+s"(last));
    g80_compute(c, last, res);
    if (last) break;
    p4_compute(c);
    STAMP(15);
    asm volatile(".Lblk_end1:");
    last = 1;
  }
  STAMP(18);
  asm volatile(".Lblk_end2:");
}

struct Park { f32x32 a, b, c, d, e; f32x16 f; f32x4 g; };
#define PARK_REGS(m, pk) m "{v[76:107]}"(pk.a), m "{v[108:139]}"(pk.b), m "{v[140:171]}"(pk.c), m "{v[172:203]}"(pk.d), m "{v[204:235]}"(pk.e), m "{v[236:251]}"(pk.f), m "{v[252:255]}"(pk.g)
#define PKL1(k, j, o) "buffer_load_dwordx4 " VR(76 + k * 20 + j * 4, 4) ", %[v" #j "], %[rs], %[so] offen offset:" STR(o) "\n\t"
#define PKL(k, ch) "s_mov_b32 %[so], " STR(ch * 163840) "\n\t" PKL1(k, 0, 0) PKL1(k, 1, 1024) PKL1(k, 2, 2048) PKL1(k, 3, 3072) PKL1(k, 4, 0)
#define W80_OPS(r) [rs] "s"(r), [v0] "v"(vo80[0]), [v1] "v"(vo80[1]), [v2] "v"(vo80[2]), [v3] "v"(vo80[3]), [v4] "v"(vo80[4])
#define W80_DMA(r, dst, off) asm volatile("s_mov_b32 m0, %[d]\n\ts_nop 0\n\t" \
    "buffer_load_dwordx4 %[v0], %[rs], %[so] offen lds\n\tbuffer_load_dwordx4 %[v1], %[rs], %[so] offen offset:1024 lds\n\t" \
    "buffer_load_dwordx4 %[v2], %[rs], %[so] offen offset:2048 lds\n\tbuffer_load_dwordx4 %[v3], %[rs], %[so] offen offset:3072 lds\n\t" \
    "s_add_u32 m0, %[d], 4096\n\ts_nop 0\n\tbuffer_load_dwordx4 %[v4], %[rs], %[so] offen lds" :: [d] "s"(dst), [so] "s"(off), W80_OPS(r) : "memory", "scc")
#define PKS1(k, j) "ds_write_b128 %[va], " VR(76 + k * 20 + j * 4, 4) " offset:" STR(j * 1024) "\n\t"
#define PUT(k, slot) asm volatile(PKS1(k, 0) PKS1(k, 1) PKS1(k, 2) PKS1(k, 3) PKS1(k, 4) :: [va] "v"(wa + slot * 20480), PARK_REGS("", pk) : "memory")
#define P1_STATE [sa] "+&s"(sa), [sb] "+&s"(sb), [md] "+&s"(md)
#define P1_CONST [rx] "s"(rx), [rw] "s"(rw), [l0] "s"(lds0 + lw * 4096), [a0] "v"(va[0]), [a1] "v"(va[1]), [a2] "v"(va[2]), [a3] "v"(va[3]), [b0] "v"(vb[0]), [b1x] "v"(vb[1]), [b2] "v"(vb[2]), [b3] "v"(vb[3])
#define P1_LD(v, r, s, o) "buffer_load_dwordx4 %[" v "], %[" r "], %[" s "] offen offset:" #o " lds\n\t"
#define P1_STAGE \
  "s_mov_b32 m0, %[md]\n\ts_add_u32 %[md], %[md], 16384\n\t" \
  P1_LD("a0", "rx", "sa", 0) P1_LD("a1", "rx", "sa", 1024) P1_LD("a2", "rx", "sa", 2048) P1_LD("a3", "rx", "sa", 3072) \
  "s_mov_b32 m0, %[md]\n\ts_add_u32 %[sa], %[sa], 128\n\t" \
  P1_LD("b0", "rw", "sb", 0) P1_LD("b1x", "rw", "sb", 1024) P1_LD("b2", "rw", "sb", 2048) P1_LD("b3", "rw", "sb", 3072) \
  "s_add_u32 %[sb], %[sb], 245760\n\t" \
  "s_sub_u32 %[md], %[md], %[l0]\n\ts_add_u32 %[md], %[md], 16384\n\ts_and_b32 %[md], %[md], 0x18000\n\ts_add_u32 %[md], %[md], %[l0]\n\t"
#define W1_OPS [rw] "s"(rw1), [dw] "s"(dw), [v0] "v"(vo[0]), [v1] "v"(vo[1]), [v2] "v"(vo[2]), [v3] "v"(vo[3]), [v4] "v"(vo[4]), [v5] "v"(vo[5])
#define LW1(v, o) "buffer_load_dwordx4 %[" v "], %[rw], %[t0] offen offset:" STR(o) " lds\n\t"
#define LW3_0 LW1("v0", 0) LW1("v1", 1024) LW1("v2", 2048)
#define LW3_1 LW1("v3", 0) LW1("v4", 1024) LW1("v5", 2048)
#define LW3(ch, u) "s_add_u32 m0, %[dw], " STR((ch - 5 * (ch / 5)) * 24576 + u * 3072) "\n\ts_mov_b32 %[t0], " STR(ch * 196608) "\n\t" LW3_##u
#define LA(ch) "s_add_u32 m0, %[da], " STR((ch & 3) * 8192) "\n\ts_mov_b32 %[t0], " STR(ch * 8192) "\n\t" \
  "buffer_load_dwordx4 %[va], %[rs], %[t0] offen sc1 lds\n\tbuffer_load_dwordx4 %[va], %[rs], %[t0] offen offset:1024 sc1 lds\n\t"
#define CKR(ch) "ds_read_b128 v[60:63], %[la] offset:" STR((ch & 3) * 8192) "\n\tds_read_b128 v[64:67], %[la] offset:" STR((ch & 3) * 8192 + 1024) "\n\t"
#define CKT "s_waitcnt lgkmcnt(0)\n\tv_max3_u32 v60, v60, v61, v62\n\tv_max3_u32 v64, v63, v64, v65\n\tv_max3_u32 v60, v60, v66, v67\n\tv_max_u32 v60, v60, v64\n\tv_cmp_eq_u32 vcc, -1, v60\n\t"
#define FIX(ch) "s_cbranch_vccz 2f\n\ts_mov_b32 %[t1], 0\n1:\n\ts_sleep 2\n\t" LA(ch) "s_waitcnt vmcnt(0)\n\t" CKR(ch) CKT \
  "s_cbranch_vccz 2f\n\ts_add_u32 %[t1], %[t1], 1\n\ts_cmp_lt_u32 %[t1], 0x40000\n\ts_cbranch_scc1 1b\n2:\n\t"
#define IVL(ch, VA, FETCH, VW) "s_waitcnt vmcnt(" STR(VA) ")\n\t" CKR(ch) FETCH CKT FIX(ch) "s_waitcnt vmcnt(" STR(VW) ")\n\t"

DI void loader_waves(Ctx& c) {
  const Params& p = c.p;
  const int l = c.l, lw = c.w - 4, b = c.b, t4 = lw * 64 + l, cs = c.cs, rt = c.rt;
  const unsigned lds0 = c.lds0, lin = l * 16;
  {
    {
      unsigned vo = lw < 2 ? lin : (l >> 3) * 3840 + (l & 7) * 16;
      unsigned so = lw < 2 ? c.xr0 * 1280 + lw * 5120 : ((c.wi & 7) * 80 + (lw - 2) * 40) * 3840 + (15 * (b & 1) + (c.wi >> 3)) * 128, st = lw < 2 ? 1024 : 8 * 3840;
      int isx = lw;
      asm volatile("" : "+s"(isx), "+v"(vo), "+s"(so), "+s"(st));
      __builtin_amdgcn_sched_barrier(0);
      STAMP(0);
      const char* src = (isx < 2 ? p.x : p.wqkv) + so;
#pragma unroll
      for (int i = 0; i < 5; ++i) glds(src + i * st, vo, lds0 + L1_XS + lw * 5120 + i * 1024);
      __builtin_amdgcn_sched_barrier(0);
    }
    unsigned va[4], vb[4], sa = 0, sb = 0, md = lds0 + 65536 + lw * 4096;
#pragma unroll
    for (int i = 0; i < 4; ++i) {
      va[i] = (32 * lw + 8 * i + (l >> 3)) * 1280 + (((l & 7) ^ ((4 * i + (l >> 4)) & 7)) << 4) - i * 1024;
      vb[i] = (16 * lw + 4 * i + (l >> 4)) * 3840 + (((l & 15) ^ (((l >> 4) << 2) | (i & 2))) << 4) - i * 1024;
    }
    const rsrc_t rx = __builtin_amdgcn_make_buffer_rsrc((void*)(p.x + c.panel * 163840), 0, 163840, 0x00020000);
    const rsrc_t rw = __builtin_amdgcn_make_buffer_rsrc((void*)(p.wqkv + c.ct * 256), 0, 640 * 3840 - c.ct * 256, 0x00020000);
    if (c.ct < 15) asm volatile("s_sleep " STR(SDELAY) "\n\t" P1_STAGE "s_waitcnt vmcnt(8)\n\ts_barrier\n\t" P1_STAGE : P1_STATE : P1_CONST : "memory", "scc");
    else { read_tag(c); __builtin_amdgcn_s_sleep(CPD); spare_poison(c); wait_bar<0>(); }
    read_tag(c);
    STAMP(2);
    {
      const char* pc; unsigned len;
      asm volatile("s_getpc_b64 %0\n.Lblk_pc:\n\ts_mov_b32 %1, .Lblk_end1-.Lblk_pc\n\ts_max_i32 %1, %1, .Lblk_end2-.Lblk_pc" : "=s"(pc), "=s"(len) :: "scc");
      const unsigned off = (((b >> 3) * 4 + lw) * 5 + umin(l, 4)) * 128;
      touch(pc, off < len ? off : 0u, lds0 + L_TOUCH);
    }
    if (c.ct < 15) {
      unsigned n = 0;
      asm volatile("s_waitcnt vmcnt(9)\n\ts_barrier\n\t" P1_STAGE
                   "buffer_store_dwordx4 %[ps], %[p0], %[rs], 0 offen sc1\n\tbuffer_store_dwordx4 %[ps], %[p0], %[rs], 0 offen offset:1024 sc1\n\t"
                   "s_waitcnt vmcnt(10)\n\ts_barrier\n\t" P1_STAGE "s_waitcnt vmcnt(10)\n"
                   "1:\n\ts_barrier\n\t" P1_STAGE
                   "s_add_u32 %[n], %[n], 1\n\ts_cmp_lt_u32 %[n], 6\n\ts_waitcnt vmcnt(8)\n\ts_cbranch_scc1 1b"
                   : P1_STATE, [n] "+&s"(n)
                   : P1_CONST, [p0] "v"(WS_QK + (640 + 8 * b + 2 * lw) * 1024 + lin), [rs] "s"(c.rs), [ps] "v"(u32x4{POISON, POISON, POISON, POISON}) : "memory", "scc");
      STAMP(5);
      glds_d(p.ws + WS_CS1 + (2 * lw * 1920 + c.ct * 128) * 8, lin, lds0 + L1_CG + lw * 2048);
      glds_d(p.ws + WS_CS1 + ((2 * lw + 1) * 1920 + c.ct * 128) * 8, lin, lds0 + L1_CG + lw * 2048 + 1024);
      glds_m(p.ws + WS_RS1 + (c.panel * 128 + 32 * lw) * 16, lin, lds0 + L1_RG + lw * 512, 0xFFFFFFFFull);
    }
    if (lw == 0 && l < 4) *(LAS unsigned*)(c.lds + L_CNT0 + l * 4) = 0u;
    wait_bar<11>();
    if (c.ct == 15) { publish<0>(c, L_CNT0 + 12, 8, WS_F0 + c.b * 8); __builtin_amdgcn_s_sleep(WOT); }
    {
      const unsigned pv = (l & 1) ? 0u : ONES2;
      if (l < 32) *(LAS u32x4*)(c.lds + VRING + (16 * lw + (l >> 1)) * 352 + 320 + (l & 1) * 16) = u32x4{pv, pv, pv, pv};
    }
    if (lw == 3) poll256(p.ws, WS_F0 + l * 8, c.tag);
    if (c.ct < 15) wait_bar<0>();
    STAMP(6);
    if (c.ct < 15) {
      LAS float* ra = (LAS float*)(c.lds + L1_FIN);
      const unsigned tag = (unsigned)c.tag;
      if (lw < 2) {
        u32x4 g = *(LAS u32x4*)(c.lds + L1_RG + t4 * 16);
#pragma unroll 1
        for (unsigned n = 0; (g[1] != tag || g[3] != tag) && n < SPIN_CAP; ++n) { FENCE(); g = ld128_d(c.rs, WS_RS1 + (c.panel * 128 + t4) * 16); }
        const float sc = c.ct < 5 ? QSCALE : 1.f;
        ra[t4] = __uint_as_float(g[0]) * sc; ra[128 + t4] = __uint_as_float(g[2]) * sc;
      } else {
        const int n0 = t4 - 128; float sum = 0.f; bool ok = true;
#pragma unroll
        for (int i = 0; i < 8; ++i) { const u32x2 g = *(LAS u32x2*)(c.lds + L1_CG + i * 1024 + n0 * 8); sum += __uint_as_float(g[0]); ok = ok && g[1] == tag; }
        if (__any(!ok)) {
          sum = 0.f;
#pragma unroll 1
          for (int i = 0; i < 8; ++i) {
            u32x2 g = {0u, ~tag};
#pragma unroll 1
            for (unsigned n = 0; g[1] != tag && n < SPIN_CAP; ++n) { FENCE(); g = ld64_d(c.rs, WS_CS1 + (i * 1920 + c.ct * 128 + n0) * 8); }
            sum += __uint_as_float(g[0]);
          }
        }
        ra[256 + n0] = sum;
      }
    }
    STAMP(7);
    if (c.ct < 15) LDS_BAR();
  }
  Park pk;
  const rsrc_t rw2 = __builtin_amdgcn_make_buffer_rsrc((void*)(p.w2 + cs * 160), 0, FF * D * 2 - cs * 160, 0x00020000);
  unsigned vo80[5];
  {
    const int h = c.h, e = h & 1, cb = 5 * (h >> 1) + 2 * e;
    unsigned vk[3], vv[3]; u64 mv[3], mt = 0;
#pragma unroll
    for (int i = 0; i < 3; ++i) {
      const int n = 64 * (3 * lw + i) + l, kg = n / 22, dd = n - 22 * kg;
      vk[i] = WS_QK + ((80 + cb + i) * 2048 + 64 + 64 * lw - 256) * 16 + lin - i * 1024;
      vv[i] = WS_V + (h * 256 + 4 - 32 + kg) * 320 + dd * 16 - i * 1024;
      mv[i] = __builtin_amdgcn_ballot_w64(dd < 20 && kg < 32);
      if (i == 0) mt = __builtin_amdgcn_ballot_w64(dd < 20 && kg < 28);
    }
    auto issue = [&](int s, bool withk, u64 m0, u64 m1, u64 m2) {
      unsigned t;
      if (withk) asm volatile("s_mov_b32 m0, %[d]\n\ts_lshl_b32 %[t], %[s], 12\n\t"
                              "buffer_load_dwordx4 %[v0], %[rs], %[t] offen sc1 lds\n\tbuffer_load_dwordx4 %[v1], %[rs], %[t] offen offset:1024 sc1 lds\n\tbuffer_load_dwordx4 %[v2], %[rs], %[t] offen offset:2048 sc1 lds"
                              : [t] "=&s"(t) : [d] "s"(lds0 + KRING + (s & 1) * 12288 + lw * 3072), [s] "s"(s + 1), [rs] "s"(c.rs), [v0] "v"(vk[0]), [v1] "v"(vk[1]), [v2] "v"(vk[2]) : "memory", "scc");
      asm volatile("s_mov_b32 m0, %[d]\n\ts_mul_i32 %[t], %[s], 10240\n\t"
                   "s_mov_b64 exec, %[m0]\n\tbuffer_load_dwordx4 %[v0], %[rs], %[t] offen sc1 lds\n\t"
                   "s_mov_b64 exec, %[m1]\n\ts_cbranch_execz 1f\n\tbuffer_load_dwordx4 %[v1], %[rs], %[t] offen offset:1024 sc1 lds\n\t"
                   "s_mov_b64 exec, %[m2]\n\ts_cbranch_execz 1f\n\tbuffer_load_dwordx4 %[v2], %[rs], %[t] offen offset:2048 sc1 lds\n1:\n\ts_mov_b64 exec, -1"
                   : [t] "=&s"(t) : [d] "s"(lds0 + VRING + (s & 1) * 11264 + lw * 3072), [s] "s"(s + 1), [rs] "s"(c.rs), [v0] "v"(vv[0]), [v1] "v"(vv[1]), [v2] "v"(vv[2]),
                     [m0] "s"(m0), [m1] "s"(m1), [m2] "s"(m2) : "memory");
    };
    auto bad = [&](int s, u64 m0, u64 m1, u64 m2) {
      u64 any;
      asm volatile("ds_read_b128 v[76:79], %[k]\n\tds_read_b128 v[80:83], %[k] offset:1024\n\tds_read_b128 v[84:87], %[k] offset:2048\n\t"
                   "ds_read_b128 v[88:91], %[v]\n\tds_read_b128 v[92:95], %[v] offset:1024\n\tds_read_b128 v[96:99], %[v] offset:2048\n\t"
                   WL(4) "v_max3_u32 v76, v76, v77, v78\n\tv_max3_u32 v80, v79, v80, v81\n\tv_max3_u32 v76, v76, v82, v83\n\t"
                   WL(3) "v_max3_u32 v80, v80, v84, v85\n\tv_max3_u32 v76, v76, v86, v87\n\t"
                   WL(0) "s_mov_b64 exec, %[m0]\n\tv_max3_u32 v80, v80, v88, v89\n\tv_max3_u32 v76, v76, v90, v91\n\t"
                   "s_mov_b64 exec, %[m1]\n\tv_max3_u32 v80, v80, v92, v93\n\tv_max3_u32 v76, v76, v94, v95\n\t"
                   "s_mov_b64 exec, %[m2]\n\tv_max3_u32 v80, v80, v96, v97\n\tv_max3_u32 v76, v76, v98, v99\n\t"
                   "s_mov_b64 exec, -1\n\tv_max_u32 v76, v76, v80\n\tv_cmp_eq_u32 %[any], -1, v76\n\ts_nop 0"
                   : [any] "=s"(any) : [k] "v"(lds0 + KRING + (s & 1) * 12288 + lw * 3072 + lin), [v] "v"(lds0 + VRING + (s & 1) * 11264 + lw * 3072 + lin), [m0] "s"(m0), [m1] "s"(m1), [m2] "s"(m2)
                   : "memory", CL8(76, 77, 78, 79, 80, 81, 82, 83), CL8(84, 85, 86, 87, 88, 89, 90, 91), CL8(92, 93, 94, 95, 96, 97, 98, 99));
      return any != 0;
    };
#pragma unroll
    for (int i = 0; i < 5; ++i) { const int n = 64 * i + l, rr = n / 10; vo80[i] = swap23(32 * lw + rr) * 1280 + (n - 10 * rr) * 16 - (i & 3) * 1024; }
    const rsrc_t rwo = __builtin_amdgcn_make_buffer_rsrc((void*)(p.wo + cs * 160), 0, D * D * 2 - cs * 160, 0x00020000);
    const unsigned xw = (b >> 3) * 4 + lw, vt60 = (l < 60 ? l : 0) * 256;
    u32x4 psn = {POISON, POISON, POISON, POISON};
    asm volatile("" : "+v"(psn));
    const int b1 = b < 192 ? b : 0, rg1 = b1 / 24, cg1 = b1 - 24 * rg1;
    unsigned w1o = (t4 >> 3) * 3072 + (l & 7) * 16, cs2o = WS_CS2 + (rg1 * 1536 + cg1 * 64 + 16 * lw + l) * 4;
    const char* bw = (l & 32 ? p.w2 : p.w1) + ((b * 4 + lw) * 15 + umin(l & 31, 14)) * 128;
    asm volatile("" : "+v"(w1o), "+v"(cs2o), "+v"(bw), "+v"(vo80[0]), "+v"(vo80[1]), "+v"(vo80[2]), "+v"(vo80[3]), "+v"(vo80[4]));
    LDS_BAR();
    __builtin_amdgcn_s_sleep(KDELAY);
#pragma unroll 1
    for (unsigned n = 0; n < SPIN_CAP; ++n) {
      issue(0, true, mv[0], mv[1], mv[2]);
      DRAIN();
      if (!bad(0, mv[0], mv[1], mv[2])) break;
      __builtin_amdgcn_s_sleep(RDELAY);
    }
    STAMP(9);
    LDS_BAR();
    __builtin_amdgcn_sched_barrier(0);
    __builtin_amdgcn_s_setprio(3);
#pragma unroll 1
    for (int s = 1; s < 5; ++s) {
      if (s > 1) LDS_BAR();
      STAMPV(20 + s);
      issue(s, true, mv[0], mv[1], mv[2]);
#pragma unroll
      for (int k = 0; k < 3; ++k) {
        const int q = 12 * lw + 3 * (s - 1) + k;
        asm volatile("buffer_store_dwordx4 %0, %1, %2, %3 offen sc1" :: "v"(psn), "v"(lin), "s"(c.rs),
                     "s"(q < 10 ? WS_O + rt * 81920 + cs * 10240 + q * 1024 : q < 20 ? WS_H + rt * 81920 + cs * 10240 + (q - 10) * 1024 : q < 44 ? WS_F + rt * 196608 + cs * 24576 + (q - 20) * 1024 : WS_RS2 + (rt * 8 + cs) * 512) : "memory");
      }
      if (s == 1) {
        wait_vm<3>();
#pragma unroll 1
        for (unsigned n = 0; bad(1, mv[0], mv[1], mv[2]) && n < SPIN_CAP; ++n) { issue(1, true, mv[0], mv[1], mv[2]); DRAIN(); }
        if (lw == 0) {
          poll256(p.ws, WS_F1 + l * 8, c.tag);
          if (b == 0 && l == 0) stflag(p.ws, WS_EPOCH, c.tag);
        }
      } else {
#pragma unroll 1
        for (int k = 2 * s - 4; k < (s < 4 ? 2 * s - 2 : 5); ++k)
        { const int kr = k + (rt & 3) < 5 ? k + (rt & 3) : k + (rt & 3) - 5; W80_DMA(rwo, lds0 + L_WO + kr * 20480 + lw * 5120, kr * 163840); }
        if (s == 4) { touch64(bw, lds0 + L_TOUCH); wait_vm<9>(); }
        wait_vm<13>();
        if (s == 3) {
          const char* src = p.w1 + (rg1 * 80 * 3072 + cg1 * 128);
          glds(src, w1o, lds0 + L_W1B + lw * 1024); glds(src + 32 * 3072, w1o, lds0 + L_W1B + 4096 + lw * 1024);
          if (lw < 2) glds(src + 64 * 3072, w1o, lds0 + L_W1B + 8192 + lw * 1024);
        }
      }
      STAMPV(9 + s);
    }
    unsigned so;
#define PARK2(m, k0, c0, k1, c1) asm volatile(PKL(k0, c0) PKL(k1, c1) : PARK_REGS(m, pk), [so] "=&s"(so) : W80_OPS(rw2) : "memory")
    LDS_BAR(); STAMP(25);
    issue(5, true, mv[0], mv[1], mv[2]);
    {
      const float cs2 = colsum(c.lds + L_W1B, l, lw);
      if (l < 16 && b < 192) st32_d(c.rs, cs2o, __float_as_uint(cs2));
    }
    PARK2("=&", 0, 0, 1, 1);
    wait_bar<10>(); STAMP(26);
    publish<10>(c, L_CNTP, 4, WS_FP + b * 8);
    issue(6, true, mv[0], mv[1], mv[2]);
    PARK2("+", 2, 2, 3, 3);
    wait_bar<10>(); STAMP(27);
    issue(7, lw != 3, lw == 3 ? mt : mv[0], lw == 3 ? 0 : mv[1], mv[2]);
    PARK2("+", 4, 4, 5, 8);
    wait_bar<10>(); STAMP(28);
    if (lw == 0) poll256(p.ws, WS_FP + l * 8, c.tag);
    PARK2("+", 6, 9, 7, 10);
    if (lw == 1) {
      const int i = l & 7, k = l >> 3;
      touch64(k == 0 ? p.ws + (WS_O + c.qb * 327680 + i * 40960) : k == 1 ? p.ws + (WS_O + rt * 81920 + i * 10240) : k == 2 ? p.ws + (WS_H + rt * 81920 + i * 10240) : k == 3 ? p.ws + (WS_F + rt * 196608 + i * 24576)
              : k == 4 ? p.out + (rt * 81920 + i * 10240) : k == 5 ? p.x + (rt * 81920 + i * 10240) : p.ws + (WS_RS2 + rt * 4096 + i * 512), lds0 + L_TOUCH);
    }
    touch(p.w1 + xw * 15360, vt60, lds0 + L_TOUCH);
    touch(p.w1 + (xw * 15360 + 128), vt60, lds0 + L_TOUCH);
  }
  {
    unsigned vo[6];
#pragma unroll
    for (int i = 0; i < 3; ++i) {
      const int n = 64 * i + l, rr = n / 24, pos = n - 24 * rr;
      vo[i] = ((rr & 3) + 8 * (rr >> 2)) * 3072 + ((((pos >> 1) ^ ((rr >> 1) & 3)) << 1) | (pos & 1)) * 16 - i * 1024;
      vo[3 + i] = vo[i] + 4 * 3072;
    }
    const rsrc_t rw1 = __builtin_amdgcn_make_buffer_rsrc((void*)(p.w1 + cs * 384 + lw * 16 * 3072), 0, D * FF * 2 - cs * 384 - lw * 16 * 3072, 0x00020000);
    const unsigned dw = lds0 + lw * 6144, da = lds0 + L4_A + lw * 2048, va = WS_H + rt * 81920 + lw * 2048 + lin;
    const unsigned so = WS_RS2 + (rt * 8 + (l >> 4)) * 512 + (16 * lw + (l & 15)) * 8;
    unsigned t0, t1r; u32x2 s0, s1;
    LDS_BAR();
    STAMP(17);
    LDS_BAR();
    asm volatile(LW3(0, 0) LW3(0, 1) LW3(1, 0) LW3(1, 1) : [t0] "=&s"(t0) : W1_OPS : "memory");
    if (lw < 3) {
      float t = 0.f;
#pragma unroll
      for (int i = 0; i < 8; ++i) t += __uint_as_float(ld32_d(c.rs, WS_CS2 + (i * 1536 + cs * 192 + t4) * 4));
      *(LAS float*)(c.lds + L_CSUM + t4 * 4) = t;
    }
    asm volatile(PKL(8, 11) : PARK_REGS("+", pk), [so] "=&s"(t0) : W80_OPS(rw2) : "memory");
    LDS_BAR(); LDS_BAR(); LDS_BAR();
    STAMP(18);
    LDS_BAR();
    asm volatile(
        LW3(2, 0) LW3(2, 1) LW3(3, 0) LW3(3, 1)
        "s_mov_b32 %[t1], 0\n"
        "1:\n\ts_sleep " STR(ADELAY) "\n\t" LA(0) LA(1) LA(2) LA(3)
        "s_waitcnt vmcnt(6)\n\t" CKR(0) CKT
        "s_cbranch_vccz 2f\n\ts_waitcnt vmcnt(0)\n\ts_add_u32 %[t1], %[t1], 1\n\ts_cmp_lt_u32 %[t1], 0x40000\n\ts_cbranch_scc1 1b\n"
        "2:\n\ts_barrier\n\t"
        IVL(1, 4, LW3(4, 0) LW3(4, 1), 63) "s_barrier\n\t"
        IVL(2, 8, LA(4) LW3(5, 0), 63) "s_barrier\n\t"
        IVL(3, 11, LA(5) LW3(5, 1), 63) "s_barrier\n\t"
        IVL(4, 8, LA(6) LW3(6, 0) LW3(6, 1), 63) "s_barrier\n\t"
        IVL(5, 11, LA(7) LW3(7, 0), 13) "s_barrier\n\t"
        IVL(6, 11, LA(8) LW3(7, 1) LW3(8, 0), 13) "s_barrier\n\t"
        IVL(7, 11, LA(9) LW3(8, 1), 8) "s_barrier\n\t"
        IVL(8, 11, "buffer_load_dwordx2 %[s0], %[so], %[rs], 0 offen sc1\n\tbuffer_load_dwordx2 %[s1], %[so], %[rs], 0 offen offset:2048 sc1\n\t" LW3(9, 0) LW3(9, 1), 8) "s_barrier\n\t"
        IVL(9, 11, "", 6)
        : [t0] "=&s"(t0), [t1] "=&s"(t1r), [s0] "=&v"(s0), [s1] "=&v"(s1)
        : W1_OPS, [rs] "s"(c.rs), [da] "s"(da), [va] "v"(va), [la] "v"(da + lin), [so] "v"(so) : "memory", "scc", "vcc", "v60", "v61", "v62", "v63", "v64", "v65", "v66", "v67");
    STAMP(29);
#pragma unroll 1
    for (unsigned n = 0; __any(umax(umax(s0[0], s0[1]), umax(s1[0], s1[1])) == POISON) && n < SPIN_CAP; ++n) { FENCE(); s0 = ld64_d(c.rs, so); s1 = ld64_d(c.rs, so + 2048); }
    float t1 = __uint_as_float(s0[0]) + __uint_as_float(s1[0]), t2 = __uint_as_float(s0[1]) + __uint_as_float(s1[1]);
    t1 = swap16_add(t1, t1); t2 = swap16_add(t2, t2); t1 = swap32_add(t1, t1); t2 = swap32_add(t2, t2);
    const float mean = t1 * (1.f / D), var = fmaxf(t2 * (1.f / D) - mean * mean, 0.f), a = __builtin_amdgcn_rsqf(var + LN_EPS);
    if (l < 32) *(LAS float*)(c.lds + L_RA + (l >> 4) * 256 + (16 * lw + (l & 15)) * 4) = l < 16 ? a : -mean * a;
    wait_bar<0>();
  }
  {
    const unsigned wa = lds0 + lw * 5120 + lin;
    {
      touch(p.w2 + cs * 160, (640 + ((b >> 6) * 4 + lw) * 24 + umin(l >> 1, 23)) * 1280 + (l & 1) * 156, lds0 + L_TOUCH);
    }
    LDS_BAR();
    __builtin_amdgcn_s_setprio(0);
    STAMP(30);
    PUT(0, 0); PUT(1, 1); PUT(2, 2);
    LDS_BAR();
#pragma unroll 1
    for (int k = 5; k < 8; ++k) W80_DMA(rw2, lds0 + k * 20480 + lw * 5120, k * 163840);
    PUT(3, 3); PUT(4, 4);
    STAMP(31);
    LDS_BAR();
    wait_bar<0>(); PUT(5, 0); PUT(6, 1); PUT(7, 2); PUT(8, 3);
    LDS_BAR(); LDS_BAR();
  }
}

__global__ void __launch_bounds__(512, 2) block_kernel(Params p) {
  extern __shared__ __attribute__((aligned(16))) char smem[];
  Ctx c;
  c.st_lo = c.st_hi = 0;
  STAMP(3 + 20);
  c.p = p; c.lds = (lptr)smem; c.lds0 = (unsigned)(size_t)c.lds; c.l = threadIdx.x & 63; c.w = rfl(threadIdx.x >> 6); c.b = blockIdx.x;
  const int xcd = c.b & 7, j = c.b >> 3;
  const int t1 = 30 * (xcd & 1) + j;
  c.panel = 4 * (xcd >> 1) + (t1 & 3); c.ct = j < 30 ? t1 >> 2 : 15;
  c.h = c.rt = 4 * xcd + (j >> 3); c.qb = c.cs = j & 7;
  c.xr0 = 512 * (xcd >> 1) + 8 * (32 * (xcd & 1) + j); c.wi = j < 30 ? (xcd >> 1) * 30 + j : 0;
  if (__builtin_expect(c.w >= 4, 1)) loader_waves(c);
  else { STAMP(0); compute_waves(c); }
}

extern "C" void kernel_launch(void* const* d_in, const int* in_sizes, int n_in, void* d_out, int out_size, void* d_ws, size_t ws_size, hipStream_t stream) {
  Params p;
  p.x = (const char*)d_in[0]; p.wqkv = (const char*)d_in[1]; p.wo = (const char*)d_in[2]; p.w1 = (const char*)d_in[4]; p.w2 = (const char*)d_in[5];
  p.out = (char*)d_out; p.ws = (char*)d_ws;
  (void)hipFuncSetAttribute((const void*)block_kernel, hipFuncAttributeMaxDynamicSharedMemorySize, LDS_BYTES);
  block_kernel<<<256, 512, LDS_BYTES, stream>>>(p);
}
```
